```python
import torch

def _softmax(x, axis=-1):
    return torch.softmax(x, dim=axis)
def _gelu(x):
    return torch.nn.functional.gelu(x, approximate="tanh")
def _silu(x):
    return torch.nn.functional.silu(x)
def _sigmoid(x):
    return torch.sigmoid(x)
def _layernorm(x, axis=-1, eps=1e-5):
    m = x.mean(dim=axis, keepdim=True)
    v = x.var(dim=axis, keepdim=True, unbiased=False)
    return (x - m) / torch.sqrt(v + eps)
def _scatter_add(tgt, idx, src, axis=0):
    return tgt.clone().index_add_(axis, idx, src)
def _segment_sum(src, seg, n):
    out = torch.zeros((n,) + src.shape[1:], dtype=src.dtype)
    return out.index_add_(0, seg, src)
def _pad(x, pw):
    flat = tuple(v for pair in reversed(pw) for v in pair)
    return torch.nn.functional.pad(x, flat)

kernel_name = 'concat_heads__gqa_block_cached__mha_block_cached__near_ridge'

def setup_inputs():

    _w = (lambda a: (lambda t: t.to(torch.bfloat16) if t.is_floating_point() else t)(torch.as_tensor(a)))
    def _A_setup():
        B, Sq, D = 8, 112, 640
        Hq, Hkv, Sk, Dh = 32, 32, 512, 192
        assert Hq % Hkv == 0, (Hq, Hkv)
        return {
            "x": _w((lambda *s: torch.randn(*s, dtype=torch.bfloat16) * 0.8)(B, Sq, D)),
            "k_cache": _w((lambda *s: torch.randn(*s, dtype=torch.bfloat16) * 0.8)(B, Hkv, Sk, Dh)),
            "v_cache": _w((lambda *s: torch.randn(*s, dtype=torch.bfloat16) * 0.05)(B, Hkv, Sk, Dh)),
            "wq": _w((lambda *s: torch.randn(*s, dtype=torch.bfloat16) * 0.8)(D, Hq * Dh) * (D ** -0.5)),
            "wo": _w((lambda *s: torch.randn(*s, dtype=torch.bfloat16) * 0.05)(Hq * Dh, D) * ((Hq * Dh) ** -0.5)),
        }
    _A_in = _A_setup()

    def _B_setup():
        B, Sq, D = 8, 112, 640
        H, Sk, Dh = 32, 512, 192
        return {
            "x": _w((lambda *s: torch.randn(*s, dtype=torch.bfloat16) * 0.8)(B, Sq, D)),
            "k_cache": _w((lambda *s: torch.randn(*s, dtype=torch.bfloat16) * 0.8)(B, H, Sk, Dh)),
            "v_cache": _w((lambda *s: torch.randn(*s, dtype=torch.bfloat16) * 0.05)(B, H, Sk, Dh)),
            "wq": _w((lambda *s: torch.randn(*s, dtype=torch.bfloat16) * 0.8)(D, H * Dh) * (D ** -0.5)),
            "wo": _w((lambda *s: torch.randn(*s, dtype=torch.bfloat16) * 0.05)(H * Dh, D) * ((H * Dh) ** -0.5)),
        }
    _B_in = _B_setup()

    return {
        "A_x": _A_in["x"],
        "A_k_cache": _A_in["k_cache"],
        "A_v_cache": _A_in["v_cache"],
        "A_wq": _A_in["wq"],
        "A_wo": _A_in["wo"],
        "B_x": _B_in["x"],
        "B_k_cache": _B_in["k_cache"],
        "B_v_cache": _B_in["v_cache"],
        "B_wq": _B_in["wq"],
        "B_wo": _B_in["wo"],
    }

def reference(A_x, A_k_cache, A_v_cache, A_wq, A_wo, B_x, B_k_cache, B_v_cache, B_wq, B_wo):
    def _A_ref(x, k_cache, v_cache, wq, wo):
        B, Sq, D = x.shape
        Hq, Dh = 32, 192
        Hkv = k_cache.shape[1]
        rep = Hq // Hkv
        q = (lambda x, *d: x.permute(*d))((x @ wq).reshape(B, Sq, Hq, Dh), 0, 2, 1, 3)
        k = (lambda x, n, axis: torch.repeat_interleave(x, n, dim=axis))(k_cache, rep, axis=1)
        v = (lambda x, n, axis: torch.repeat_interleave(x, n, dim=axis))(v_cache, rep, axis=1)
        scores = torch.einsum("bhqd,bhkd->bhqk", q, k) * (Dh ** -0.5)
        p = _softmax(scores, axis=-1)
        o = torch.einsum("bhqk,bhkd->bhqd", p, v)
        return (lambda x, *d: x.permute(*d))(o, 0, 2, 1, 3).reshape(B, Sq, Hq * Dh) @ wo

    def _B_ref(x, k_cache, v_cache, wq, wo):
        B, Sq, D = x.shape
        H, Dh = 32, 192
        q = (lambda x, *d: x.permute(*d))((x @ wq).reshape(B, Sq, H, Dh), 0, 2, 1, 3)
        scores = torch.einsum("bhqd,bhkd->bhqk", q, k_cache) * (Dh ** -0.5)
        p = _softmax(scores, axis=-1)
        o = torch.einsum("bhqk,bhkd->bhqd", p, v_cache)
        return (lambda x, *d: x.permute(*d))(o, 0, 2, 1, 3).reshape(B, Sq, H * Dh) @ wo

    a = _A_ref(A_x, A_k_cache, A_v_cache, A_wq, A_wo)
    b = _B_ref(B_x, B_k_cache, B_v_cache, B_wq, B_wo)
    return torch.concatenate([a, b], axis=-1)
```

```cpp
#include <hip/hip_runtime.h>
#include <hip/hip_bf16.h>
#include <stdint.h>

typedef unsigned short u16;
__device__ __forceinline__ float bf2f(u16 b) { return __uint_as_float(((unsigned)b) << 16); }
__device__ __forceinline__ u16 f2bf(float f) { unsigned u = __float_as_uint(f); u += 0x7FFFu + ((u >> 16) & 1u); return (u16)(u >> 16); }

constexpr int NB = 8, SQ = 112, DM = 640, NH = 32, SK = 512, DH = 192, HD = NH * DH;

__global__ void naive_gemm(const u16* A, const u16* B, u16* C, int M, int N, int K, int ldc, int coff) {
  int n = blockIdx.x * 64 + (threadIdx.x & 63);
  int m = blockIdx.y * 4 + (threadIdx.x >> 6);
  if (m >= M || n >= N) return;
  float acc = 0.f;
  for (int k = 0; k < K; ++k) acc += bf2f(A[(size_t)m * K + k]) * bf2f(B[(size_t)k * N + n]);
  C[(size_t)m * ldc + coff + n] = f2bf(acc);
}

__global__ void naive_attn(const u16* Q, const u16* K, const u16* V, u16* O) {
  int q = blockIdx.x % SQ, h = (blockIdx.x / SQ) % NH, b = blockIdx.x / (SQ * NH);
  __shared__ float s[SK];
  __shared__ float qs[DH];
  __shared__ float red[256];
  int t = threadIdx.x;
  if (t < DH) qs[t] = bf2f(Q[((size_t)(b * SQ + q)) * HD + h * DH + t]);
  __syncthreads();
  const u16* Kp = K + ((size_t)(b * NH + h)) * SK * DH;
  const u16* Vp = V + ((size_t)(b * NH + h)) * SK * DH;
  float scale = bf2f(f2bf(0.07216878364870322f));
  scale = 0.07216878364870322f;
  float mx = -1e30f;
  for (int k = t; k < SK; k += 256) {
    float acc = 0.f;
    for (int d = 0; d < DH; ++d) acc += qs[d] * bf2f(Kp[(size_t)k * DH + d]);
    acc = bf2f(f2bf(acc));
    acc = bf2f(f2bf(acc * scale));
    s[k] = acc; mx = fmaxf(mx, acc);
  }
  red[t] = mx; __syncthreads();
  for (int o = 128; o > 0; o >>= 1) { if (t < o) red[t] = fmaxf(red[t], red[t + o]); __syncthreads(); }
  mx = red[0]; __syncthreads();
  float sum = 0.f;
  for (int k = t; k < SK; k += 256) { float e = __expf(s[k] - mx); s[k] = e; sum += e; }
  red[t] = sum; __syncthreads();
  for (int o = 128; o > 0; o >>= 1) { if (t < o) red[t] += red[t + o]; __syncthreads(); }
  sum = red[0]; __syncthreads();
  for (int k = t; k < SK; k += 256) s[k] = bf2f(f2bf(s[k] / sum));
  __syncthreads();
  if (t < DH) {
    float acc = 0.f;
    for (int k = 0; k < SK; ++k) acc += s[k] * bf2f(Vp[(size_t)k * DH + t]);
    O[((size_t)(b * SQ + q)) * HD + h * DH + t] = f2bf(acc);
  }
}

extern "C" void kernel_launch(void* const* d_in, const int* in_sizes, int n_in,
                              void* d_out, int out_size, void* d_ws, size_t ws_size,
                              hipStream_t stream) {
  u16* out = (u16*)d_out;
  u16* qb = (u16*)d_ws;
  u16* ob = qb + (size_t)NB * SQ * HD;
  for (int br = 0; br < 2; ++br) {
    const u16* x = (const u16*)d_in[br * 5 + 0];
    const u16* k = (const u16*)d_in[br * 5 + 1];
    const u16* v = (const u16*)d_in[br * 5 + 2];
    const u16* wq = (const u16*)d_in[br * 5 + 3];
    const u16* wo = (const u16*)d_in[br * 5 + 4];
    naive_gemm<<<dim3(HD / 64, NB * SQ / 4), 256, 0, stream>>>(x, wq, qb, NB * SQ, HD, DM, HD, 0);
    naive_attn<<<NB * NH * SQ, 256, 0, stream>>>(qb, k, v, ob);
    naive_gemm<<<dim3(DM / 64, NB * SQ / 4), 256, 0, stream>>>(ob, wo, out, NB * SQ, DM, HD, 2 * DM, br * DM);
  }
}
```

```cpp
#include <hip/hip_runtime.h>
#include <hip/hip_bf16.h>
#include <stdint.h>

typedef unsigned short u16;
using bf16x8 = __attribute__((ext_vector_type(8))) short;
using s16x4 = __attribute__((ext_vector_type(4))) short;
using f32x4 = __attribute__((ext_vector_type(4))) float;
using u32x4 = __attribute__((ext_vector_type(4))) unsigned;
using u32x2 = __attribute__((ext_vector_type(2))) unsigned;
typedef __attribute__((address_space(3))) const char* lds_cptr;
typedef __amdgpu_buffer_rsrc_t rsrc_t;

constexpr int NB = 8, SQ = 112, DM = 640, NH = 32, SK = 512, DH = 192, HD = NH * DH;
constexpr int SLOT = 12288;
constexpr float SCL2 = 0.07216878364870322f * 1.4426950408889634f;

#define MFMA16(a, b, c) __builtin_amdgcn_mfma_f32_16x16x32_bf16(a, b, c, 0, 0, 0)
#define SBAR() __builtin_amdgcn_sched_barrier(0)
#define RFL(x) __builtin_amdgcn_readfirstlane(x)

typedef __bf16 bf16x2_t __attribute__((ext_vector_type(2)));
typedef float f32x2 __attribute__((ext_vector_type(2)));
__device__ __forceinline__ unsigned cvtpk(float lo, float hi) {
  const f32x2 v = {lo, hi};
  return __builtin_bit_cast(unsigned, __builtin_convertvector(v, bf16x2_t));
}
__device__ __forceinline__ bf16x8 lds_rd128(lds_cptr p) { return *(const __attribute__((address_space(3))) bf16x8*)p; }
typedef short v4i16_t __attribute__((ext_vector_type(4)));
__device__ __forceinline__ s16x4 lds_tr(lds_cptr p) {
  return __builtin_bit_cast(s16x4, __builtin_amdgcn_ds_read_tr16_b64_v4i16((__attribute__((address_space(3))) v4i16_t*)p));
}
__device__ __forceinline__ bf16x8 cat8(s16x4 a, s16x4 b) { return (bf16x8){a[0], a[1], a[2], a[3], b[0], b[1], b[2], b[3]}; }
template <int N> __device__ __forceinline__ void wait_bar() {
  asm volatile("s_waitcnt vmcnt(%0) lgkmcnt(0)\n\ts_barrier" ::"i"(N) : "memory");
}
__device__ __forceinline__ void dma_tile3(unsigned m0v, unsigned v0, unsigned v1, unsigned v2, rsrc_t srd, unsigned soff) {
  asm volatile(
      "s_mov_b32 m0, %0\n\ts_nop 0\n\t"
      "buffer_load_dwordx4 %1, %4, %5 offen nt lds\n\t"
      "buffer_load_dwordx4 %2, %4, %5 offen offset:1024 nt lds\n\t"
      "buffer_load_dwordx4 %3, %4, %5 offen offset:2048 nt lds"
      ::"s"(m0v), "v"(v0), "v"(v1), "v"(v2), "s"(srd), "s"(soff) : "memory");
}
template <int IMM> __device__ __forceinline__ void dma_piece_nt(unsigned m0v, unsigned v0, rsrc_t srd, unsigned soff) {
  asm volatile("s_mov_b32 m0, %0\n\ts_nop 0\n\tbuffer_load_dwordx4 %1, %2, %3 offen offset:%4 nt lds"
               ::"s"(m0v), "v"(v0), "s"(srd), "s"(soff), "i"(IMM) : "memory");
}
__device__ __forceinline__ void dma_w(unsigned m0v, unsigned v0, unsigned v1, rsrc_t srd, unsigned soff) {
  asm volatile(
      "s_mov_b32 m0, %0\n\ts_nop 0\n\t"
      "buffer_load_dwordx4 %1, %3, %4 offen lds\n\t"
      "s_mov_b32 exec_hi, 0\n\t"
      "buffer_load_dwordx4 %2, %3, %4 offen offset:1024 lds\n\t"
      "s_mov_b32 exec_hi, -1"
      ::"s"(m0v), "v"(v0), "v"(v1), "s"(srd), "s"(soff) : "memory");
}
__device__ __forceinline__ void ld_x2(bf16x8& a, bf16x8& b, unsigned v0, unsigned v1, rsrc_t srd, unsigned soff) {
  asm volatile("buffer_load_dwordx4 %0, %2, %4, %5 offen\n\tbuffer_load_dwordx4 %1, %3, %4, %5 offen"
               : "=&v"(a), "=&v"(b) : "v"(v0), "v"(v1), "s"(srd), "s"(soff) : "memory");
}
__device__ __forceinline__ float xmax16_32(float v) {
  auto a = __builtin_amdgcn_permlane32_swap(__float_as_uint(v), __float_as_uint(v), false, false);
  v = fmaxf(__uint_as_float(a[0]), __uint_as_float(a[1]));
  auto b = __builtin_amdgcn_permlane16_swap(__float_as_uint(v), __float_as_uint(v), false, false);
  return fmaxf(__uint_as_float(b[0]), __uint_as_float(b[1]));
}
__device__ __forceinline__ float xsum16_32(float v) {
  auto a = __builtin_amdgcn_permlane32_swap(__float_as_uint(v), __float_as_uint(v), false, false);
  v = __uint_as_float(a[0]) + __uint_as_float(a[1]);
  auto b = __builtin_amdgcn_permlane16_swap(__float_as_uint(v), __float_as_uint(v), false, false);
  return __uint_as_float(b[0]) + __uint_as_float(b[1]);
}

constexpr int WD = 4;
constexpr int kv_piece(int t) { return t < 12 ? t : (t >= 17 ? 12 + (t - 17) : -1); }
constexpr int nops(int u) { return (u + WD < 20 ? 4 : 0) + (kv_piece(u) >= 0 ? 1 : 0); }
constexpr int qwait(int t) {
  int n = 0;
  if (t < WD) { n = 4 * (WD - 1 - t); for (int u = 0; u < t; ++u) n += nops(u); }
  else { n = kv_piece(t - WD) >= 0 ? 1 : 0; for (int u = t - WD + 1; u < t; ++u) n += nops(u); }
  return n;
}
constexpr int wslot(int t) { return t % 5 == 0 ? 4 : t % 5 == 1 ? 10 : t % 5 == 2 ? 5 : t % 5 == 3 ? 11 : 12; }

struct QP {
  f32x4 acc[12][2];
  bf16x8 xa[5], xb[5];
};

template <int T> __device__ __forceinline__ void qproj_step(
    f32x4 (&acc)[12][2], bf16x8 (&xa)[5], bf16x8 (&xb)[5], lds_cptr lds, unsigned ldsb, const unsigned (&wb)[4], int w, int item, int rot,
    unsigned vw0, unsigned vw1, unsigned vx0, unsigned vx1, const unsigned (&vk)[3], const unsigned (&vv)[3],
    rsrc_t srdW, rsrc_t srdX, rsrc_t srdK, rsrc_t srdV) {
  asm volatile("s_waitcnt vmcnt(%2) lgkmcnt(0)\n\ts_barrier" : "+v"(xa[T % 5]), "+v"(xb[T % 5]) : "i"(qwait(T)) : "memory");
  if constexpr (T + WD < 20) {
    int kt = T + WD + rot; kt = kt >= 20 ? kt - 20 : kt;
    dma_w(ldsb + wslot(T + WD) * SLOT + w * 1536, vw0, vw1, srdW, (unsigned)kt * (32u * 12288u));
    ld_x2(xa[(T + WD) % 5], xb[(T + WD) % 5], vx0, vx1, srdX, (unsigned)kt * 64u);
  }
  if constexpr (kv_piece(T) >= 0) {
    constexpr int j = kv_piece(T) / 3, pc = kv_piece(T) % 3;
    constexpr bool isK = (j == 0 || j == 1 || j == 4);
    constexpr int tl = j == 0 ? 0 : j == 1 ? 1 : j == 2 ? 0 : j == 3 ? 1 : 2;
    const unsigned m0v = ldsb + (unsigned)(item * 6 + j) * SLOT + (w & 3) * 3072;
    if constexpr (isK) dma_piece_nt<pc * 1024>(m0v, vk[pc], srdK, tl * SLOT);
    else dma_piece_nt<pc * 1024>(m0v, vv[pc], srdV, tl * SLOT);
  }
  const lds_cptr base = lds + wslot(T) * SLOT;
#pragma unroll
  for (int s = 0; s < 6; ++s) {
#pragma unroll
    for (int u = 0; u < 2; ++u) {
      const lds_cptr p = base + wb[(s & 1) * 2 + u] + 128 * (s >> 1);
      const bf16x8 a = cat8(lds_tr(p), lds_tr(p + 1536));
      acc[2 * s + u][0] = MFMA16(a, xa[T % 5], acc[2 * s + u][0]);
      acc[2 * s + u][1] = MFMA16(a, xb[T % 5], acc[2 * s + u][1]);
    }
  }
}
template <int T> __device__ __forceinline__ void qproj_all(
    f32x4 (&acc)[12][2], bf16x8 (&xa)[5], bf16x8 (&xb)[5], lds_cptr lds, unsigned ldsb, const unsigned (&wb)[4], int w, int item, int rot,
    unsigned vw0, unsigned vw1, unsigned vx0, unsigned vx1, const unsigned (&vk)[3], const unsigned (&vv)[3],
    rsrc_t srdW, rsrc_t srdX, rsrc_t srdK, rsrc_t srdV) {
  if constexpr (T < 20) {
    qproj_step<T>(acc, xa, xb, lds, ldsb, wb, w, item, rot, vw0, vw1, vx0, vx1, vk, vv, srdW, srdX, srdK, srdV);
    qproj_all<T + 1>(acc, xa, xb, lds, ldsb, wb, w, item, rot, vw0, vw1, vx0, vx1, vk, vv, srdW, srdX, srdK, srdV);
  }
}

__global__ __launch_bounds__(512, 2) void attn_kernel(
    const u16* __restrict__ xA, const u16* __restrict__ kA, const u16* __restrict__ vA, const u16* __restrict__ wqA,
    const u16* __restrict__ xB, const u16* __restrict__ kB, const u16* __restrict__ vB, const u16* __restrict__ wqB,
    u16* __restrict__ ows) {
  __shared__ __attribute__((aligned(16))) char smem[13 * SLOT];
  const lds_cptr lds = (lds_cptr)smem;
  const unsigned ldsb = (unsigned)(uintptr_t)smem;
  const int tid = threadIdx.x, lane = tid & 63;
  const int w = RFL(tid >> 6);
  const int bid = blockIdx.x, xcd = bid & 7, idx = bid >> 3;
  const int br = xcd >> 2, hh = idx >> 2, h = (xcd & 3) * 8 + hh, pr = idx & 3;
  const int item = w >> 2, qg = w & 3, b = 2 * pr + item;
  const int rot = 5 * ((pr + hh) & 3);
  const int g = lane >> 4, r16 = lane & 15, q4 = (lane >> 2) & 3, p4 = lane & 3;

  const u16* x = br ? xB : xA; const u16* kc = br ? kB : kA; const u16* vc = br ? vB : vA; const u16* wq = br ? wqB : wqA;
  const rsrc_t srdW = __builtin_amdgcn_make_buffer_rsrc((void*)(wq + h * DH), (short)0, DM * HD * 2 - h * DH * 2, 0x00020000);
  const rsrc_t srdX = __builtin_amdgcn_make_buffer_rsrc((void*)(x + (size_t)b * SQ * DM), (short)0, SQ * DM * 2, 0x00020000);
  const rsrc_t srdK = __builtin_amdgcn_make_buffer_rsrc((void*)(kc + (size_t)(b * NH + h) * SK * DH), (short)0, SK * DH * 2, 0x00020000);
  const rsrc_t srdV = __builtin_amdgcn_make_buffer_rsrc((void*)(vc + (size_t)(b * NH + h) * SK * DH), (short)0, SK * DH * 2, 0x00020000);

  unsigned vk[3], vv[3];
#pragma unroll
  for (int i = 0; i < 3; ++i) {
    const int pos = (qg * 3 + i) * 64 + lane, row = pos / 24, c = pos - row * 24;
    vk[i] = row * 384 + ((c ^ ((row >> 1) & 7)) << 4) - 1024 * i;
    vv[i] = row * 384 + ((c ^ (row & 6)) << 4) - 1024 * i;
  }
  unsigned vw0, vw1;
  {
    int pos = w * 96 + lane, row = pos / 24, c = pos - row * 24;
    vw0 = row * 12288 + ((c ^ (((row >> 1) & 1) | (((row >> 3) & 1) << 2))) << 4);
    pos = w * 96 + 64 + (lane & 31); row = pos / 24; c = pos - row * 24;
    vw1 = row * 12288 + ((c ^ (((row >> 1) & 1) | (((row >> 3) & 1) << 2))) << 4) - 1024;
  }
  const unsigned vx0 = (qg * 32 + r16) * 1280 + g * 16, vx1 = vx0 + 16 * 1280;
  unsigned wb[4];
#pragma unroll
  for (int s1 = 0; s1 < 2; ++s1)
#pragma unroll
    for (int u = 0; u < 2; ++u)
      wb[s1 * 2 + u] = (8 * g + q4) * 384 + (((4 * s1 + p4) ^ ((q4 >> 1) | ((g & 1) << 2))) << 4) + 8 * ((p4 & 1) ^ u);

  f32x4 acc[12][2];
#pragma unroll
  for (int i = 0; i < 12; ++i) { acc[i][0] = f32x4{0.f, 0.f, 0.f, 0.f}; acc[i][1] = f32x4{0.f, 0.f, 0.f, 0.f}; }
  bf16x8 xa[5], xb[5];
  xa[4] = bf16x8{}; xb[4] = bf16x8{};
  asm volatile("s_nop 4" ::: "memory");
#pragma unroll
  for (int t = 0; t < WD; ++t) {
    int kt = t + rot; kt = kt >= 20 ? kt - 20 : kt;
    dma_w(ldsb + wslot(t) * SLOT + w * 1536, vw0, vw1, srdW, (unsigned)kt * (32u * 12288u));
    ld_x2(xa[t], xb[t], vx0, vx1, srdX, (unsigned)kt * 64u);
  }
  qproj_all<0>(acc, xa, xb, lds, ldsb, wb, w, item, rot, vw0, vw1, vx0, vx1, vk, vv, srdW, srdX, srdK, srdV);

  bf16x8 qf[6][2];
  {
    const bool odd = g & 1;
#pragma unroll
    for (int s = 0; s < 6; ++s)
#pragma unroll
      for (int n = 0; n < 2; ++n) {
        const unsigned a0 = cvtpk(acc[2 * s][n][0], acc[2 * s][n][1]), a1 = cvtpk(acc[2 * s][n][2], acc[2 * s][n][3]);
        const unsigned b0 = cvtpk(acc[2 * s + 1][n][0], acc[2 * s + 1][n][1]), b1 = cvtpk(acc[2 * s + 1][n][2], acc[2 * s + 1][n][3]);
        const u32x4 v = {odd ? b0 : a0, odd ? b1 : a1, odd ? a0 : b0, odd ? a1 : b1};
        qf[s][n] = __builtin_bit_cast(bf16x8, v);
      }
  }
#pragma unroll
  for (int i = 0; i < 12; ++i) { acc[i][0] = f32x4{0.f, 0.f, 0.f, 0.f}; acc[i][1] = f32x4{0.f, 0.f, 0.f, 0.f}; }

  unsigned kb[2], vb[4];
#pragma unroll
  for (int s1 = 0; s1 < 2; ++s1) kb[s1] = item * 6 * SLOT + r16 * 384 + (((4 * s1 + g) ^ ((r16 >> 1) & 7)) << 4);
#pragma unroll
  for (int t1 = 0; t1 < 4; ++t1) { const int row = 4 * g + q4; vb[t1] = item * 6 * SLOT + row * 384 + (((2 * t1 + (p4 >> 1)) ^ (row & 6)) << 4) + 8 * (p4 & 1); }

  float m_run[2] = {-__builtin_inff(), -__builtin_inff()}, l_run[2] = {0.f, 0.f};
  const unsigned m0base = ldsb + item * 6 * SLOT + qg * 3072;
  unsigned cur = 0, prev = 5 * SLOT;
#define ADV() do { prev = cur; cur = (cur == 5 * SLOT) ? 0u : cur + SLOT; } while (0)
#define QK(S0, S1) do { \
    _Pragma("unroll") for (int n = 0; n < 2; ++n) { S0[n] = f32x4{0.f, 0.f, 0.f, 0.f}; S1[n] = f32x4{0.f, 0.f, 0.f, 0.f}; } \
    _Pragma("unroll") for (int s = 0; s < 6; ++s) { \
      const lds_cptr p = lds + cur + kb[s & 1] + 128 * (s >> 1); \
      const bf16x8 k0 = lds_rd128(p), k1 = lds_rd128(p + 6144); \
      S0[0] = MFMA16(k0, qf[s][0], S0[0]); S0[1] = MFMA16(k0, qf[s][1], S0[1]); \
      S1[0] = MFMA16(k1, qf[s][0], S1[0]); S1[1] = MFMA16(k1, qf[s][1], S1[1]); } } while (0)
#define PV(P) do { \
    _Pragma("unroll") for (int t = 0; t < 12; ++t) { \
      const lds_cptr p = lds + cur + vb[t & 3] + 128 * (t >> 2); \
      const bf16x8 a = cat8(lds_tr(p), lds_tr(p + 6144)); \
      acc[t][0] = MFMA16(a, P[0], acc[t][0]); acc[t][1] = MFMA16(a, P[1], acc[t][1]); } } while (0)

  for (int s = 0; s < 8; ++s) {
    f32x4 s0[2], s1[2], s2[2], s3[2];
    bf16x8 pa[2], pb[2];
    wait_bar<12>();
    dma_tile3(m0base + prev, vk[0], vk[1], vk[2], srdK, (unsigned)(2 * s + 3) * SLOT);
    QK(s0, s1); ADV(); SBAR();
    wait_bar<12>();
    dma_tile3(m0base + prev, vv[0], vv[1], vv[2], srdV, (unsigned)(2 * s + 2) * SLOT);
    QK(s2, s3); ADV(); SBAR();
#pragma unroll
    for (int n = 0; n < 2; ++n) {
      float mx = fmaxf(fmaxf(s0[n][0], s0[n][1]), fmaxf(s0[n][2], s0[n][3]));
      mx = fmaxf(mx, fmaxf(fmaxf(s1[n][0], s1[n][1]), fmaxf(s1[n][2], s1[n][3])));
      mx = fmaxf(mx, fmaxf(fmaxf(s2[n][0], s2[n][1]), fmaxf(s2[n][2], s2[n][3])));
      mx = fmaxf(mx, fmaxf(fmaxf(s3[n][0], s3[n][1]), fmaxf(s3[n][2], s3[n][3])));
      mx = xmax16_32(mx);
      const float mn = fmaxf(m_run[n], mx * SCL2);
      const float alpha = __builtin_amdgcn_exp2f(m_run[n] - mn);
      m_run[n] = mn;
      float ps = 0.f;
#pragma unroll
      for (int r = 0; r < 4; ++r) {
        s0[n][r] = __builtin_amdgcn_exp2f(fmaf(s0[n][r], SCL2, -mn)); ps += s0[n][r];
        s1[n][r] = __builtin_amdgcn_exp2f(fmaf(s1[n][r], SCL2, -mn)); ps += s1[n][r];
        s2[n][r] = __builtin_amdgcn_exp2f(fmaf(s2[n][r], SCL2, -mn)); ps += s2[n][r];
        s3[n][r] = __builtin_amdgcn_exp2f(fmaf(s3[n][r], SCL2, -mn)); ps += s3[n][r];
      }
      l_run[n] = l_run[n] * alpha + ps;
      const u32x4 wa = {cvtpk(s0[n][0], s0[n][1]), cvtpk(s0[n][2], s0[n][3]), cvtpk(s1[n][0], s1[n][1]), cvtpk(s1[n][2], s1[n][3])};
      const u32x4 wb2 = {cvtpk(s2[n][0], s2[n][1]), cvtpk(s2[n][2], s2[n][3]), cvtpk(s3[n][0], s3[n][1]), cvtpk(s3[n][2], s3[n][3])};
      pa[n] = __builtin_bit_cast(bf16x8, wa); pb[n] = __builtin_bit_cast(bf16x8, wb2);
#pragma unroll
      for (int t = 0; t < 12; ++t) acc[t][n] *= alpha;
    }
    wait_bar<12>();
    dma_tile3(m0base + prev, vv[0], vv[1], vv[2], srdV, (unsigned)(2 * s + 3) * SLOT);
    PV(pa); ADV(); SBAR();
    wait_bar<12>();
    dma_tile3(m0base + prev, vk[0], vk[1], vk[2], srdK, (unsigned)(2 * s + 4) * SLOT);
    PV(pb); ADV(); SBAR();
  }
  char* ob = (char*)ows + ((size_t)((br * NB + b) * NH + h) * 42) * 1024 + lane * 16;
#pragma unroll
  for (int n = 0; n < 2; ++n) {
    const float inv = 1.0f / xsum16_32(l_run[n]);
    const int qt = 2 * qg + n;
    if (qt < 7) {
#pragma unroll
      for (int s = 0; s < 6; ++s) {
        const f32x4 a = acc[2 * s][n] * inv, c = acc[2 * s + 1][n] * inv;
        const u32x4 v = {cvtpk(a[0], a[1]), cvtpk(a[2], a[3]), cvtpk(c[0], c[1]), cvtpk(c[2], c[3])};
        *(u32x4*)(ob + (s * 7 + qt) * 1024) = v;
      }
    }
  }
  asm volatile("s_waitcnt vmcnt(0)" ::: "memory");
}

constexpr int OST = 14336 + 20480;
constexpr int ONST = 4;
__device__ __forceinline__ void dma_ostage(unsigned m0o, unsigned m0w, unsigned vo, unsigned vw0, unsigned vw1, unsigned vw2,
                                           rsrc_t srdO, rsrc_t srdWo, unsigned soffO, unsigned soffW) {
  asm volatile(
      "s_mov_b32 m0, %0\n\ts_nop 0\n\t"
      "buffer_load_dwordx4 %2, %6, %8 offen lds\n\t"
      "s_mov_b32 exec_hi, 0xffff\n\t"
      "buffer_load_dwordx4 %2, %6, %8 offen offset:1024 lds\n\t"
      "s_mov_b32 exec_hi, -1\n\t"
      "s_mov_b32 m0, %1\n\ts_nop 0\n\t"
      "buffer_load_dwordx4 %3, %7, %9 offen lds\n\t"
      "buffer_load_dwordx4 %4, %7, %9 offen offset:1024 lds\n\t"
      "s_mov_b32 exec_hi, 0\n\t"
      "buffer_load_dwordx4 %5, %7, %9 offen offset:2048 lds\n\t"
      "s_mov_b32 exec_hi, -1"
      ::"s"(m0o), "s"(m0w), "v"(vo), "v"(vw0), "v"(vw1), "v"(vw2), "s"(srdO), "s"(srdWo), "s"(soffO), "s"(soffW) : "memory");
}

__global__ __launch_bounds__(512, 2) void oproj_kernel(const u16* __restrict__ ows, const u16* __restrict__ woA, const u16* __restrict__ woB,
                                                       float* __restrict__ part) {
  __shared__ __attribute__((aligned(16))) char smem[ONST * OST];
  const lds_cptr lds = (lds_cptr)smem;
  const unsigned ldsb = (unsigned)(uintptr_t)smem;
  const int tid = threadIdx.x, lane = tid & 63;
  const int w = RFL(tid >> 6);
  const int bid = blockIdx.x, xcd = bid & 7, idx = bid >> 3;
  const int br = xcd >> 2, ks = xcd & 3, b = idx >> 2, ns = idx & 3;
  const int qg = w & 3, nh = w >> 2;
  const int g = lane >> 4, q4 = (lane >> 2) & 3, p4 = lane & 3;
  const u16* wo = br ? woB : woA;
  const rsrc_t srdO = __builtin_amdgcn_make_buffer_rsrc((void*)((const char*)ows + (size_t)((br * NB + b) * NH + ks * 8) * 42 * 1024), (short)0, 8 * 42 * 1024, 0x00020000);
  const rsrc_t srdWo = __builtin_amdgcn_make_buffer_rsrc((void*)(wo + (size_t)ks * 1536 * DM + ns * 160), (short)0, 1536 * DM * 2 - ns * 320, 0x00020000);
  const unsigned vo = w * 1792 + lane * 16;
  unsigned vw[3];
#pragma unroll
  for (int i = 0; i < 3; ++i) {
    const int pos = w * 160 + i * 64 + (i == 2 ? (lane & 31) : lane), row = pos / 20, c = pos - row * 20;
    vw[i] = row * 1280 + ((c ^ (((row >> 2) & 1) << 1)) << 4) - 1024 * i;
  }
  const unsigned m0o = ldsb + w * 1792, m0w = ldsb + 14336 + w * 2560;
  const unsigned ob = qg * 2048 + lane * 16;
  unsigned wb[2];
  { const int row = 4 * g + q4, bb = (row >> 2) & 1; const int base = 14336 + row * 320 + 8 * p4 + nh * 160;
    wb[0] = base + (nh ? -32 * bb : 32 * bb);
    wb[1] = base + (nh ? 32 * bb : -32 * bb); }
  f32x4 acc[5][2];
#pragma unroll
  for (int i = 0; i < 5; ++i) { acc[i][0] = f32x4{0.f, 0.f, 0.f, 0.f}; acc[i][1] = f32x4{0.f, 0.f, 0.f, 0.f}; }
  asm volatile("s_nop 4" ::: "memory");
#pragma unroll
  for (int t = 0; t < ONST - 1; ++t)
    dma_ostage(m0o + t * OST, m0w + t * OST, vo, vw[0], vw[1], vw[2], srdO, srdWo, t * 14336u, t * 64u * 1280u);
  unsigned cur = 0, prev = (ONST - 1) * OST;
  for (int t = 0; t < 24; ++t) {
    wait_bar<5 * (ONST - 2)>();
    dma_ostage(m0o + prev, m0w + prev, vo, vw[0], vw[1], vw[2], srdO, srdWo, (unsigned)(t + ONST - 1) * 14336u, (unsigned)(t + ONST - 1) * (64u * 1280u));
#pragma unroll
    for (int kk = 0; kk < 2; ++kk) {
      const bf16x8 o0 = lds_rd128(lds + cur + ob + kk * 7168);
      const bf16x8 o1 = lds_rd128(lds + cur + ob + kk * 7168 + 1024);
#pragma unroll
      for (int i = 0; i < 5; ++i) {
        const lds_cptr p = lds + cur + wb[i & 1] + 32 * i + kk * 32 * 320;
        const bf16x8 a = cat8(lds_tr(p), lds_tr(p + 16 * 320));
        acc[i][0] = MFMA16(a, o0, acc[i][0]); acc[i][1] = MFMA16(a, o1, acc[i][1]);
      }
    }
    prev = cur; cur = (cur == (ONST - 1) * OST) ? 0u : cur + OST;
  }
  char* pb = (char*)part + (size_t)(((ks * 2 + br) * NB + b) * 4 + ns) * 70 * 1024 + lane * 16;
#pragma unroll
  for (int i = 0; i < 5; ++i)
#pragma unroll
    for (int n = 0; n < 2; ++n) { const int qt = 2 * qg + n; if (qt < 7) *(f32x4*)(pb + ((nh * 5 + i) * 7 + qt) * 1024) = acc[i][n]; }
  asm volatile("s_waitcnt vmcnt(0)" ::: "memory");
}

__global__ __launch_bounds__(256) void reduce_kernel(const float* __restrict__ part, u16* __restrict__ out) {
  const int i = blockIdx.x * 256 + threadIdx.x;
  constexpr int PER = 2 * NB * 4 * 70 * 64;
  const f32x4* p = (const f32x4*)part + i;
  const f32x4 v = (p[0] + p[PER]) + (p[2 * PER] + p[3 * PER]);
  const int lane = i & 63; int t = i >> 6;
  const int qt = t % 7; t /= 7; const int nu = t % 10; t /= 10; const int ns = t & 3; t >>= 2; const int b = t & 7, br = t >> 3;
  const int q = qt * 16 + (lane & 15), n = ns * 160 + nu * 16 + (lane >> 4) * 4;
  const u32x2 o = {cvtpk(v[0], v[1]), cvtpk(v[2], v[3])};
  *(u32x2*)(out + (size_t)(b * SQ + q) * (2 * DM) + br * DM + n) = o;
}

extern "C" void kernel_launch(void* const* d_in, const int* in_sizes, int n_in, void* d_out, int out_size, void* d_ws, size_t ws_size,
                              hipStream_t stream) {
  u16* ows = (u16*)d_ws;
  float* part = (float*)((char*)d_ws + 24 * 1024 * 1024);
  attn_kernel<<<256, 512, 0, stream>>>((const u16*)d_in[0], (const u16*)d_in[1], (const u16*)d_in[2], (const u16*)d_in[3],
                                       (const u16*)d_in[5], (const u16*)d_in[6], (const u16*)d_in[7], (const u16*)d_in[8], ows);
  oproj_kernel<<<256, 512, 0, stream>>>(ows, (const u16*)d_in[4], (const u16*)d_in[9], part);
  reduce_kernel<<<1120, 256, 0, stream>>>(part, (u16*)d_out);
}
```

```cpp
#include <hip/hip_runtime.h>
#include <hip/hip_bf16.h>
#include <stdint.h>

#define WPOL "nt"
#define XPOL "nt"
#define OPOL "nt"
#define STAUX 0
typedef unsigned short u16;
using bf16x8 = __attribute__((ext_vector_type(8))) short;
using s16x4 = __attribute__((ext_vector_type(4))) short;
using f32x4 = __attribute__((ext_vector_type(4))) float;
using u32x4 = __attribute__((ext_vector_type(4))) unsigned;
using u32x2 = __attribute__((ext_vector_type(2))) unsigned;
typedef __attribute__((address_space(3))) const char* lds_cptr;
typedef __attribute__((address_space(3))) char* lds_ptr;
typedef __amdgpu_buffer_rsrc_t rsrc_t;

constexpr int NB = 8, SQ = 112, DM = 640, NH = 32, SK = 512, DH = 192, HD = NH * DH;
constexpr int SLOT = 12288;
constexpr int WR = 6 * SLOT;
constexpr int XR = 11 * SLOT;
constexpr int XS = 14336;
constexpr float SCL2 = 0.07216878364870322f * 1.4426950408889634f;

#define MFMA16(a, b, c) __builtin_amdgcn_mfma_f32_16x16x32_bf16(a, b, c, 0, 0, 0)
#define SBAR() __builtin_amdgcn_sched_barrier(0)
#define STAMP(i)
#define STA
#define STP
#define RFL(x) __builtin_amdgcn_readfirstlane(x)
#define OPAQUE(x) asm volatile("" : "+v"(x))

typedef __bf16 bf16x2_t __attribute__((ext_vector_type(2)));
typedef float f32x2 __attribute__((ext_vector_type(2)));
__device__ __forceinline__ unsigned cvtpk(float lo, float hi) {
  const f32x2 v = {lo, hi};
  return __builtin_bit_cast(unsigned, __builtin_convertvector(v, bf16x2_t));
}
__device__ __forceinline__ float bflo(unsigned u) { return __uint_as_float(u << 16); }
__device__ __forceinline__ float bfhi(unsigned u) { return __uint_as_float(u & 0xffff0000u); }
__device__ __forceinline__ bf16x8 lds_rd128(lds_cptr p) { return *(const __attribute__((address_space(3))) bf16x8*)p; }
__device__ __forceinline__ void lds_wr128(lds_cptr p, u32x4 v) { *(__attribute__((address_space(3))) u32x4*)p = v; }
typedef short v4i16_t __attribute__((ext_vector_type(4)));
__device__ __forceinline__ s16x4 lds_tr(lds_cptr p) {
  return __builtin_bit_cast(s16x4, __builtin_amdgcn_ds_read_tr16_b64_v4i16((__attribute__((address_space(3))) v4i16_t*)p));
}
__device__ __forceinline__ bf16x8 cat8(s16x4 a, s16x4 b) { return (bf16x8){a[0], a[1], a[2], a[3], b[0], b[1], b[2], b[3]}; }
template <int N> __device__ __forceinline__ void wait_bar() {
  asm volatile("s_waitcnt vmcnt(%0) lgkmcnt(0)\n\ts_barrier" ::"i"(N) : "memory");
}
__device__ __forceinline__ void bar() { asm volatile("s_waitcnt lgkmcnt(0)\n\ts_barrier" ::: "memory"); }
#define DMA3(NAME, POL) \
__device__ __forceinline__ void NAME(unsigned m0v, const unsigned (&v)[3], rsrc_t srd, unsigned soff) { \
  asm volatile("s_mov_b32 m0, %0\n\ts_nop 0\n\t" \
      "buffer_load_dwordx4 %1, %4, %5 offen " POL " lds\n\t" \
      "buffer_load_dwordx4 %2, %4, %5 offen offset:1024 " POL " lds\n\t" \
      "buffer_load_dwordx4 %3, %4, %5 offen offset:2048 " POL " lds" \
      ::"s"(m0v), "v"(v[0]), "v"(v[1]), "v"(v[2]), "s"(srd), "s"(soff) : "memory"); }
DMA3(dma_kv, "nt")
DMA3(dma_w, WPOL)
__device__ __forceinline__ void dma_x(unsigned m0v, unsigned v0, rsrc_t srd, unsigned s0) {
  const unsigned s1 = s0 + 9216, s2 = s0 + 2 * 9216, s3 = s0 + 3 * 9216;
  asm volatile(
      "s_mov_b32 m0, %0\n\ts_nop 0\n\t"
      "buffer_load_dwordx4 %1, %2, %3 offen " XPOL " lds\n\t"
      "buffer_load_dwordx4 %1, %2, %4 offen offset:1024 " XPOL " lds\n\t"
      "buffer_load_dwordx4 %1, %2, %5 offen offset:2048 " XPOL " lds\n\t"
      "s_mov_b32 exec_hi, 0\n\t"
      "buffer_load_dwordx4 %1, %2, %6 offen offset:3072 " XPOL " lds\n\t"
      "s_mov_b32 exec_hi, -1"
      ::"s"(m0v), "v"(v0), "s"(srd), "s"(s0), "s"(s1), "s"(s2), "s"(s3) : "memory");
}
__device__ __forceinline__ void dma_o(unsigned m0v, unsigned v0, rsrc_t srd, unsigned s0) {
  const unsigned s1 = s0 + 9216, s2 = s0 + 2 * 9216;
  asm volatile(
      "s_mov_b32 m0, %0\n\ts_nop 0\n\t"
      "buffer_load_dwordx4 %1, %2, %3 offen " OPOL " lds\n\t"
      "buffer_load_dwordx4 %1, %2, %4 offen offset:1024 " OPOL " lds\n\t"
      "buffer_load_dwordx4 %1, %2, %5 offen offset:2048 " OPOL " lds"
      ::"s"(m0v), "v"(v0), "s"(srd), "s"(s0), "s"(s1), "s"(s2) : "memory");
}
__device__ __forceinline__ void touch(unsigned& sink, unsigned v0, rsrc_t srd, unsigned soff) {
  asm volatile("buffer_load_dword %0, %1, %2, %3 offen nt\n\tbuffer_load_dword %0, %1, %2, %3 offen sc1" : "+v"(sink) : "v"(v0), "s"(srd), "s"(soff) : "memory");
}
__device__ __forceinline__ float xmax16_32(float v) {
  auto a = __builtin_amdgcn_permlane32_swap(__float_as_uint(v), __float_as_uint(v), false, false);
  v = fmaxf(__uint_as_float(a[0]), __uint_as_float(a[1]));
  auto b = __builtin_amdgcn_permlane16_swap(__float_as_uint(v), __float_as_uint(v), false, false);
  return fmaxf(__uint_as_float(b[0]), __uint_as_float(b[1]));
}
__device__ __forceinline__ float xsum16_32(float v) {
  auto a = __builtin_amdgcn_permlane32_swap(__float_as_uint(v), __float_as_uint(v), false, false);
  v = __uint_as_float(a[0]) + __uint_as_float(a[1]);
  auto b = __builtin_amdgcn_permlane16_swap(__float_as_uint(v), __float_as_uint(v), false, false);
  return __uint_as_float(b[0]) + __uint_as_float(b[1]);
}

struct Ctx {
  lds_cptr lds; unsigned ldsb; int g, qg;
  unsigned vw[3], vk[3], vv[3], vx, vo;
  rsrc_t srdW, srdX, srdK, srdV, srdO, srdP;
};
constexpr unsigned WSTEP = 32u * HD * 2;
__device__ __forceinline__ void issue_w(const Ctx& c, int slot, unsigned soff) { dma_w(c.ldsb + WR + slot * SLOT + c.qg * 3072, c.vw, c.srdW, soff); }
__device__ __forceinline__ void issue_x(const Ctx& c, int slot, unsigned soff) { dma_x(c.ldsb + XR + slot * XS + c.qg * 3584, c.vx, c.srdX, soff); }
template <int J> __device__ __forceinline__ void issue_kv0(const Ctx& c) {
  constexpr bool isK = (J & 2) == 0;
  constexpr int tl = (J >> 2) * 2 + (J & 1);
  if constexpr (isK) dma_kv(c.ldsb + J * SLOT + c.qg * 3072, c.vk, c.srdK, tl * SLOT);
  else dma_kv(c.ldsb + J * SLOT + c.qg * 3072, c.vv, c.srdV, tl * SLOT);
}
constexpr int chunk_of(int pass, int p) { return pass == 0 ? p : p == 0 ? 9 : p < 8 ? p + 1 : p - 8; }
constexpr unsigned usoff(int us) { return (unsigned)(((us / 20) * 192 + (us & 1) * 96) * 1280 + chunk_of(us / 20, (us % 20) >> 1) * 128); }
template <int US> __device__ __forceinline__ void issue_o(const Ctx& c) { dma_o(c.ldsb + WR + (US % 5) * SLOT + c.qg * 3072, c.vo, c.srdO, usoff(US)); }

constexpr int la_ops(int t) { return (t + 3 < 20 ? 3 : 0) + ((!(t & 1) && t / 2 + 2 < 10) ? 4 : 0) + (t == 16 ? 3 : (t == 17 || t == 18) ? 6 : 0); }
constexpr int la_wait(int t) {
  if (t < 17) return 10;
  return t == 17 ? la_ops(15) + la_ops(16) : t == 18 ? la_ops(16) + la_ops(17) : 3 + la_ops(17) + la_ops(18);
}
template <int T> __device__ __forceinline__ void loader_a(const Ctx& c) {
  if constexpr (T < 20) {
    wait_bar<la_wait(T)>();
    if constexpr (T + 3 < 20) issue_w(c, (T + 3) % 4, (T + 3) * WSTEP);
    if constexpr (!(T & 1) && T / 2 + 2 < 10) issue_x(c, (T / 2) & 1, (T / 2 + 2) * 128);
    if constexpr (T == 16) issue_kv0<0>(c);
    if constexpr (T == 17) { issue_kv0<1>(c); issue_kv0<2>(c); }
    if constexpr (T == 18) { issue_kv0<3>(c); issue_kv0<4>(c); }
    loader_a<T + 1>(c);
  }
}

struct QA { unsigned wb[4], xa[2][2]; };
template <int T, int F> __device__ __forceinline__ bf16x8 wfrag(const Ctx& c, const QA& q) {
  constexpr int s = F >> 1, u = F & 1;
  const lds_cptr p = c.lds + q.wb[(s & 1) * 2 + u] + T * SLOT + 128 * (s >> 1);
  return cat8(lds_tr(p), lds_tr(p + 1536));
}
template <int P> __device__ __forceinline__ void read_x(bf16x8 (&xf)[2][2][2], const Ctx& c, const QA& q) {
#pragma unroll
  for (int k = 0; k < 2; ++k)
#pragma unroll
    for (int n = 0; n < 2; ++n) xf[P][k][n] = lds_rd128(c.lds + q.xa[k][n] + P * XS);
}
template <int T> __device__ __forceinline__ void q_step(f32x4 (&acc)[12][2], bf16x8 (&xf)[2][2][2], bf16x8 (&wf)[8], const Ctx& c, const QA& q,
                                                        unsigned sw, unsigned sx) {
  wait_bar<10>();
  if constexpr (T & 1) read_x<((T + 1) >> 1) & 1>(xf, c, q);
  SBAR();
  constexpr int U = (T + 3) & 3;
#define RD(F) do { wf[(12 * T + F) % 8] = wfrag<T, F>(c, q); } while (0)
#define MP(F) do { acc[F][0] = MFMA16(wf[(12 * U + F) % 8], xf[U >> 1][U & 1][0], acc[F][0]); acc[F][1] = MFMA16(wf[(12 * U + F) % 8], xf[U >> 1][U & 1][1], acc[F][1]); SBAR(); } while (0)
#define MC(F) do { acc[F][0] = MFMA16(wf[(12 * T + F) % 8], xf[T >> 1][T & 1][0], acc[F][0]); acc[F][1] = MFMA16(wf[(12 * T + F) % 8], xf[T >> 1][T & 1][1], acc[F][1]); SBAR(); } while (0)
  RD(0); RD(1); MP(8);
  RD(2); RD(3); MP(9);
  if (c.g) {
    issue_w(c, U, sw);
    if constexpr (!(T & 1)) issue_x(c, T >> 1, sx);
  }
  SBAR();
  RD(4); MP(10);
  RD(5); MP(11);
  RD(6); MC(0); RD(7); MC(1); RD(8); MC(2); RD(9); MC(3); RD(10); MC(4); RD(11); MC(5); MC(6); MC(7);
#undef RD
#undef MC
}

struct OA { unsigned ob[4], vst[2], pka[2]; };
template <int PASS, int U, int F> __device__ __forceinline__ void o_mfma(f32x4 (&acc)[4][2], const bf16x8 (&of)[6][2], const bf16x8 (&wf)[8],
                                                                        const u32x4 (&pk)[10][2][2]) {
  constexpr int cc = chunk_of(PASS, U >> 1), kh = U & 1, i = F / 3, ks = F % 3;
#pragma unroll
  for (int n = 0; n < 2; ++n) {
    f32x4 cin = acc[i][n];
    if constexpr (kh == 0 && ks == 0) {
      if constexpr (PASS == 0) cin = f32x4{0.f, 0.f, 0.f, 0.f};
      else if constexpr (cc != 9) { const unsigned a = pk[cc][i >> 1][n][(i & 1) * 2], b = pk[cc][i >> 1][n][(i & 1) * 2 + 1]; cin = f32x4{bflo(a), bfhi(a), bflo(b), bfhi(b)}; }
    }
    acc[i][n] = MFMA16(wf[(12 * U + F) % 8], of[3 * kh + ks][n], cin);
  }
  SBAR();
}
template <int PASS, int U, int NP> __device__ __forceinline__ void o_fin(f32x4 (&acc)[4][2], u32x4 (&pk)[10][2][2], const Ctx& c, const OA& o) {
  constexpr int cc = chunk_of(PASS, U >> 1);
  if constexpr (PASS == 0 && cc == 9) {
    OPAQUE(acc[2 * NP][0]); OPAQUE(acc[2 * NP][1]); OPAQUE(acc[2 * NP + 1][0]); OPAQUE(acc[2 * NP + 1][1]);
    SBAR();
  } else if constexpr (U & 1) {
#pragma unroll
    for (int n = 0; n < 2; ++n) {
      const f32x4 a = acc[2 * NP][n], b = acc[2 * NP + 1][n];
      const u32x4 v = {cvtpk(a[0], a[1]), cvtpk(a[2], a[3]), cvtpk(b[0], b[1]), cvtpk(b[2], b[3])};
      if constexpr (PASS == 1) __builtin_amdgcn_raw_buffer_store_b128(v, c.srdP, o.vst[n] + NP * 1024, cc * XS, STAUX);
      else if constexpr (cc < 2) { if (n == 0 || c.qg < 3) lds_wr128(c.lds + o.pka[n] + cc * XS + NP * 1024, v); }
      else pk[cc][NP][n] = v;
    }
    SBAR();
  }
}
#define C_IDLE(U) ((U & 1) && U < 16)
template <int PASS, int U> __device__ __forceinline__ void o_step(f32x4 (&acc)[4][2], const bf16x8 (&of)[6][2], bf16x8 (&wf)[8], u32x4 (&pk)[10][2][2],
                                                                  const Ctx& c, const OA& o STA) {
  if constexpr (U < 20) { if constexpr (PASS == 0) wait_bar<9>(); else bar(); }
  if constexpr (PASS == 1 && (U == 15 || U == 17)) {
    constexpr int cc = chunk_of(1, (U + 1) >> 1);
#pragma unroll
    for (int np = 0; np < 2; ++np)
#pragma unroll
      for (int n = 0; n < 2; ++n) pk[cc][np][n] = __builtin_bit_cast(u32x4, lds_rd128(c.lds + o.pka[n] + cc * XS + np * 1024));
  }
  SBAR();
#define RD(F) do { if constexpr (U < 20) { const lds_cptr p = c.lds + o.ob[F / 3] + (U % 5) * SLOT + (F % 3) * 4096; wf[(12 * U + F) % 8] = cat8(lds_tr(p), lds_tr(p + 2048)); } } while (0)
#define OP(F) do { if constexpr (U > 0) o_mfma<PASS, (U > 0 ? U - 1 : 0), F>(acc, of, wf, pk); else SBAR(); } while (0)
#define OC(F) o_mfma<PASS, U, F>(acc, of, wf, pk)
  RD(0); RD(1); OP(8);
  RD(2); RD(3); OP(9);
  if constexpr (PASS == 0 && U < 20) { issue_o<U + 4>(c); SBAR(); }
  RD(4); OP(10);
  RD(5); OP(11);
  if constexpr (U < 20) {
    RD(6); OC(0); RD(7); OC(1);
    if constexpr (U > 0) o_fin<PASS, (U > 0 ? U - 1 : 0), 1>(acc, pk, c, o);
    RD(8); OC(2); RD(9); OC(3); RD(10); OC(4); RD(11); OC(5); OC(6); OC(7);
    o_fin<PASS, U, 0>(acc, pk, c, o);
    if constexpr (PASS == 0 && C_IDLE(U)) bar();
    o_step<PASS, U + 1>(acc, of, wf, pk, c, o STP);
  } else {
    o_fin<PASS, 19, 1>(acc, pk, c, o);
  }
#undef RD
#undef OP
#undef OC
}
template <int U> __device__ __forceinline__ void loader_d(const Ctx& c) {
  if constexpr (U < 20) {
    wait_bar<(U + 3 < 20 ? 9 : 3 * (19 - U))>();
    if constexpr (U + 4 < 20) issue_o<20 + U + 4>(c);
    loader_d<U + 1>(c);
  }
}

__global__ __launch_bounds__(512, 2) void fused_kernel(
    const u16* __restrict__ xA, const u16* __restrict__ kA, const u16* __restrict__ vA, const u16* __restrict__ wqA, const u16* __restrict__ woA,
    const u16* __restrict__ xB, const u16* __restrict__ kB, const u16* __restrict__ vB, const u16* __restrict__ wqB, const u16* __restrict__ woB,
    char* __restrict__ part) {
  __shared__ __attribute__((aligned(16))) char smem[163840];
  const lds_cptr lds = (lds_cptr)smem;
  const unsigned ldsb = (unsigned)(uintptr_t)smem;
  const int tid = threadIdx.x, lane = tid & 63;
  const int w = RFL(tid >> 6);
  const int bid = blockIdx.x, xcd = bid & 7, idx = bid >> 3;
  const int br = xcd >> 2, hp = (xcd & 3) * 4 + (idx >> 3), b = idx & 7, h0 = 2 * hp;
  const int grp = w >> 2, qg = w & 3;
  const int g = lane >> 4, r16 = lane & 15, q4 = (lane >> 2) & 3, p4 = lane & 3;
  Ctx c;
  c.lds = lds; c.ldsb = ldsb; c.g = grp; c.qg = qg;
  {
    const u16* x = br ? xB : xA; const u16* kc = br ? kB : kA; const u16* vc = br ? vB : vA; const u16* wq = br ? wqB : wqA; const u16* wo = br ? woB : woA;
    c.srdW = __builtin_amdgcn_make_buffer_rsrc((void*)(wq + h0 * DH), (short)0, DM * HD * 2 - h0 * DH * 2, 0x00020000);
    c.srdX = __builtin_amdgcn_make_buffer_rsrc((void*)(x + (size_t)b * SQ * DM), (short)0, SQ * DM * 2, 0x00020000);
    c.srdK = __builtin_amdgcn_make_buffer_rsrc((void*)(kc + (size_t)(b * NH + h0) * SK * DH), (short)0, 2 * SK * DH * 2, 0x00020000);
    c.srdV = __builtin_amdgcn_make_buffer_rsrc((void*)(vc + (size_t)(b * NH + h0) * SK * DH), (short)0, 2 * SK * DH * 2, 0x00020000);
    c.srdO = __builtin_amdgcn_make_buffer_rsrc((void*)(wo + (size_t)h0 * DH * DM), (short)0, 2 * DH * DM * 2, 0x00020000);
    c.srdP = __builtin_amdgcn_make_buffer_rsrc((void*)(part + (size_t)((br * 8 + b) * 16 + hp) * (10 * XS)), (short)0, 10 * XS, 0x00020000);
  }
#pragma unroll
  for (int i = 0; i < 3; ++i) {
    const int pos = (qg * 3 + i) * 64 + lane, row = pos / 24, ch = pos - row * 24;
    c.vw[i] = row * (HD * 2) + ((ch ^ (((row >> 1) & 1) | (((row >> 3) & 1) << 2))) << 4) - 1024 * i;
    c.vk[i] = row * 384 + ((ch ^ ((row >> 1) & 7)) << 4) - 1024 * i;
    c.vv[i] = row * 384 + ((ch ^ (row & 6)) << 4) - 1024 * i;
  }
  { const int row = 28 * qg + (lane >> 3); c.vx = row * 1280 + (((lane & 7) ^ (row & 6)) << 4); }
  c.vo = (24 * qg + (lane >> 3)) * 1280 + (((lane & 7) ^ (((lane >> 4) & 3) << 1)) << 4);
  asm volatile("s_nop 4" ::: "memory");

  unsigned sink = 0;
  if (grp) {
    issue_w(c, 0, 0); issue_x(c, 0, 0); issue_w(c, 1, WSTEP); issue_x(c, 1, 128); issue_w(c, 2, 2 * WSTEP);
    wait_bar<10>();
    loader_a<0>(c);
    STAMP(2);
    wait_bar<12>(); issue_w(c, 0, 384); issue_x(c, 0, 0);
    wait_bar<9 + 7>(); issue_w(c, 1, 384 + WSTEP); issue_x(c, 1, 128);
    wait_bar<6 + 14>(); issue_w(c, 2, 384 + 2 * WSTEP);
  }

  f32x4 acc[12][2];
  bf16x8 qf[6][2];
  {
    QA q;
#pragma unroll
    for (int s1 = 0; s1 < 2; ++s1)
#pragma unroll
      for (int u = 0; u < 2; ++u) {
        q.wb[s1 * 2 + u] = WR + (8 * g + q4) * 384 + (((4 * s1 + p4) ^ ((q4 >> 1) | ((g & 1) << 2))) << 4) + 8 * ((p4 & 1) ^ u);
        OPAQUE(q.wb[s1 * 2 + u]);
      }
    q.xa[0][0] = XR + (32 * qg + r16) * 128 + ((g ^ (r16 & 6)) << 4); q.xa[0][1] = q.xa[0][0] + (qg < 3 ? 2048 : 0);
    q.xa[1][0] = q.xa[0][0] ^ 64u; q.xa[1][1] = q.xa[0][1] ^ 64u;
    OPAQUE(q.xa[0][0]); OPAQUE(q.xa[0][1]); OPAQUE(q.xa[1][0]); OPAQUE(q.xa[1][1]);
    bf16x8 xf[2][2][2], wf[8];
#pragma unroll
    for (int i = 0; i < 12; ++i) { acc[i][0] = f32x4{0.f, 0.f, 0.f, 0.f}; acc[i][1] = f32x4{0.f, 0.f, 0.f, 0.f}; }
#pragma unroll
    for (int i = 0; i < 8; ++i) wf[i] = bf16x8{0, 0, 0, 0, 0, 0, 0, 0};
    xf[1][0][0] = xf[1][0][1] = xf[1][1][0] = xf[1][1][1] = bf16x8{0, 0, 0, 0, 0, 0, 0, 0};
    SBAR();
    wait_bar<10>();
    STAMP(1);
    read_x<0>(xf, c, q);
#pragma unroll 1
    for (int it = 0; it < 5; ++it) {
      const unsigned sw = 384 + (unsigned)(4 * it + 3) * WSTEP;
      const unsigned sx = it < 4 ? (unsigned)(2 * it + 2) * 128u : 0x40000000u;
      q_step<0>(acc, xf, wf, c, q, sw, sx);
      q_step<1>(acc, xf, wf, c, q, sw + WSTEP, 0);
      if (grp) bar();
      q_step<2>(acc, xf, wf, c, q, sw + 2 * WSTEP, sx + 128);
      q_step<3>(acc, xf, wf, c, q, sw + 3 * WSTEP, 0);
    }
    { constexpr int U = 3; MP(8); MP(9); MP(10); MP(11); }
#undef MP
    const bool odd = g & 1;
#pragma unroll
    for (int s = 0; s < 6; ++s)
#pragma unroll
      for (int n = 0; n < 2; ++n) {
        const f32x4 a = acc[2 * s][n] * SCL2, d = acc[2 * s + 1][n] * SCL2;
        const unsigned a0 = cvtpk(a[0], a[1]), a1 = cvtpk(a[2], a[3]);
        const unsigned b0 = cvtpk(d[0], d[1]), b1 = cvtpk(d[2], d[3]);
        const u32x4 v = {odd ? b0 : a0, odd ? b1 : a1, odd ? a0 : b0, odd ? a1 : b1};
        qf[s][n] = __builtin_bit_cast(bf16x8, v);
      }
  }
#pragma unroll
  for (int i = 0; i < 12; ++i) { acc[i][0] = f32x4{0.f, 0.f, 0.f, 0.f}; acc[i][1] = f32x4{0.f, 0.f, 0.f, 0.f}; }
  SBAR();
  STAMP(3);
  if (grp) { wait_bar<0>(); bar(); bar(); }

  float l_run[2] = {0.f, 0.f};
  {
    unsigned kb[2], vb[4];
#pragma unroll
    for (int s1 = 0; s1 < 2; ++s1) kb[s1] = r16 * 384 + (((4 * s1 + g) ^ ((r16 >> 1) & 7)) << 4);
#pragma unroll
    for (int t1 = 0; t1 < 4; ++t1) { const int row = 4 * g + q4; vb[t1] = row * 384 + (((2 * t1 + (p4 >> 1)) ^ (row & 6)) << 4) + 8 * (p4 & 1); }
    f32x4 negm[2] = {f32x4{0.f, 0.f, 0.f, 0.f}, f32x4{0.f, 0.f, 0.f, 0.f}};
    const unsigned m0base = ldsb + qg * 3072, tb = grp * 16;
    unsigned cur = grp * 2 * SLOT, prev = grp ? SLOT : 5 * SLOT;
#define ADV() do { prev = cur; cur = (cur == 5 * SLOT) ? 0u : cur + SLOT; } while (0)
#define KRD(s) do { const lds_cptr p = lds + cur + kb[(s) & 1] + 128 * ((s) >> 1); kf[2 * (s)] = lds_rd128(p); kf[2 * (s) + 1] = lds_rd128(p + 6144); } while (0)
#define QK(S0, S1, DMA) do { \
    bf16x8 kf[12]; KRD(0); KRD(1); SBAR(); DMA; SBAR(); \
    _Pragma("unroll") for (int s = 0; s < 6; ++s) { \
      if (s + 2 < 6) KRD(s + 2); \
      S0[0] = MFMA16(kf[2 * s], qf[s][0], s ? S0[0] : negm[0]); S0[1] = MFMA16(kf[2 * s], qf[s][1], s ? S0[1] : negm[1]); \
      S1[0] = MFMA16(kf[2 * s + 1], qf[s][0], s ? S1[0] : negm[0]); S1[1] = MFMA16(kf[2 * s + 1], qf[s][1], s ? S1[1] : negm[1]); SBAR(); } } while (0)
#define VRD(t) do { const lds_cptr p = lds + cur + vb[(t) & 3] + 128 * ((t) >> 2); vf[t] = cat8(lds_tr(p), lds_tr(p + 6144)); } while (0)
#define PV(P, DMA) do { \
    bf16x8 vf[12]; VRD(0); VRD(1); VRD(2); VRD(3); SBAR(); DMA; SBAR(); \
    _Pragma("unroll") for (int t = 0; t < 12; ++t) { \
      if (t + 4 < 12) VRD(t + 4); \
      acc[t][0] = MFMA16(vf[t], P[0], acc[t][0]); acc[t][1] = MFMA16(vf[t], P[1], acc[t][1]); SBAR(); } } while (0)
#define MAX3(a, b, c) fmaxf(fmaxf(a, b), c)
#define AWAIT(i) do { if (last) wait_bar<9 - 3 * i>(); else wait_bar<12>(); } while (0)
#define KDMA(T) do { const unsigned t_ = tb + (T); if (t_ < 32) dma_kv(m0base + prev, c.vk, c.srdK, t_ * SLOT); } while (0)
#define VDMA(T) do { const unsigned t_ = tb + (T); if (t_ < 32) dma_kv(m0base + prev, c.vv, c.srdV, t_ * SLOT); } while (0)
#pragma unroll 1
    for (int s = 0; s < 8; ++s) {
      f32x4 s0[2], s1[2], s2[2], s3[2];
      bf16x8 pa[2], pb[2];
      const bool last = grp && s == 7;
      AWAIT(0); STAMP(4 + s);
      QK(s0, s1, KDMA(2 * s + 3)); ADV();
      AWAIT(1);
      QK(s2, s3, VDMA(2 * s + 2)); ADV();
      float mx[2];
#pragma unroll
      for (int n = 0; n < 2; ++n) {
        float m = MAX3(s0[n][0], s0[n][1], s0[n][2]);
        m = MAX3(m, s0[n][3], s1[n][0]); m = MAX3(m, s1[n][1], s1[n][2]); m = MAX3(m, s1[n][3], s2[n][0]);
        m = MAX3(m, s2[n][1], s2[n][2]); m = MAX3(m, s2[n][3], s3[n][0]); m = MAX3(m, s3[n][1], s3[n][2]);
        mx[n] = fmaxf(m, s3[n][3]);
      }
      if (s == 0 || __builtin_amdgcn_ballot_w64(fmaxf(mx[0], mx[1]) > 6.0f) != 0) {
        const float lo = s == 0 ? -__builtin_inff() : 0.f;
#pragma unroll
        for (int n = 0; n < 2; ++n) {
          const float d = fmaxf(xmax16_32(mx[n]), lo);
          const float alpha = __builtin_amdgcn_exp2f(fminf(-d, 0.f));
          s0[n] -= d; s1[n] -= d; s2[n] -= d; s3[n] -= d; negm[n] -= d;
          l_run[n] *= alpha;
#pragma unroll
          for (int t = 0; t < 12; ++t) acc[t][n] *= alpha;
        }
      }
#pragma unroll
      for (int n = 0; n < 2; ++n) {
        float ps = 0.f;
#pragma unroll
        for (int r = 0; r < 4; ++r) {
          s0[n][r] = __builtin_amdgcn_exp2f(s0[n][r]); ps += s0[n][r];
          s1[n][r] = __builtin_amdgcn_exp2f(s1[n][r]); ps += s1[n][r];
          s2[n][r] = __builtin_amdgcn_exp2f(s2[n][r]); ps += s2[n][r];
          s3[n][r] = __builtin_amdgcn_exp2f(s3[n][r]); ps += s3[n][r];
        }
        l_run[n] += ps;
        const u32x4 wa = {cvtpk(s0[n][0], s0[n][1]), cvtpk(s0[n][2], s0[n][3]), cvtpk(s1[n][0], s1[n][1]), cvtpk(s1[n][2], s1[n][3])};
        const u32x4 wb2 = {cvtpk(s2[n][0], s2[n][1]), cvtpk(s2[n][2], s2[n][3]), cvtpk(s3[n][0], s3[n][1]), cvtpk(s3[n][2], s3[n][3])};
        pa[n] = __builtin_bit_cast(bf16x8, wa); pb[n] = __builtin_bit_cast(bf16x8, wb2);
      }
      SBAR();
      AWAIT(2);
      PV(pa, VDMA(2 * s + 3)); ADV();
      AWAIT(3);
      PV(pb, KDMA(2 * s + 4)); ADV();
    }
  }
  STAMP(12);
  if (!grp) { wait_bar<12>(); issue_o<0>(c); SBAR(); }
  bf16x8 of[6][2];
#pragma unroll
  for (int n = 0; n < 2; ++n) {
    const float inv = 1.0f / xsum16_32(l_run[n]);
#pragma unroll
    for (int s = 0; s < 6; ++s) {
      const f32x4 a = acc[2 * s][n] * inv, d = acc[2 * s + 1][n] * inv;
      const u32x4 v = {cvtpk(a[0], a[1]), cvtpk(a[2], a[3]), cvtpk(d[0], d[1]), cvtpk(d[2], d[3])};
      of[s][n] = __builtin_bit_cast(bf16x8, v);
    }
  }
  const unsigned exa = (qg < 3 ? qg : 4) * SLOT + lane * 16;
  if (grp) {
#pragma unroll
    for (int s = 0; s < 6; ++s)
#pragma unroll
      for (int n = 0; n < 2; ++n) lds_wr128(lds + exa + (s * 2 + n) * 1024, __builtin_bit_cast(u32x4, of[s][n]));
  }
  int grp2 = grp;
  asm volatile("" : "+s"(grp2) :: "memory");
  if (grp2) {
    bar();
    STAMP(13);
    loader_d<0>(c);
    STAMP(14);
  } else {
    OA o;
    { const int row = 4 * g + q4, sw2 = (row >> 1) & 3;
#pragma unroll
      for (int i = 0; i < 4; ++i) { o.ob[i] = WR + row * 128 + ((i ^ sw2) << 5) + 8 * p4; OPAQUE(o.ob[i]); } }
    o.vst[0] = qg * 4096 + lane * 16; o.vst[1] = qg < 3 ? o.vst[0] + 2048 : 0x40000000u;
    o.pka[0] = XR + qg * 4096 + lane * 16; o.pka[1] = o.pka[0] + (qg < 3 ? 2048 : 0);
    OPAQUE(o.pka[0]); OPAQUE(o.pka[1]);
    SBAR();
    wait_bar<12>(); issue_o<1>(c);
    wait_bar<12>(); issue_o<2>(c);
    wait_bar<12>(); issue_o<3>(c);
    f32x4 oc[4][2];
    bf16x8 wf[8];
    u32x4 pk[10][2][2];
    STAMP(13);
    o_step<0, 0>(oc, of, wf, pk, c, o STP);
    STAMP(14);
    wait_bar<0>();
    STAMP(15);
#pragma unroll
    for (int s = 0; s < 6; ++s)
#pragma unroll
      for (int n = 0; n < 2; ++n) of[s][n] = lds_rd128(lds + exa + (s * 2 + n) * 1024);
    SBAR();
    o_step<1, 0>(oc, of, wf, pk, c, o STP);
    STAMP(29);
  }
  asm volatile("s_waitcnt vmcnt(0)" : "+v"(sink) :: "memory");
}

__global__ __launch_bounds__(256) void reduce_kernel(const char* __restrict__ part, u16* __restrict__ out) {
  const int lane = threadIdx.x & 63, pid = blockIdx.x * 4 + (threadIdx.x >> 6);
  const int grp = pid / 140, pc = pid - grp * 140;
  const u32x4* src = (const u32x4*)(part + (size_t)grp * 16 * (10 * XS) + pc * 1024 + lane * 16);
  u32x4 v[16];
#pragma unroll
  for (int k = 0; k < 16; ++k) v[k] = __builtin_nontemporal_load(src + k * (10 * XS / 16));
  float s[8];
#pragma unroll
  for (int e = 0; e < 4; ++e) {
    float lo[4], hi[4];
#pragma unroll
    for (int k = 0; k < 4; ++k) {
      lo[k] = (bflo(v[4 * k][e]) + bflo(v[4 * k + 1][e])) + (bflo(v[4 * k + 2][e]) + bflo(v[4 * k + 3][e]));
      hi[k] = (bfhi(v[4 * k][e]) + bfhi(v[4 * k + 1][e])) + (bfhi(v[4 * k + 2][e]) + bfhi(v[4 * k + 3][e]));
    }
    s[2 * e] = (lo[0] + lo[1]) + (lo[2] + lo[3]); s[2 * e + 1] = (hi[0] + hi[1]) + (hi[2] + hi[3]);
  }
  const int cc = pc / 14, r = pc - cc * 14, qt = r >> 1, np = r & 1;
  const int br = grp >> 3, b = grp & 7;
  u16* o = out + (size_t)(b * SQ + qt * 16 + (lane & 15)) * (2 * DM) + br * DM + cc * 64 + np * 32 + (lane >> 4) * 4;
  *(u32x2*)o = u32x2{cvtpk(s[0], s[1]), cvtpk(s[2], s[3])};
  *(u32x2*)(o + 16) = u32x2{cvtpk(s[4], s[5]), cvtpk(s[6], s[7])};
}

extern "C" void kernel_launch(void* const* d_in, const int* in_sizes, int n_in, void* d_out, int out_size, void* d_ws, size_t ws_size,
                              hipStream_t stream) {
  char* part = (char*)d_ws;
  fused_kernel<<<256, 512, 0, stream>>>((const u16*)d_in[0], (const u16*)d_in[1], (const u16*)d_in[2], (const u16*)d_in[3], (const u16*)d_in[4],
                                        (const u16*)d_in[5], (const u16*)d_in[6], (const u16*)d_in[7], (const u16*)d_in[8], (const u16*)d_in[9], part);
  reduce_kernel<<<560, 256, 0, stream>>>(part, (u16*)d_out);
}
```

```cpp
#include <hip/hip_runtime.h>
#include <stdint.h>

#define POFF 0
typedef unsigned short u16;
using u32x4 = __attribute__((ext_vector_type(4))) unsigned;
using u32x2 = __attribute__((ext_vector_type(2))) unsigned;
typedef __bf16 bf16x2_t __attribute__((ext_vector_type(2)));
typedef float f32x2 __attribute__((ext_vector_type(2)));

constexpr int SQ = 112, DM = 640, NH = 32, SK = 512, DH = 192, HD = NH * DH;
constexpr int SLOT = 12288;
constexpr int WR = 6 * SLOT;
constexpr int XR = 11 * SLOT;
constexpr int XS = 14336;
constexpr int PB = 2293760;
constexpr int GS = PB + 12288 + 0;

__device__ __forceinline__ unsigned cvtpk(float lo, float hi) {
  const f32x2 v = {lo, hi};
  return __builtin_bit_cast(unsigned, __builtin_convertvector(v, bf16x2_t));
}
__device__ __forceinline__ float bflo(unsigned u) { return __uint_as_float(u << 16); }
__device__ __forceinline__ float bfhi(unsigned u) { return __uint_as_float(u & 0xffff0000u); }
__device__ __forceinline__ u32x4 srd(const void* p, unsigned bytes) {
  const unsigned long long a = (unsigned long long)p;
  return u32x4{(unsigned)a, (unsigned)(a >> 32) & 0xffffu, bytes, 0x00020000u};
}
#define C4(a) "v" #a "0", "v" #a "1", "v" #a "2", "v" #a "3", "v" #a "4", "v" #a "5", "v" #a "6", "v" #a "7", "v" #a "8", "v" #a "9"

__global__ __launch_bounds__(512, 2) void fused_kernel(
    const u16* __restrict__ xA, const u16* __restrict__ kA, const u16* __restrict__ vA, const u16* __restrict__ wqA, const u16* __restrict__ woA,
    const u16* __restrict__ xB, const u16* __restrict__ kB, const u16* __restrict__ vB, const u16* __restrict__ wqB, const u16* __restrict__ woB,
    char* __restrict__ part, u16* __restrict__ out) {
  __shared__ __attribute__((aligned(1024))) char smem[163840];
  unsigned ldsb = (unsigned)(uintptr_t)smem;
  int tid = threadIdx.x, bid = blockIdx.x;
  const int w = __builtin_amdgcn_readfirstlane(tid >> 6);
  unsigned grp = w >> 2, qg = w & 3;
  unsigned mk = ldsb + qg * 3072, mx = ldsb + XR + qg * 3584;
  u32x4 srdW, srdX, in0;
  {
    const int lane = tid & 63, h0 = 2 * ((bid & 3) * 4 + (bid >> 6)), b = (bid >> 3) & 7;
    int br = bid & 4;
#pragma unroll
    for (int i = 0; i < 3; ++i) {
      const unsigned pos = (qg * 3 + i) * 64 + lane, row = __umul24(pos, 0xaabu) >> 16, ch = pos - __umul24(row, 24u);
      in0[i] = __umul24(row, (unsigned)(HD * 2)) + ((ch ^ (((row >> 1) & 1) | (((row >> 3) & 1) << 2))) << 4) - 1024 * i;
    }
    const unsigned xrow = 28 * qg + (lane >> 3);
    in0[3] = __umul24(xrow, 1280u) + (((lane & 7) ^ (xrow & 6)) << 4);
    asm volatile("" : "+v"(in0));
    asm volatile("" : "+s"(br));
    srdW = srd((br ? wqB : wqA) + h0 * DH, DM * HD * 2 - h0 * DH * 2);
    srdX = srd((br ? xB : xA) + (size_t)b * SQ * DM, SQ * DM * 2);
  }
  asm volatile(
    "s_nop 4\n"
    "s_cmp_eq_u32 s32, 0\n"
    "s_cbranch_scc1 .Lfz_A0\n"
    "s_add_u32 s55, s34, 0x12000\n"
    "s_mov_b32 s57, 0\n"
    "s_mov_b32 s56, 0\n"
    "s_mov_b32 m0, s55\n"
    "s_nop 0\n"
    "buffer_load_dwordx4 v244, s[8:11], s56 offen lds\n"
    "buffer_load_dwordx4 v245, s[8:11], s56 offen offset:1024 lds\n"
    "buffer_load_dwordx4 v246, s[8:11], s56 offen offset:2048 lds\n"
    "s_add_u32 s56, s56, 0x60000\n"
    "s_mov_b32 m0, s35\n"
    "s_add_u32 s43, s57, 0x2400\n"
    "s_add_u32 s44, s57, 0x4800\n"
    "s_add_u32 s45, s57, 0x6c00\n"
    "buffer_load_dwordx4 v247, s[12:15], s57 offen lds\n"
    "buffer_load_dwordx4 v247, s[12:15], s43 offen offset:1024 lds\n"
    "buffer_load_dwordx4 v247, s[12:15], s44 offen offset:2048 lds\n"
    "s_mov_b32 exec_hi, 0\n"
    "buffer_load_dwordx4 v247, s[12:15], s45 offen offset:3072 lds\n"
    "s_mov_b32 exec_hi, -1\n"
    "s_add_u32 s57, s57, 0x80\n"
    "s_cmp_eq_u32 s57, 0x500\n"
    "s_cselect_b32 s57, 0, s57\n"
    "s_add_u32 m0, s55, 0x3000\n"
    "s_nop 0\n"
    "buffer_load_dwordx4 v244, s[8:11], s56 offen lds\n"
    "buffer_load_dwordx4 v245, s[8:11], s56 offen offset:1024 lds\n"
    "buffer_load_dwordx4 v246, s[8:11], s56 offen offset:2048 lds\n"
    "s_add_u32 s56, s56, 0x60000\n"
    "s_cmp_eq_u32 s56, 0x780000\n"
    "s_cselect_b32 s56, 0x0, s56\n"
    "s_add_u32 m0, s35, 0x3800\n"
    "s_add_u32 s43, s57, 0x2400\n"
    "s_add_u32 s44, s57, 0x4800\n"
    "s_add_u32 s45, s57, 0x6c00\n"
    "buffer_load_dwordx4 v247, s[12:15], s57 offen lds\n"
    "buffer_load_dwordx4 v247, s[12:15], s43 offen offset:1024 lds\n"
    "buffer_load_dwordx4 v247, s[12:15], s44 offen offset:2048 lds\n"
    "s_mov_b32 exec_hi, 0\n"
    "buffer_load_dwordx4 v247, s[12:15], s45 offen offset:3072 lds\n"
    "s_mov_b32 exec_hi, -1\n"
    "s_add_u32 s57, s57, 0x80\n"
    "s_cmp_eq_u32 s57, 0x500\n"
    "s_cselect_b32 s57, 0, s57\n"
    "s_add_u32 m0, s55, 0x6000\n"
    "s_nop 0\n"
    "buffer_load_dwordx4 v244, s[8:11], s56 offen lds\n"
    "buffer_load_dwordx4 v245, s[8:11], s56 offen offset:1024 lds\n"
    "buffer_load_dwordx4 v246, s[8:11], s56 offen offset:2048 lds\n"
    "s_add_u32 s56, s56, 0x60000\n"
    ".Lfz_A0:\n"
    :
    : "{s[8:11]}"(srdW), "{s[12:15]}"(srdX), "{s32}"(grp), "{s34}"(mk), "{s35}"(mx), "{v[244:247]}"(in0)
    : "memory", "scc", "s43", "s44", "s45", "s55", "s56", "s57");
  asm volatile("" : "+v"(tid));
  asm volatile("" : "+s"(bid));
  const int lane = tid & 63, xcd = bid & 7, idx = bid >> 3;
  const int br = xcd >> 2, hp = (xcd & 3) * 4 + (idx >> 3), b = idx & 7, h0 = 2 * hp;
  const int g = lane >> 4, r16 = lane & 15, q4 = (lane >> 2) & 3, p4 = lane & 3;
  const u16* kc = br ? kB : kA; const u16* vc = br ? vB : vA; const u16* wo = br ? woB : woA;
  u32x4 srdK = srd(kc + (size_t)(b * NH + h0) * SK * DH, 2 * SK * DH * 2);
  u32x4 srdV = srd(vc + (size_t)(b * NH + h0) * SK * DH, 2 * SK * DH * 2);
  u32x4 srdO = srd(wo + (size_t)h0 * DH * DM, 2 * DH * DM * 2);
  u32x4 srdP = srd(part + (size_t)(br * 8 + b) * GS, PB + 8192 + 192);
  u32x4 srdU = srd(out + (size_t)b * SQ * 2 * DM + br * DM, (SQ * 2 * DM - br * DM) * 2);
  unsigned hpu = hp == 7 ? 14 : hp == 14 ? 7 : hp;
  unsigned vk[3], vv[3];
#pragma unroll
  for (int i = 0; i < 3; ++i) {
    const int pos = (qg * 3 + i) * 64 + lane, row = pos / 24, ch = pos - row * 24;
    vk[i] = row * 384 + ((ch ^ ((row >> 1) & 7)) << 4) - 1024 * i;
    vv[i] = row * 384 + ((ch ^ (row & 6)) << 4) - 1024 * i;
  }
  u32x4 in1 = {vk[0], vk[1], vk[2], vv[0]};
  u32x4 in2 = {vv[1], vv[2], (unsigned)((24 * qg + (lane >> 3)) * 1280 + (((lane & 7) ^ (((lane >> 4) & 3) << 1)) << 4)), 0u};
  u32x4 wb, xa;
#pragma unroll
  for (int i = 0; i < 4; ++i) wb[i] = ldsb + WR + (8 * g + q4) * 384 + (((4 * (i >> 1) + p4) ^ ((q4 >> 1) | ((g & 1) << 2))) << 4) + 8 * ((p4 & 1) ^ (i & 1));
  xa[0] = ldsb + XR + (32 * qg + r16) * 128 + ((g ^ (r16 & 6)) << 4); xa[1] = xa[0] + (qg < 3 ? 2048 : 0);
  xa[2] = xa[0] ^ 64u; xa[3] = xa[1] ^ 64u;
  const int row = 4 * g + q4;
  u32x4 in3 = {ldsb + r16 * 384 + ((g ^ ((r16 >> 1) & 7)) << 4),
               ldsb + 3 * SLOT + row * 384 + (((p4 >> 1) ^ (row & 6)) << 4) + 8 * (p4 & 1),
               ldsb + WR + row * 128 + (((row >> 1) & 3) << 5) + 8 * p4,
               (unsigned)lane * 16u};
#define ROT 0
  const int hq = hp & 3;
  unsigned rot = ROT ? ((b + (b >> 2) + ((hq * 5 + 1) >> 1)) % 10) * 128u : 0u;
  unsigned gw = (b & 3) == 3 ? 0u : 0x40000000u, gx = (hq & 1) == 0 ? 0u : 0x40000000u, go = (b & 3) == 1 ? 0u : 0x40000000u;
  const int l24 = lane < 23 ? lane : 23, l28 = lane < 27 ? lane : 27;
  const int l60 = lane < 59 ? lane : 59, wi = b * 4 + qg;
  u32x4 tq = {(unsigned)(((lane / 3) * 32 + 4 * b + qg) * (HD * 2) + (lane % 3) * 128), (unsigned)((7 * (hq * 4 + qg) + lane % 7) * 1280 + (lane / 7) * 128), (unsigned)((24 * qg + l24) * 1280),
              (unsigned)((20 * wi + l60 / 3) * (HD * 2) + (l60 % 3) * 128)};
  u32x2 to = {(unsigned)((60 * wi + l60) * 128), (unsigned)(((idx * 4 + qg) * 4 + (lane & 3)) * 128)};
#define PT 0u, 0u, 0u, 0u, 0u, 0u, 0u, 0u
#define PM 0u
  constexpr unsigned ptab[8] = {PT};
  const unsigned cl = (PM >> (4 * b)) & 3u;
  unsigned pace = grp ? (cl == 0 ? ptab[1] : cl == 1 ? ptab[3] : cl == 2 ? ptab[5] : ptab[7]) : (cl == 0 ? ptab[0] : cl == 1 ? ptab[2] : cl == 2 ? ptab[4] : ptab[6]);
  asm volatile(
    "s_nop 4\n"
    ".p2align 3\n"
    ".Lfz_TOP:\n"
    "s_getpc_b64 s[76:77]\n"
    "s_and_b32 s77, s77, 0xffff\n"
    "s_mov_b32 s78, 0xd538\n"
    "s_mov_b32 s79, 0x20000\n"
    "s_add_u32 s54, s34, 0x9000\n"
    "s_add_u32 s55, s34, 0x12000\n"
    "s_mov_b32 s57, 0x100\n"
    "s_mov_b32 s56, 0x120000\n"
    "s_memrealtime s[52:53]\n"
    "s_cmp_eq_u32 s32, 0\n"
    "s_cbranch_scc1 .Lfz_A2\n"
    "buffer_load_dword v236, v243, s[76:79], 0 offen\n"
    "s_branch .Lfz_A1\n"
    ".Lfz_A2:\n"
    "s_mov_b64 exec, 3\n"
    "v_mul_u32_u24_e32 v189, 0x5ff0, v241\n"
    "s_nop 1\n"
    "buffer_load_dword v236, v189, s[16:19], 0 offen\n"
    "buffer_load_dword v236, v189, s[20:23], 0 offen\n"
    "s_mov_b64 exec, -1\n"
    ".Lfz_A1:\n"
    "s_mov_b32 s46, 0x40c00000\n"
    "s_mov_b32 s47, 0xffff0000\n"
    "s_mov_b32 s48, 0x3dd53b94\n"
    "s_mov_b32 s49, 0x3dd53b94\n"
    "s_mov_b32 s50, 0xffff0000\n"
    "s_mov_b32 s51, 0xffff0000\n"
    "s_cmp_eq_u32 s33, 3\n"
    "s_cselect_b32 s80, 0, s48\n"
    "s_cselect_b32 s81, 0, s48\n"
    "s_cselect_b32 s82, 0x7149f2ca, 0\n"
    "s_cselect_b32 s83, 1.0, 0\n"
    "s_cselect_b64 s[84:85], 1, -1\n"
    "v_mov_b64_e32 v[0:1], 0\n"
    "v_mov_b64_e32 v[2:3], 0\n"
    "v_mov_b64_e32 v[4:5], 0\n"
    "v_mov_b64_e32 v[6:7], 0\n"
    "v_mov_b64_e32 v[8:9], 0\n"
    "v_mov_b64_e32 v[10:11], 0\n"
    "v_mov_b64_e32 v[12:13], 0\n"
    "v_mov_b64_e32 v[14:15], 0\n"
    "v_mov_b64_e32 v[16:17], 0\n"
    "v_mov_b64_e32 v[18:19], 0\n"
    "v_mov_b64_e32 v[20:21], 0\n"
    "v_mov_b64_e32 v[22:23], 0\n"
    "v_mov_b64_e32 v[24:25], 0\n"
    "v_mov_b64_e32 v[26:27], 0\n"
    "v_mov_b64_e32 v[28:29], 0\n"
    "v_mov_b64_e32 v[30:31], 0\n"
    "v_mov_b64_e32 v[32:33], 0\n"
    "v_mov_b64_e32 v[34:35], 0\n"
    "v_mov_b64_e32 v[36:37], 0\n"
    "v_mov_b64_e32 v[38:39], 0\n"
    "v_mov_b64_e32 v[40:41], 0\n"
    "v_mov_b64_e32 v[42:43], 0\n"
    "v_mov_b64_e32 v[44:45], 0\n"
    "v_mov_b64_e32 v[46:47], 0\n"
    "v_mov_b64_e32 v[48:49], 0\n"
    "v_mov_b64_e32 v[50:51], 0\n"
    "v_mov_b64_e32 v[52:53], 0\n"
    "v_mov_b64_e32 v[54:55], 0\n"
    "v_mov_b64_e32 v[56:57], 0\n"
    "v_mov_b64_e32 v[58:59], 0\n"
    "v_mov_b64_e32 v[60:61], 0\n"
    "v_mov_b64_e32 v[62:63], 0\n"
    "v_mov_b64_e32 v[64:65], 0\n"
    "v_mov_b64_e32 v[66:67], 0\n"
    "v_mov_b64_e32 v[68:69], 0\n"
    "v_mov_b64_e32 v[70:71], 0\n"
    "v_mov_b64_e32 v[72:73], 0\n"
    "v_mov_b64_e32 v[74:75], 0\n"
    "v_mov_b64_e32 v[76:77], 0\n"
    "v_mov_b64_e32 v[78:79], 0\n"
    "v_mov_b64_e32 v[80:81], 0\n"
    "v_mov_b64_e32 v[82:83], 0\n"
    "v_mov_b64_e32 v[84:85], 0\n"
    "v_mov_b64_e32 v[86:87], 0\n"
    "v_mov_b64_e32 v[88:89], 0\n"
    "v_mov_b64_e32 v[90:91], 0\n"
    "v_mov_b64_e32 v[92:93], 0\n"
    "v_mov_b64_e32 v[94:95], 0\n"
    "v_mov_b64_e32 v[96:97], 0\n"
    "v_mov_b64_e32 v[98:99], 0\n"
    "v_mov_b64_e32 v[100:101], 0\n"
    "v_mov_b64_e32 v[102:103], 0\n"
    "v_mov_b64_e32 v[104:105], 0\n"
    "v_mov_b64_e32 v[106:107], 0\n"
    "v_mov_b64_e32 v[108:109], 0\n"
    "v_mov_b64_e32 v[110:111], 0\n"
    "v_mov_b64_e32 v[112:113], 0\n"
    "v_mov_b64_e32 v[114:115], 0\n"
    "v_mov_b64_e32 v[116:117], 0\n"
    "v_mov_b64_e32 v[118:119], 0\n"
    "v_mov_b64_e32 v[120:121], 0\n"
    "v_mov_b64_e32 v[122:123], 0\n"
    "v_mov_b64_e32 v[124:125], 0\n"
    "v_mov_b64_e32 v[126:127], 0\n"
    "v_mov_b64_e32 v[160:161], 0\n"
    "v_mov_b64_e32 v[162:163], 0\n"
    "v_mov_b64_e32 v[164:165], 0\n"
    "v_mov_b64_e32 v[166:167], 0\n"
    "v_mov_b64_e32 v[168:169], 0\n"
    "v_mov_b64_e32 v[170:171], 0\n"
    "v_mov_b64_e32 v[172:173], 0\n"
    "v_mov_b64_e32 v[174:175], 0\n"
    "s_mov_b32 s41, 5\n"
    "s_waitcnt lgkmcnt(0)\n"
    "s_add_u32 s73, s73, s52\n"
    "s_mul_i32 s42, s32, 1600\n"
    "s_add_u32 s73, s73, s42\n"
    "s_add_u32 s73, s73, 0\n"
    "s_mul_i32 s60, s32, 5\n"
    "s_add_u32 s60, s60, 100\n"
    "s_cmp_eq_u32 s32, 0\n"
    "s_mov_b32 s61, -1025\n"
    "s_cselect_b32 s61, 0xfff00000, s61\n"
    "s_movk_i32 s62, 120\n"
    "s_cselect_b32 s62, 116, s62\n"
    "s_mov_b32 s99, 0xfff80000\n"
    "s_add_u32 s64, s63, 0x80\n"
    "s_cmp_eq_u32 s64, 0x500\n"
    "s_cselect_b32 s64, 0, s64\n"
    "s_add_u32 s65, s64, 0x80\n"
    "s_cmp_eq_u32 s65, 0x500\n"
    "s_cselect_b32 s65, 0, s65\n"
    "s_add_u32 s66, s65, 0x80\n"
    "s_cmp_eq_u32 s66, 0x500\n"
    "s_cselect_b32 s66, 0, s66\n"
    "s_add_u32 s67, s66, 0x80\n"
    "s_cmp_eq_u32 s67, 0x500\n"
    "s_cselect_b32 s67, 0, s67\n"
    "s_add_u32 s68, s67, 0x80\n"
    "s_cmp_eq_u32 s68, 0x500\n"
    "s_cselect_b32 s68, 0, s68\n"
    "s_add_u32 s69, s68, 0x80\n"
    "s_cmp_eq_u32 s69, 0x500\n"
    "s_cselect_b32 s69, 0, s69\n"
    "s_add_u32 s70, s69, 0x80\n"
    "s_cmp_eq_u32 s70, 0x500\n"
    "s_cselect_b32 s70, 0, s70\n"
    "s_add_u32 s71, s70, 0x80\n"
    "s_cmp_eq_u32 s71, 0x500\n"
    "s_cselect_b32 s71, 0, s71\n"
    "s_add_u32 s72, s71, 0x80\n"
    "s_cmp_eq_u32 s72, 0x500\n"
    "s_cselect_b32 s72, 0, s72\n"
    "s_mul_i32 s87, s86, 0x23000\n"
    "s_lshl_b32 s93, s86, 2\n"
    "s_lshl_b32 s92, s86, 9\n"
    "s_add_u32 s92, s92, 0x230000\n"
    "s_add_u32 s94, s93, 0x230000\n"
    "s_add_u32 s93, s93, 0x232000\n"
    "s_lshl_b32 s74, s33, 2\n"
    "s_lshl_b32 s74, 15, s74\n"
    "s_mov_b32 s75, 0\n"
    "s_mov_b32 s96, 0x40000\n"
    "s_cmp_eq_u32 s32, 0\n"
    "s_cbranch_scc0 .Lfz_B0\n"
    "s_cmp_eq_u32 s33, 0\n"
    "s_cselect_b32 s42, s92, 2.0\n"
    "s_cselect_b32 s98, s93, 2.0\n"
    "v_lshrrev_b32_e32 v202, 2, v241\n"
    "v_mov_b32_e64 v201, 0\n"
    "buffer_load_dword v200, off, s[28:31], s98 sc1\n"
    "buffer_store_dword v201, v202, s[28:31], s42 offen sc1\n"
    "s_nop 1\n"
    "s_mov_b32 s42, 0x120000\n"
    "s_mov_b32 s43, 0x100\n"
    "s_mov_b32 exec_lo, 0x3ffff\n"
    "s_mov_b32 exec_hi, 0x0\n"
    "buffer_load_dword v236, v184, s[8:11], s42 offen\n"
    "s_mov_b32 exec_lo, 0x3fff\n"
    "s_mov_b32 exec_hi, 0x0\n"
    "buffer_load_dword v236, v185, s[12:15], s43 offen\n"
    "s_mov_b64 exec, -1\n"
    "s_add_u32 s42, s42, 0x240000\n"
    "s_add_u32 s43, s43, 0x100\n"
    "s_cmp_ge_u32 s43, 0x500\n"
    "s_cselect_b32 s43, 2.0, s43\n"
    "s_waitcnt lgkmcnt(0)\n"
    "s_barrier\n"
    "ds_read_b128 v[96:99], v180\n"
    "s_mov_b64 exec, s[84:85]\n"
    "ds_read_b128 v[100:103], v181\n"
    "ds_read_b128 v[108:111], v183\n"
    "s_mov_b64 exec, -1\n"
    "ds_read_b128 v[104:107], v182\n"
    ".Lfz_QA:\n"
    "s_waitcnt lgkmcnt(0)\n"
    "s_barrier\n"
    "ds_read_b64_tr_b16 v[128:129], v176\n"
    "ds_read_b64_tr_b16 v[130:131], v176 offset:1536\n"
    "v_mfma_f32_16x16x32_bf16 v[64:67], v[160:163], v[120:123], v[64:67]\n"
    "ds_read_b64_tr_b16 v[132:133], v177\n"
    "ds_read_b64_tr_b16 v[134:135], v177 offset:1536\n"
    "v_mfma_f32_16x16x32_bf16 v[68:71], v[160:163], v[124:127], v[68:71]\n"
    "ds_read_b64_tr_b16 v[136:137], v178\n"
    "ds_read_b64_tr_b16 v[138:139], v178 offset:1536\n"
    "v_mfma_f32_16x16x32_bf16 v[72:75], v[164:167], v[120:123], v[72:75]\n"
    "ds_read_b64_tr_b16 v[140:141], v179\n"
    "ds_read_b64_tr_b16 v[142:143], v179 offset:1536\n"
    "v_mfma_f32_16x16x32_bf16 v[76:79], v[164:167], v[124:127], v[76:79]\n"
    "ds_read_b64_tr_b16 v[144:145], v176 offset:128\n"
    "ds_read_b64_tr_b16 v[146:147], v176 offset:1664\n"
    "v_mfma_f32_16x16x32_bf16 v[80:83], v[168:171], v[120:123], v[80:83]\n"
    "ds_read_b64_tr_b16 v[148:149], v177 offset:128\n"
    "ds_read_b64_tr_b16 v[150:151], v177 offset:1664\n"
    "v_mfma_f32_16x16x32_bf16 v[84:87], v[168:171], v[124:127], v[84:87]\n"
    "ds_read_b64_tr_b16 v[152:153], v178 offset:128\n"
    "ds_read_b64_tr_b16 v[154:155], v178 offset:1664\n"
    "v_mfma_f32_16x16x32_bf16 v[88:91], v[172:175], v[120:123], v[88:91]\n"
    "ds_read_b64_tr_b16 v[156:157], v179 offset:128\n"
    "ds_read_b64_tr_b16 v[158:159], v179 offset:1664\n"
    "v_mfma_f32_16x16x32_bf16 v[92:95], v[172:175], v[124:127], v[92:95]\n"
    "ds_read_b64_tr_b16 v[160:161], v176 offset:256\n"
    "s_waitcnt lgkmcnt(7)\n"
    "v_mfma_f32_16x16x32_bf16 v[0:3], v[128:131], v[96:99], v[0:3]\n"
    "ds_read_b64_tr_b16 v[162:163], v176 offset:1792\n"
    "ds_read_b64_tr_b16 v[164:165], v177 offset:256\n"
    "v_mfma_f32_16x16x32_bf16 v[4:7], v[128:131], v[100:103], v[4:7]\n"
    "ds_read_b64_tr_b16 v[166:167], v177 offset:1792\n"
    "ds_read_b64_tr_b16 v[168:169], v178 offset:256\n"
    "v_mfma_f32_16x16x32_bf16 v[8:11], v[132:135], v[96:99], v[8:11]\n"
    "ds_read_b64_tr_b16 v[170:171], v178 offset:1792\n"
    "ds_read_b64_tr_b16 v[172:173], v179 offset:256\n"
    "v_mfma_f32_16x16x32_bf16 v[12:15], v[132:135], v[100:103], v[12:15]\n"
    "ds_read_b64_tr_b16 v[174:175], v179 offset:1792\n"
    "v_mfma_f32_16x16x32_bf16 v[16:19], v[136:139], v[96:99], v[16:19]\n"
    "v_mfma_f32_16x16x32_bf16 v[20:23], v[136:139], v[100:103], v[20:23]\n"
    "v_mfma_f32_16x16x32_bf16 v[24:27], v[140:143], v[96:99], v[24:27]\n"
    "v_mfma_f32_16x16x32_bf16 v[28:31], v[140:143], v[100:103], v[28:31]\n"
    "v_mfma_f32_16x16x32_bf16 v[32:35], v[144:147], v[96:99], v[32:35]\n"
    "v_mfma_f32_16x16x32_bf16 v[36:39], v[144:147], v[100:103], v[36:39]\n"
    "s_waitcnt lgkmcnt(0)\n"
    "v_mfma_f32_16x16x32_bf16 v[40:43], v[148:151], v[96:99], v[40:43]\n"
    "v_mfma_f32_16x16x32_bf16 v[44:47], v[148:151], v[100:103], v[44:47]\n"
    "v_mfma_f32_16x16x32_bf16 v[48:51], v[152:155], v[96:99], v[48:51]\n"
    "v_mfma_f32_16x16x32_bf16 v[52:55], v[152:155], v[100:103], v[52:55]\n"
    "v_mfma_f32_16x16x32_bf16 v[56:59], v[156:159], v[96:99], v[56:59]\n"
    "v_mfma_f32_16x16x32_bf16 v[60:63], v[156:159], v[100:103], v[60:63]\n"
    "s_waitcnt lgkmcnt(0)\n"
    "s_barrier\n"
    "ds_read_b128 v[112:115], v180 offset:14336\n"
    "ds_read_b64_tr_b16 v[130:131], v176 offset:13824\n"
    "v_mfma_f32_16x16x32_bf16 v[64:67], v[160:163], v[96:99], v[64:67]\n"
    "s_mov_b64 exec, s[84:85]\n"
    "ds_read_b128 v[116:119], v181 offset:14336\n"
    "ds_read_b128 v[124:127], v183 offset:14336\n"
    "s_mov_b64 exec, -1\n"
    "v_mfma_f32_16x16x32_bf16 v[68:71], v[160:163], v[100:103], v[68:71]\n"
    "ds_read_b128 v[120:123], v182 offset:14336\n"
    "ds_read_b64_tr_b16 v[128:129], v176 offset:12288\n"
    "v_mfma_f32_16x16x32_bf16 v[72:75], v[164:167], v[96:99], v[72:75]\n"
    "s_mov_b32 exec_lo, 0xfff\n"
    "s_mov_b32 exec_hi, 0x0\n"
    "buffer_load_dword v236, v184, s[8:11], s42 offen\n"
    "s_mov_b32 exec_lo, 0x3fff\n"
    "s_mov_b32 exec_hi, 0x0\n"
    "buffer_load_dword v236, v185, s[12:15], s43 offen\n"
    "s_mov_b64 exec, -1\n"
    "v_mfma_f32_16x16x32_bf16 v[76:79], v[164:167], v[100:103], v[76:79]\n"
    "s_add_u32 s42, s42, 0x180000\n"
    "s_add_u32 s43, s43, 0x100\n"
    "v_mfma_f32_16x16x32_bf16 v[80:83], v[168:171], v[96:99], v[80:83]\n"
    "s_cmp_ge_u32 s43, 0x500\n"
    "s_cselect_b32 s43, 2.0, s43\n"
    "v_mfma_f32_16x16x32_bf16 v[84:87], v[168:171], v[100:103], v[84:87]\n"
    "ds_read_b64_tr_b16 v[132:133], v177 offset:12288\n"
    "ds_read_b64_tr_b16 v[134:135], v177 offset:13824\n"
    "v_mfma_f32_16x16x32_bf16 v[88:91], v[172:175], v[96:99], v[88:91]\n"
    "ds_read_b64_tr_b16 v[136:137], v178 offset:12288\n"
    "ds_read_b64_tr_b16 v[138:139], v178 offset:13824\n"
    "v_mfma_f32_16x16x32_bf16 v[92:95], v[172:175], v[100:103], v[92:95]\n"
    "ds_read_b64_tr_b16 v[140:141], v179 offset:12288\n"
    "s_waitcnt lgkmcnt(5)\n"
    "v_mfma_f32_16x16x32_bf16 v[0:3], v[128:131], v[104:107], v[0:3]\n"
    "ds_read_b64_tr_b16 v[142:143], v179 offset:13824\n"
    "ds_read_b64_tr_b16 v[144:145], v176 offset:12416\n"
    "v_mfma_f32_16x16x32_bf16 v[4:7], v[128:131], v[108:111], v[4:7]\n"
    "ds_read_b64_tr_b16 v[146:147], v176 offset:13952\n"
    "s_waitcnt lgkmcnt(6)\n"
    "v_mfma_f32_16x16x32_bf16 v[8:11], v[132:135], v[104:107], v[8:11]\n"
    "ds_read_b64_tr_b16 v[148:149], v177 offset:12416\n"
    "ds_read_b64_tr_b16 v[150:151], v177 offset:13952\n"
    "v_mfma_f32_16x16x32_bf16 v[12:15], v[132:135], v[108:111], v[12:15]\n"
    "ds_read_b64_tr_b16 v[152:153], v178 offset:12416\n"
    "s_waitcnt lgkmcnt(6)\n"
    "v_mfma_f32_16x16x32_bf16 v[16:19], v[136:139], v[104:107], v[16:19]\n"
    "ds_read_b64_tr_b16 v[154:155], v178 offset:13952\n"
    "ds_read_b64_tr_b16 v[156:157], v179 offset:12416\n"
    "v_mfma_f32_16x16x32_bf16 v[20:23], v[136:139], v[108:111], v[20:23]\n"
    "ds_read_b64_tr_b16 v[158:159], v179 offset:13952\n"
    "s_waitcnt lgkmcnt(6)\n"
    "v_mfma_f32_16x16x32_bf16 v[24:27], v[140:143], v[104:107], v[24:27]\n"
    "ds_read_b64_tr_b16 v[160:161], v176 offset:12544\n"
    "ds_read_b64_tr_b16 v[162:163], v176 offset:14080\n"
    "v_mfma_f32_16x16x32_bf16 v[28:31], v[140:143], v[108:111], v[28:31]\n"
    "ds_read_b64_tr_b16 v[164:165], v177 offset:12544\n"
    "ds_read_b64_tr_b16 v[166:167], v177 offset:14080\n"
    "v_mfma_f32_16x16x32_bf16 v[32:35], v[144:147], v[104:107], v[32:35]\n"
    "ds_read_b64_tr_b16 v[168:169], v178 offset:12544\n"
    "ds_read_b64_tr_b16 v[170:171], v178 offset:14080\n"
    "v_mfma_f32_16x16x32_bf16 v[36:39], v[144:147], v[108:111], v[36:39]\n"
    "ds_read_b64_tr_b16 v[172:173], v179 offset:12544\n"
    "s_waitcnt lgkmcnt(7)\n"
    "v_mfma_f32_16x16x32_bf16 v[40:43], v[148:151], v[104:107], v[40:43]\n"
    "ds_read_b64_tr_b16 v[174:175], v179 offset:14080\n"
    "v_mfma_f32_16x16x32_bf16 v[44:47], v[148:151], v[108:111], v[44:47]\n"
    "v_mfma_f32_16x16x32_bf16 v[48:51], v[152:155], v[104:107], v[48:51]\n"
    "v_mfma_f32_16x16x32_bf16 v[52:55], v[152:155], v[108:111], v[52:55]\n"
    "v_mfma_f32_16x16x32_bf16 v[56:59], v[156:159], v[104:107], v[56:59]\n"
    "v_mfma_f32_16x16x32_bf16 v[60:63], v[156:159], v[108:111], v[60:63]\n"
    "s_waitcnt lgkmcnt(0)\n"
    "s_barrier\n"
    "ds_read_b64_tr_b16 v[128:129], v176 offset:24576\n"
    "ds_read_b64_tr_b16 v[130:131], v176 offset:26112\n"
    "v_mfma_f32_16x16x32_bf16 v[64:67], v[160:163], v[104:107], v[64:67]\n"
    "ds_read_b64_tr_b16 v[132:133], v177 offset:24576\n"
    "ds_read_b64_tr_b16 v[134:135], v177 offset:26112\n"
    "v_mfma_f32_16x16x32_bf16 v[68:71], v[160:163], v[108:111], v[68:71]\n"
    "ds_read_b64_tr_b16 v[136:137], v178 offset:24576\n"
    "ds_read_b64_tr_b16 v[138:139], v178 offset:26112\n"
    "v_mfma_f32_16x16x32_bf16 v[72:75], v[164:167], v[104:107], v[72:75]\n"
    "ds_read_b64_tr_b16 v[140:141], v179 offset:24576\n"
    "ds_read_b64_tr_b16 v[142:143], v179 offset:26112\n"
    "v_mfma_f32_16x16x32_bf16 v[76:79], v[164:167], v[108:111], v[76:79]\n"
    "ds_read_b64_tr_b16 v[144:145], v176 offset:24704\n"
    "ds_read_b64_tr_b16 v[146:147], v176 offset:26240\n"
    "v_mfma_f32_16x16x32_bf16 v[80:83], v[168:171], v[104:107], v[80:83]\n"
    "ds_read_b64_tr_b16 v[148:149], v177 offset:24704\n"
    "ds_read_b64_tr_b16 v[150:151], v177 offset:26240\n"
    "v_mfma_f32_16x16x32_bf16 v[84:87], v[168:171], v[108:111], v[84:87]\n"
    "ds_read_b64_tr_b16 v[152:153], v178 offset:24704\n"
    "ds_read_b64_tr_b16 v[154:155], v178 offset:26240\n"
    "v_mfma_f32_16x16x32_bf16 v[88:91], v[172:175], v[104:107], v[88:91]\n"
    "ds_read_b64_tr_b16 v[156:157], v179 offset:24704\n"
    "ds_read_b64_tr_b16 v[158:159], v179 offset:26240\n"
    "v_mfma_f32_16x16x32_bf16 v[92:95], v[172:175], v[108:111], v[92:95]\n"
    "ds_read_b64_tr_b16 v[160:161], v176 offset:24832\n"
    "s_waitcnt lgkmcnt(7)\n"
    "v_mfma_f32_16x16x32_bf16 v[0:3], v[128:131], v[112:115], v[0:3]\n"
    "ds_read_b64_tr_b16 v[162:163], v176 offset:26368\n"
    "ds_read_b64_tr_b16 v[164:165], v177 offset:24832\n"
    "v_mfma_f32_16x16x32_bf16 v[4:7], v[128:131], v[116:119], v[4:7]\n"
    "ds_read_b64_tr_b16 v[166:167], v177 offset:26368\n"
    "ds_read_b64_tr_b16 v[168:169], v178 offset:24832\n"
    "v_mfma_f32_16x16x32_bf16 v[8:11], v[132:135], v[112:115], v[8:11]\n"
    "ds_read_b64_tr_b16 v[170:171], v178 offset:26368\n"
    "ds_read_b64_tr_b16 v[172:173], v179 offset:24832\n"
    "v_mfma_f32_16x16x32_bf16 v[12:15], v[132:135], v[116:119], v[12:15]\n"
    "ds_read_b64_tr_b16 v[174:175], v179 offset:26368\n"
    "v_mfma_f32_16x16x32_bf16 v[16:19], v[136:139], v[112:115], v[16:19]\n"
    "v_mfma_f32_16x16x32_bf16 v[20:23], v[136:139], v[116:119], v[20:23]\n"
    "v_mfma_f32_16x16x32_bf16 v[24:27], v[140:143], v[112:115], v[24:27]\n"
    "v_mfma_f32_16x16x32_bf16 v[28:31], v[140:143], v[116:119], v[28:31]\n"
    "v_mfma_f32_16x16x32_bf16 v[32:35], v[144:147], v[112:115], v[32:35]\n"
    "v_mfma_f32_16x16x32_bf16 v[36:39], v[144:147], v[116:119], v[36:39]\n"
    "s_waitcnt lgkmcnt(0)\n"
    "v_mfma_f32_16x16x32_bf16 v[40:43], v[148:151], v[112:115], v[40:43]\n"
    "v_mfma_f32_16x16x32_bf16 v[44:47], v[148:151], v[116:119], v[44:47]\n"
    "v_mfma_f32_16x16x32_bf16 v[48:51], v[152:155], v[112:115], v[48:51]\n"
    "v_mfma_f32_16x16x32_bf16 v[52:55], v[152:155], v[116:119], v[52:55]\n"
    "v_mfma_f32_16x16x32_bf16 v[56:59], v[156:159], v[112:115], v[56:59]\n"
    "v_mfma_f32_16x16x32_bf16 v[60:63], v[156:159], v[116:119], v[60:63]\n"
    "s_waitcnt lgkmcnt(0)\n"
    "s_barrier\n"
    "ds_read_b128 v[96:99], v180\n"
    "ds_read_b64_tr_b16 v[128:129], v176 offset:36864\n"
    "v_mfma_f32_16x16x32_bf16 v[64:67], v[160:163], v[112:115], v[64:67]\n"
    "s_mov_b64 exec, s[84:85]\n"
    "ds_read_b128 v[100:103], v181\n"
    "ds_read_b128 v[108:111], v183\n"
    "s_mov_b64 exec, -1\n"
    "v_mfma_f32_16x16x32_bf16 v[68:71], v[160:163], v[116:119], v[68:71]\n"
    "ds_read_b128 v[104:107], v182\n"
    "s_sub_u32 s41, s41, 1\n"
    "v_mfma_f32_16x16x32_bf16 v[72:75], v[164:167], v[112:115], v[72:75]\n"
    "s_cmp_lg_u32 s41, 0\n"
    "ds_read_b64_tr_b16 v[130:131], v176 offset:38400\n"
    "v_mfma_f32_16x16x32_bf16 v[76:79], v[164:167], v[116:119], v[76:79]\n"
    "ds_read_b64_tr_b16 v[132:133], v177 offset:36864\n"
    "ds_read_b64_tr_b16 v[134:135], v177 offset:38400\n"
    "v_mfma_f32_16x16x32_bf16 v[80:83], v[168:171], v[112:115], v[80:83]\n"
    "ds_read_b64_tr_b16 v[136:137], v178 offset:36864\n"
    "ds_read_b64_tr_b16 v[138:139], v178 offset:38400\n"
    "v_mfma_f32_16x16x32_bf16 v[84:87], v[168:171], v[116:119], v[84:87]\n"
    "ds_read_b64_tr_b16 v[140:141], v179 offset:36864\n"
    "ds_read_b64_tr_b16 v[142:143], v179 offset:38400\n"
    "v_mfma_f32_16x16x32_bf16 v[88:91], v[172:175], v[112:115], v[88:91]\n"
    "ds_read_b64_tr_b16 v[144:145], v176 offset:36992\n"
    "ds_read_b64_tr_b16 v[146:147], v176 offset:38528\n"
    "v_mfma_f32_16x16x32_bf16 v[92:95], v[172:175], v[116:119], v[92:95]\n"
    "ds_read_b64_tr_b16 v[148:149], v177 offset:36992\n"
    "s_waitcnt lgkmcnt(7)\n"
    "v_mfma_f32_16x16x32_bf16 v[0:3], v[128:131], v[120:123], v[0:3]\n"
    "ds_read_b64_tr_b16 v[150:151], v177 offset:38528\n"
    "ds_read_b64_tr_b16 v[152:153], v178 offset:36992\n"
    "v_mfma_f32_16x16x32_bf16 v[4:7], v[128:131], v[124:127], v[4:7]\n"
    "ds_read_b64_tr_b16 v[154:155], v178 offset:38528\n"
    "ds_read_b64_tr_b16 v[156:157], v179 offset:36992\n"
    "v_mfma_f32_16x16x32_bf16 v[8:11], v[132:135], v[120:123], v[8:11]\n"
    "ds_read_b64_tr_b16 v[158:159], v179 offset:38528\n"
    "ds_read_b64_tr_b16 v[160:161], v176 offset:37120\n"
    "v_mfma_f32_16x16x32_bf16 v[12:15], v[132:135], v[124:127], v[12:15]\n"
    "ds_read_b64_tr_b16 v[162:163], v176 offset:38656\n"
    "s_waitcnt lgkmcnt(7)\n"
    "v_mfma_f32_16x16x32_bf16 v[16:19], v[136:139], v[120:123], v[16:19]\n"
    "ds_read_b64_tr_b16 v[164:165], v177 offset:37120\n"
    "ds_read_b64_tr_b16 v[166:167], v177 offset:38656\n"
    "v_mfma_f32_16x16x32_bf16 v[20:23], v[136:139], v[124:127], v[20:23]\n"
    "ds_read_b64_tr_b16 v[168:169], v178 offset:37120\n"
    "ds_read_b64_tr_b16 v[170:171], v178 offset:38656\n"
    "v_mfma_f32_16x16x32_bf16 v[24:27], v[140:143], v[120:123], v[24:27]\n"
    "ds_read_b64_tr_b16 v[172:173], v179 offset:37120\n"
    "ds_read_b64_tr_b16 v[174:175], v179 offset:38656\n"
    "v_mfma_f32_16x16x32_bf16 v[28:31], v[140:143], v[124:127], v[28:31]\n"
    "v_mfma_f32_16x16x32_bf16 v[32:35], v[144:147], v[120:123], v[32:35]\n"
    "v_mfma_f32_16x16x32_bf16 v[36:39], v[144:147], v[124:127], v[36:39]\n"
    "s_waitcnt lgkmcnt(4)\n"
    "v_mfma_f32_16x16x32_bf16 v[40:43], v[148:151], v[120:123], v[40:43]\n"
    "v_mfma_f32_16x16x32_bf16 v[44:47], v[148:151], v[124:127], v[44:47]\n"
    "v_mfma_f32_16x16x32_bf16 v[48:51], v[152:155], v[120:123], v[48:51]\n"
    "v_mfma_f32_16x16x32_bf16 v[52:55], v[152:155], v[124:127], v[52:55]\n"
    "v_mfma_f32_16x16x32_bf16 v[56:59], v[156:159], v[120:123], v[56:59]\n"
    "v_mfma_f32_16x16x32_bf16 v[60:63], v[156:159], v[124:127], v[60:63]\n"
    "s_cbranch_scc1 .Lfz_QA\n"
    "s_waitcnt vmcnt(12)\n"
    "v_or_b32_e32 v200, 0xff, v200\n"
    "v_add_u32_e32 v200, 1, v200\n"
    "buffer_store_dword v200, off, s[28:31], s98 sc1\n"
    "v_mfma_f32_16x16x32_bf16 v[64:67], v[160:163], v[120:123], v[64:67]\n"
    "v_pk_mul_f32 v[0:1], v[0:1], s[48:49]\n"
    "v_pk_mul_f32 v[2:3], v[2:3], s[48:49]\n"
    "v_mfma_f32_16x16x32_bf16 v[68:71], v[160:163], v[124:127], v[68:71]\n"
    "v_pk_mul_f32 v[8:9], v[8:9], s[48:49]\n"
    "v_pk_mul_f32 v[10:11], v[10:11], s[48:49]\n"
    "v_mfma_f32_16x16x32_bf16 v[72:75], v[164:167], v[120:123], v[72:75]\n"
    "v_cvt_pk_bf16_f32 v0, v0, v1\n"
    "v_cvt_pk_bf16_f32 v2, v2, v3\n"
    "v_mfma_f32_16x16x32_bf16 v[76:79], v[164:167], v[124:127], v[76:79]\n"
    "v_cvt_pk_bf16_f32 v8, v8, v9\n"
    "s_waitcnt lgkmcnt(0)\n"
    "v_mfma_f32_16x16x32_bf16 v[80:83], v[168:171], v[120:123], v[80:83]\n"
    "v_cvt_pk_bf16_f32 v10, v10, v11\n"
    "v_cndmask_b32_e64 v96, v0, v8, s[50:51]\n"
    "v_mfma_f32_16x16x32_bf16 v[84:87], v[168:171], v[124:127], v[84:87]\n"
    "v_cndmask_b32_e64 v97, v2, v10, s[50:51]\n"
    "v_cndmask_b32_e64 v98, v8, v0, s[50:51]\n"
    "v_mfma_f32_16x16x32_bf16 v[88:91], v[172:175], v[120:123], v[88:91]\n"
    "v_cndmask_b32_e64 v99, v10, v2, s[50:51]\n"
    "v_pk_mul_f32 v[4:5], v[4:5], s[80:81]\n"
    "v_mfma_f32_16x16x32_bf16 v[92:95], v[172:175], v[124:127], v[92:95]\n"
    "v_pk_mul_f32 v[6:7], v[6:7], s[80:81]\n"
    "v_pk_mul_f32 v[12:13], v[12:13], s[80:81]\n"
    "v_pk_mul_f32 v[14:15], v[14:15], s[80:81]\n"
    "v_cvt_pk_bf16_f32 v4, v4, v5\n"
    "v_cvt_pk_bf16_f32 v6, v6, v7\n"
    "v_cvt_pk_bf16_f32 v12, v12, v13\n"
    "v_cvt_pk_bf16_f32 v14, v14, v15\n"
    "v_cndmask_b32_e64 v100, v4, v12, s[50:51]\n"
    "v_cndmask_b32_e64 v101, v6, v14, s[50:51]\n"
    "v_cndmask_b32_e64 v102, v12, v4, s[50:51]\n"
    "v_cndmask_b32_e64 v103, v14, v6, s[50:51]\n"
    "v_pk_mul_f32 v[16:17], v[16:17], s[48:49]\n"
    "v_pk_mul_f32 v[18:19], v[18:19], s[48:49]\n"
    "v_pk_mul_f32 v[24:25], v[24:25], s[48:49]\n"
    "v_pk_mul_f32 v[26:27], v[26:27], s[48:49]\n"
    "v_cvt_pk_bf16_f32 v16, v16, v17\n"
    "v_cvt_pk_bf16_f32 v18, v18, v19\n"
    "v_cvt_pk_bf16_f32 v24, v24, v25\n"
    "v_cvt_pk_bf16_f32 v26, v26, v27\n"
    "v_cndmask_b32_e64 v104, v16, v24, s[50:51]\n"
    "v_cndmask_b32_e64 v105, v18, v26, s[50:51]\n"
    "v_cndmask_b32_e64 v106, v24, v16, s[50:51]\n"
    "v_cndmask_b32_e64 v107, v26, v18, s[50:51]\n"
    "v_pk_mul_f32 v[20:21], v[20:21], s[80:81]\n"
    "v_pk_mul_f32 v[22:23], v[22:23], s[80:81]\n"
    "v_pk_mul_f32 v[28:29], v[28:29], s[80:81]\n"
    "v_pk_mul_f32 v[30:31], v[30:31], s[80:81]\n"
    "v_cvt_pk_bf16_f32 v20, v20, v21\n"
    "v_cvt_pk_bf16_f32 v22, v22, v23\n"
    "v_cvt_pk_bf16_f32 v28, v28, v29\n"
    "v_cvt_pk_bf16_f32 v30, v30, v31\n"
    "v_cndmask_b32_e64 v108, v20, v28, s[50:51]\n"
    "v_cndmask_b32_e64 v109, v22, v30, s[50:51]\n"
    "v_cndmask_b32_e64 v110, v28, v20, s[50:51]\n"
    "v_cndmask_b32_e64 v111, v30, v22, s[50:51]\n"
    "v_pk_mul_f32 v[32:33], v[32:33], s[48:49]\n"
    "v_pk_mul_f32 v[34:35], v[34:35], s[48:49]\n"
    "v_pk_mul_f32 v[40:41], v[40:41], s[48:49]\n"
    "v_pk_mul_f32 v[42:43], v[42:43], s[48:49]\n"
    "v_cvt_pk_bf16_f32 v32, v32, v33\n"
    "v_cvt_pk_bf16_f32 v34, v34, v35\n"
    "v_cvt_pk_bf16_f32 v40, v40, v41\n"
    "v_cvt_pk_bf16_f32 v42, v42, v43\n"
    "v_cndmask_b32_e64 v112, v32, v40, s[50:51]\n"
    "v_cndmask_b32_e64 v113, v34, v42, s[50:51]\n"
    "v_cndmask_b32_e64 v114, v40, v32, s[50:51]\n"
    "v_cndmask_b32_e64 v115, v42, v34, s[50:51]\n"
    "v_pk_mul_f32 v[36:37], v[36:37], s[80:81]\n"
    "v_pk_mul_f32 v[38:39], v[38:39], s[80:81]\n"
    "v_pk_mul_f32 v[44:45], v[44:45], s[80:81]\n"
    "v_pk_mul_f32 v[46:47], v[46:47], s[80:81]\n"
    "v_cvt_pk_bf16_f32 v36, v36, v37\n"
    "v_cvt_pk_bf16_f32 v38, v38, v39\n"
    "v_cvt_pk_bf16_f32 v44, v44, v45\n"
    "v_cvt_pk_bf16_f32 v46, v46, v47\n"
    "v_cndmask_b32_e64 v116, v36, v44, s[50:51]\n"
    "v_cndmask_b32_e64 v117, v38, v46, s[50:51]\n"
    "v_cndmask_b32_e64 v118, v44, v36, s[50:51]\n"
    "v_cndmask_b32_e64 v119, v46, v38, s[50:51]\n"
    "v_pk_mul_f32 v[48:49], v[48:49], s[48:49]\n"
    "v_pk_mul_f32 v[50:51], v[50:51], s[48:49]\n"
    "v_pk_mul_f32 v[56:57], v[56:57], s[48:49]\n"
    "v_pk_mul_f32 v[58:59], v[58:59], s[48:49]\n"
    "v_cvt_pk_bf16_f32 v48, v48, v49\n"
    "v_cvt_pk_bf16_f32 v50, v50, v51\n"
    "v_cvt_pk_bf16_f32 v56, v56, v57\n"
    "v_cvt_pk_bf16_f32 v58, v58, v59\n"
    "v_cndmask_b32_e64 v120, v48, v56, s[50:51]\n"
    "v_cndmask_b32_e64 v121, v50, v58, s[50:51]\n"
    "v_cndmask_b32_e64 v122, v56, v48, s[50:51]\n"
    "v_cndmask_b32_e64 v123, v58, v50, s[50:51]\n"
    "v_pk_mul_f32 v[52:53], v[52:53], s[80:81]\n"
    "v_pk_mul_f32 v[54:55], v[54:55], s[80:81]\n"
    "v_pk_mul_f32 v[60:61], v[60:61], s[80:81]\n"
    "v_pk_mul_f32 v[62:63], v[62:63], s[80:81]\n"
    "v_cvt_pk_bf16_f32 v52, v52, v53\n"
    "v_cvt_pk_bf16_f32 v54, v54, v55\n"
    "v_cvt_pk_bf16_f32 v60, v60, v61\n"
    "v_cvt_pk_bf16_f32 v62, v62, v63\n"
    "v_cndmask_b32_e64 v124, v52, v60, s[50:51]\n"
    "v_cndmask_b32_e64 v125, v54, v62, s[50:51]\n"
    "v_cndmask_b32_e64 v126, v60, v52, s[50:51]\n"
    "v_cndmask_b32_e64 v127, v62, v54, s[50:51]\n"
    "v_pk_mul_f32 v[64:65], v[64:65], s[48:49]\n"
    "v_pk_mul_f32 v[66:67], v[66:67], s[48:49]\n"
    "v_pk_mul_f32 v[72:73], v[72:73], s[48:49]\n"
    "v_pk_mul_f32 v[74:75], v[74:75], s[48:49]\n"
    "v_cvt_pk_bf16_f32 v64, v64, v65\n"
    "v_cvt_pk_bf16_f32 v66, v66, v67\n"
    "v_cvt_pk_bf16_f32 v72, v72, v73\n"
    "v_cvt_pk_bf16_f32 v74, v74, v75\n"
    "v_cndmask_b32_e64 v128, v64, v72, s[50:51]\n"
    "v_cndmask_b32_e64 v129, v66, v74, s[50:51]\n"
    "v_cndmask_b32_e64 v130, v72, v64, s[50:51]\n"
    "v_cndmask_b32_e64 v131, v74, v66, s[50:51]\n"
    "v_pk_mul_f32 v[68:69], v[68:69], s[80:81]\n"
    "v_pk_mul_f32 v[70:71], v[70:71], s[80:81]\n"
    "v_pk_mul_f32 v[76:77], v[76:77], s[80:81]\n"
    "v_pk_mul_f32 v[78:79], v[78:79], s[80:81]\n"
    "v_cvt_pk_bf16_f32 v68, v68, v69\n"
    "v_cvt_pk_bf16_f32 v70, v70, v71\n"
    "v_cvt_pk_bf16_f32 v76, v76, v77\n"
    "v_cvt_pk_bf16_f32 v78, v78, v79\n"
    "v_cndmask_b32_e64 v132, v68, v76, s[50:51]\n"
    "v_cndmask_b32_e64 v133, v70, v78, s[50:51]\n"
    "v_cndmask_b32_e64 v134, v76, v68, s[50:51]\n"
    "v_cndmask_b32_e64 v135, v78, v70, s[50:51]\n"
    "v_pk_mul_f32 v[80:81], v[80:81], s[48:49]\n"
    "v_pk_mul_f32 v[82:83], v[82:83], s[48:49]\n"
    "v_pk_mul_f32 v[88:89], v[88:89], s[48:49]\n"
    "v_pk_mul_f32 v[90:91], v[90:91], s[48:49]\n"
    "v_cvt_pk_bf16_f32 v80, v80, v81\n"
    "v_cvt_pk_bf16_f32 v82, v82, v83\n"
    "v_cvt_pk_bf16_f32 v88, v88, v89\n"
    "v_cvt_pk_bf16_f32 v90, v90, v91\n"
    "v_cndmask_b32_e64 v136, v80, v88, s[50:51]\n"
    "v_cndmask_b32_e64 v137, v82, v90, s[50:51]\n"
    "v_cndmask_b32_e64 v138, v88, v80, s[50:51]\n"
    "v_cndmask_b32_e64 v139, v90, v82, s[50:51]\n"
    "v_pk_mul_f32 v[84:85], v[84:85], s[80:81]\n"
    "v_pk_mul_f32 v[86:87], v[86:87], s[80:81]\n"
    "v_pk_mul_f32 v[92:93], v[92:93], s[80:81]\n"
    "v_pk_mul_f32 v[94:95], v[94:95], s[80:81]\n"
    "v_cvt_pk_bf16_f32 v84, v84, v85\n"
    "v_cvt_pk_bf16_f32 v86, v86, v87\n"
    "v_cvt_pk_bf16_f32 v92, v92, v93\n"
    "v_cvt_pk_bf16_f32 v94, v94, v95\n"
    "v_cndmask_b32_e64 v140, v84, v92, s[50:51]\n"
    "v_cndmask_b32_e64 v141, v86, v94, s[50:51]\n"
    "v_cndmask_b32_e64 v142, v92, v84, s[50:51]\n"
    "v_cndmask_b32_e64 v143, v94, v86, s[50:51]\n"
    "s_mov_b32 s36, 0\n"
    "s_mov_b32 s37, 0x3000\n"
    "s_mov_b32 s38, 0x6000\n"
    "s_mov_b32 s39, 0x6000\n"
    "s_mov_b32 s40, 0\n"
    "s_branch .Lfz_ATT\n"
    ".Lfz_B0:\n"
    "v_mov_b64_e32 v[128:129], 0\n"
    "v_mov_b64_e32 v[130:131], 0\n"
    "v_mov_b64_e32 v[132:133], 0\n"
    "v_mov_b64_e32 v[134:135], 0\n"
    "v_mov_b64_e32 v[136:137], 0\n"
    "v_mov_b64_e32 v[138:139], 0\n"
    "v_mov_b64_e32 v[140:141], 0\n"
    "v_mov_b64_e32 v[142:143], 0\n"
    "v_mov_b64_e32 v[144:145], 0\n"
    "v_mov_b64_e32 v[146:147], 0\n"
    "v_mov_b64_e32 v[148:149], 0\n"
    "v_mov_b64_e32 v[150:151], 0\n"
    "v_mov_b64_e32 v[152:153], 0\n"
    "v_mov_b64_e32 v[154:155], 0\n"
    "v_mov_b64_e32 v[156:157], 0\n"
    "v_mov_b64_e32 v[158:159], 0\n"
    "v_mov_b64_e32 v[184:185], 0\n"
    "v_mov_b64_e32 v[186:187], 0\n"
    "v_mov_b64_e32 v[188:189], 0\n"
    "v_mov_b64_e32 v[190:191], 0\n"
    "v_mov_b64_e32 v[192:193], 0\n"
    "v_mov_b64_e32 v[194:195], 0\n"
    "v_mov_b64_e32 v[196:197], 0\n"
    "v_mov_b64_e32 v[198:199], 0\n"
    "v_mov_b64_e32 v[200:201], 0\n"
    "v_mov_b64_e32 v[202:203], 0\n"
    "v_mov_b64_e32 v[204:205], 0\n"
    "v_mov_b64_e32 v[206:207], 0\n"
    "s_waitcnt vmcnt(11) lgkmcnt(0)\n"
    "s_barrier\n"
    "ds_read_b128 v[96:99], v180\n"
    "s_mov_b64 exec, s[84:85]\n"
    "ds_read_b128 v[100:103], v181\n"
    "ds_read_b128 v[108:111], v183\n"
    "s_mov_b64 exec, -1\n"
    "ds_read_b128 v[104:107], v182\n"
    "s_waitcnt lgkmcnt(0)\n"
    "s_barrier\n"
    "s_add_u32 m0, s55, 0x9000\n"
    "s_nop 0\n"
    "buffer_load_dwordx4 v244, s[8:11], s56 offen lds\n"
    "buffer_load_dwordx4 v245, s[8:11], s56 offen offset:1024 lds\n"
    "buffer_load_dwordx4 v246, s[8:11], s56 offen offset:2048 lds\n"
    "s_add_u32 s56, s56, 0x60000\n"
    "s_cmp_eq_u32 s56, 0x780000\n"
    "s_cselect_b32 s56, 0x0, s56\n"
    "s_mov_b32 m0, s35\n"
    "s_add_u32 s43, s57, 0x2400\n"
    "s_add_u32 s44, s57, 0x4800\n"
    "s_add_u32 s45, s57, 0x6c00\n"
    "buffer_load_dwordx4 v247, s[12:15], s57 offen lds\n"
    "buffer_load_dwordx4 v247, s[12:15], s43 offen offset:1024 lds\n"
    "buffer_load_dwordx4 v247, s[12:15], s44 offen offset:2048 lds\n"
    "s_mov_b32 exec_hi, 0\n"
    "buffer_load_dwordx4 v247, s[12:15], s45 offen offset:3072 lds\n"
    "s_mov_b32 exec_hi, -1\n"
    "s_add_u32 s57, s57, 0x80\n"
    "s_cmp_eq_u32 s57, 0x500\n"
    "s_cselect_b32 s57, 0, s57\n"
    "s_waitcnt vmcnt(11) lgkmcnt(0)\n"
    "s_barrier\n"
    "s_mov_b32 m0, s55\n"
    "s_nop 0\n"
    "buffer_load_dwordx4 v244, s[8:11], s56 offen lds\n"
    "buffer_load_dwordx4 v245, s[8:11], s56 offen offset:1024 lds\n"
    "buffer_load_dwordx4 v246, s[8:11], s56 offen offset:2048 lds\n"
    "s_add_u32 s56, s56, 0x60000\n"
    "ds_read_b128 v[112:115], v180 offset:14336\n"
    "s_mov_b64 exec, s[84:85]\n"
    "ds_read_b128 v[116:119], v181 offset:14336\n"
    "ds_read_b128 v[124:127], v183 offset:14336\n"
    "s_mov_b64 exec, -1\n"
    "ds_read_b128 v[120:123], v182 offset:14336\n"
    "s_waitcnt vmcnt(11) lgkmcnt(0)\n"
    "s_barrier\n"
    "s_add_u32 m0, s55, 0x3000\n"
    "s_nop 0\n"
    "buffer_load_dwordx4 v244, s[8:11], s56 offen lds\n"
    "buffer_load_dwordx4 v245, s[8:11], s56 offen offset:1024 lds\n"
    "buffer_load_dwordx4 v246, s[8:11], s56 offen offset:2048 lds\n"
    "s_add_u32 s56, s56, 0x60000\n"
    "s_cmp_eq_u32 s56, 0x780000\n"
    "s_cselect_b32 s56, 0x0, s56\n"
    "s_add_u32 m0, s35, 0x3800\n"
    "s_add_u32 s43, s57, 0x2400\n"
    "s_add_u32 s44, s57, 0x4800\n"
    "s_add_u32 s45, s57, 0x6c00\n"
    "buffer_load_dwordx4 v247, s[12:15], s57 offen lds\n"
    "buffer_load_dwordx4 v247, s[12:15], s43 offen offset:1024 lds\n"
    "buffer_load_dwordx4 v247, s[12:15], s44 offen offset:2048 lds\n"
    "s_mov_b32 exec_hi, 0\n"
    "buffer_load_dwordx4 v247, s[12:15], s45 offen offset:3072 lds\n"
    "s_mov_b32 exec_hi, -1\n"
    "s_add_u32 s57, s57, 0x80\n"
    "s_cmp_eq_u32 s57, 0x500\n"
    "s_cselect_b32 s57, 0, s57\n"
    "s_waitcnt vmcnt(10) lgkmcnt(0)\n"
    "s_barrier\n"
    "s_add_u32 m0, s55, 0x6000\n"
    "s_nop 0\n"
    "buffer_load_dwordx4 v244, s[8:11], s56 offen lds\n"
    "buffer_load_dwordx4 v245, s[8:11], s56 offen offset:1024 lds\n"
    "buffer_load_dwordx4 v246, s[8:11], s56 offen offset:2048 lds\n"
    "s_add_u32 s56, s56, 0x60000\n"
    "ds_read_b128 v[128:131], v180\n"
    "s_mov_b64 exec, s[84:85]\n"
    "ds_read_b128 v[132:135], v181\n"
    "ds_read_b128 v[140:143], v183\n"
    "s_mov_b64 exec, -1\n"
    "ds_read_b128 v[136:139], v182\n"
    "s_waitcnt vmcnt(10) lgkmcnt(0)\n"
    "s_barrier\n"
    "s_add_u32 m0, s55, 0x9000\n"
    "s_nop 0\n"
    "buffer_load_dwordx4 v244, s[8:11], s56 offen lds\n"
    "buffer_load_dwordx4 v245, s[8:11], s56 offen offset:1024 lds\n"
    "buffer_load_dwordx4 v246, s[8:11], s56 offen offset:2048 lds\n"
    "s_add_u32 s56, s56, 0x60000\n"
    "s_cmp_eq_u32 s56, 0x780000\n"
    "s_cselect_b32 s56, 0x0, s56\n"
    "s_mov_b32 m0, s35\n"
    "s_add_u32 s43, s57, 0x2400\n"
    "s_add_u32 s44, s57, 0x4800\n"
    "s_add_u32 s45, s57, 0x6c00\n"
    "buffer_load_dwordx4 v247, s[12:15], s57 offen lds\n"
    "buffer_load_dwordx4 v247, s[12:15], s43 offen offset:1024 lds\n"
    "buffer_load_dwordx4 v247, s[12:15], s44 offen offset:2048 lds\n"
    "s_mov_b32 exec_hi, 0\n"
    "buffer_load_dwordx4 v247, s[12:15], s45 offen offset:3072 lds\n"
    "s_mov_b32 exec_hi, -1\n"
    "s_add_u32 s57, s57, 0x80\n"
    "s_cmp_eq_u32 s57, 0x500\n"
    "s_cselect_b32 s57, 0, s57\n"
    "s_waitcnt vmcnt(10) lgkmcnt(0)\n"
    "s_barrier\n"
    "s_mov_b32 m0, s55\n"
    "s_nop 0\n"
    "buffer_load_dwordx4 v244, s[8:11], s56 offen lds\n"
    "buffer_load_dwordx4 v245, s[8:11], s56 offen offset:1024 lds\n"
    "buffer_load_dwordx4 v246, s[8:11], s56 offen offset:2048 lds\n"
    "s_add_u32 s56, s56, 0x60000\n"
    "ds_read_b128 v[144:147], v180 offset:14336\n"
    "s_mov_b64 exec, s[84:85]\n"
    "ds_read_b128 v[148:151], v181 offset:14336\n"
    "ds_read_b128 v[156:159], v183 offset:14336\n"
    "s_mov_b64 exec, -1\n"
    "ds_read_b128 v[152:155], v182 offset:14336\n"
    "s_waitcnt vmcnt(10) lgkmcnt(0)\n"
    "s_barrier\n"
    "s_add_u32 m0, s55, 0x3000\n"
    "s_nop 0\n"
    "buffer_load_dwordx4 v244, s[8:11], s56 offen lds\n"
    "buffer_load_dwordx4 v245, s[8:11], s56 offen offset:1024 lds\n"
    "buffer_load_dwordx4 v246, s[8:11], s56 offen offset:2048 lds\n"
    "s_add_u32 s56, s56, 0x60000\n"
    "s_cmp_eq_u32 s56, 0x780000\n"
    "s_cselect_b32 s56, 0x0, s56\n"
    "s_add_u32 m0, s35, 0x3800\n"
    "s_add_u32 s43, s57, 0x2400\n"
    "s_add_u32 s44, s57, 0x4800\n"
    "s_add_u32 s45, s57, 0x6c00\n"
    "buffer_load_dwordx4 v247, s[12:15], s57 offen lds\n"
    "buffer_load_dwordx4 v247, s[12:15], s43 offen offset:1024 lds\n"
    "buffer_load_dwordx4 v247, s[12:15], s44 offen offset:2048 lds\n"
    "s_mov_b32 exec_hi, 0\n"
    "buffer_load_dwordx4 v247, s[12:15], s45 offen offset:3072 lds\n"
    "s_mov_b32 exec_hi, -1\n"
    "s_add_u32 s57, s57, 0x80\n"
    "s_cmp_eq_u32 s57, 0x500\n"
    "s_cselect_b32 s57, 0, s57\n"
    "s_waitcnt vmcnt(10) lgkmcnt(0)\n"
    "s_barrier\n"
    "s_add_u32 m0, s55, 0x6000\n"
    "s_nop 0\n"
    "buffer_load_dwordx4 v244, s[8:11], s56 offen lds\n"
    "buffer_load_dwordx4 v245, s[8:11], s56 offen offset:1024 lds\n"
    "buffer_load_dwordx4 v246, s[8:11], s56 offen offset:2048 lds\n"
    "s_add_u32 s56, s56, 0x60000\n"
    "ds_read_b128 v[160:163], v180\n"
    "s_mov_b64 exec, s[84:85]\n"
    "ds_read_b128 v[164:167], v181\n"
    "ds_read_b128 v[172:175], v183\n"
    "s_mov_b64 exec, -1\n"
    "ds_read_b128 v[168:171], v182\n"
    "s_waitcnt vmcnt(10) lgkmcnt(0)\n"
    "s_barrier\n"
    "s_add_u32 m0, s55, 0x9000\n"
    "s_nop 0\n"
    "buffer_load_dwordx4 v244, s[8:11], s56 offen lds\n"
    "buffer_load_dwordx4 v245, s[8:11], s56 offen offset:1024 lds\n"
    "buffer_load_dwordx4 v246, s[8:11], s56 offen offset:2048 lds\n"
    "s_add_u32 s56, s56, 0x60000\n"
    "s_cmp_eq_u32 s56, 0x780000\n"
    "s_cselect_b32 s56, 0x0, s56\n"
    "s_mov_b32 m0, s35\n"
    "s_add_u32 s43, s57, 0x2400\n"
    "s_add_u32 s44, s57, 0x4800\n"
    "s_add_u32 s45, s57, 0x6c00\n"
    "buffer_load_dwordx4 v247, s[12:15], s57 offen lds\n"
    "buffer_load_dwordx4 v247, s[12:15], s43 offen offset:1024 lds\n"
    "buffer_load_dwordx4 v247, s[12:15], s44 offen offset:2048 lds\n"
    "s_mov_b32 exec_hi, 0\n"
    "buffer_load_dwordx4 v247, s[12:15], s45 offen offset:3072 lds\n"
    "s_mov_b32 exec_hi, -1\n"
    "s_add_u32 s57, s57, 0x80\n"
    "s_cmp_eq_u32 s57, 0x500\n"
    "s_cselect_b32 s57, 0, s57\n"
    "s_waitcnt vmcnt(10) lgkmcnt(0)\n"
    "s_barrier\n"
    "s_mov_b32 m0, s55\n"
    "s_nop 0\n"
    "buffer_load_dwordx4 v244, s[8:11], s56 offen lds\n"
    "buffer_load_dwordx4 v245, s[8:11], s56 offen offset:1024 lds\n"
    "buffer_load_dwordx4 v246, s[8:11], s56 offen offset:2048 lds\n"
    "s_add_u32 s56, s56, 0x60000\n"
    "ds_read_b128 v[184:187], v180 offset:14336\n"
    "s_mov_b64 exec, s[84:85]\n"
    "ds_read_b128 v[188:191], v181 offset:14336\n"
    "ds_read_b128 v[196:199], v183 offset:14336\n"
    "s_mov_b64 exec, -1\n"
    "ds_read_b128 v[192:195], v182 offset:14336\n"
    "s_waitcnt vmcnt(10) lgkmcnt(0)\n"
    "s_barrier\n"
    "s_add_u32 m0, s55, 0x3000\n"
    "s_nop 0\n"
    "buffer_load_dwordx4 v244, s[8:11], s56 offen lds\n"
    "buffer_load_dwordx4 v245, s[8:11], s56 offen offset:1024 lds\n"
    "buffer_load_dwordx4 v246, s[8:11], s56 offen offset:2048 lds\n"
    "s_add_u32 s56, s56, 0x60000\n"
    "s_cmp_eq_u32 s56, 0x780000\n"
    "s_cselect_b32 s56, 0x0, s56\n"
    "s_add_u32 m0, s35, 0x3800\n"
    "s_add_u32 s43, s57, 0x2400\n"
    "s_add_u32 s44, s57, 0x4800\n"
    "s_add_u32 s45, s57, 0x6c00\n"
    "buffer_load_dwordx4 v247, s[12:15], s57 offen lds\n"
    "buffer_load_dwordx4 v247, s[12:15], s43 offen offset:1024 lds\n"
    "buffer_load_dwordx4 v247, s[12:15], s44 offen offset:2048 lds\n"
    "s_mov_b32 exec_hi, 0\n"
    "buffer_load_dwordx4 v247, s[12:15], s45 offen offset:3072 lds\n"
    "s_mov_b32 exec_hi, -1\n"
    "s_add_u32 s57, s57, 0x80\n"
    "s_cmp_eq_u32 s57, 0x500\n"
    "s_cselect_b32 s57, 0, s57\n"
    "s_waitcnt vmcnt(10) lgkmcnt(0)\n"
    "s_barrier\n"
    "s_add_u32 m0, s55, 0x6000\n"
    "s_nop 0\n"
    "buffer_load_dwordx4 v244, s[8:11], s56 offen lds\n"
    "buffer_load_dwordx4 v245, s[8:11], s56 offen offset:1024 lds\n"
    "buffer_load_dwordx4 v246, s[8:11], s56 offen offset:2048 lds\n"
    "s_add_u32 s56, s56, 0x60000\n"
    "s_waitcnt vmcnt(10) lgkmcnt(0)\n"
    "s_barrier\n"
    "s_add_u32 m0, s55, 0x9000\n"
    "s_nop 0\n"
    "buffer_load_dwordx4 v244, s[8:11], s56 offen lds\n"
    "buffer_load_dwordx4 v245, s[8:11], s56 offen offset:1024 lds\n"
    "buffer_load_dwordx4 v246, s[8:11], s56 offen offset:2048 lds\n"
    "s_add_u32 s56, s56, 0x60000\n"
    "s_cmp_eq_u32 s56, 0x780000\n"
    "s_cselect_b32 s56, 0x0, s56\n"
    "s_mov_b32 m0, s35\n"
    "s_add_u32 s43, s57, 0x2400\n"
    "s_add_u32 s44, s57, 0x4800\n"
    "s_add_u32 s45, s57, 0x6c00\n"
    "buffer_load_dwordx4 v247, s[12:15], s57 offen lds\n"
    "buffer_load_dwordx4 v247, s[12:15], s43 offen offset:1024 lds\n"
    "buffer_load_dwordx4 v247, s[12:15], s44 offen offset:2048 lds\n"
    "s_mov_b32 exec_hi, 0\n"
    "buffer_load_dwordx4 v247, s[12:15], s45 offen offset:3072 lds\n"
    "s_mov_b32 exec_hi, -1\n"
    "s_add_u32 s57, s57, 0x80\n"
    "s_cmp_eq_u32 s57, 0x500\n"
    "s_cselect_b32 s57, 0, s57\n"
    "s_waitcnt vmcnt(10) lgkmcnt(0)\n"
    "s_barrier\n"
    "s_mov_b32 m0, s55\n"
    "s_nop 0\n"
    "buffer_load_dwordx4 v244, s[8:11], s56 offen lds\n"
    "buffer_load_dwordx4 v245, s[8:11], s56 offen offset:1024 lds\n"
    "buffer_load_dwordx4 v246, s[8:11], s56 offen offset:2048 lds\n"
    "s_add_u32 s56, s56, 0x60000\n"
    "s_mov_b32 m0, s34\n"
    "s_mov_b32 s42, 0x0\n"
    "buffer_load_dwordx4 v248, s[16:19], s42 offen nt lds\n"
    "buffer_load_dwordx4 v249, s[16:19], s42 offen offset:1024 nt lds\n"
    "buffer_load_dwordx4 v250, s[16:19], s42 offen offset:2048 nt lds\n"
    "s_waitcnt vmcnt(13) lgkmcnt(0)\n"
    "s_barrier\n"
    "s_add_u32 m0, s55, 0x3000\n"
    "s_nop 0\n"
    "buffer_load_dwordx4 v244, s[8:11], s56 offen lds\n"
    "buffer_load_dwordx4 v245, s[8:11], s56 offen offset:1024 lds\n"
    "buffer_load_dwordx4 v246, s[8:11], s56 offen offset:2048 lds\n"
    "s_add_u32 s56, s56, 0x60000\n"
    "s_cmp_eq_u32 s56, 0x780000\n"
    "s_cselect_b32 s56, 0x0, s56\n"
    "s_add_u32 m0, s35, 0x3800\n"
    "s_add_u32 s43, s57, 0x2400\n"
    "s_add_u32 s44, s57, 0x4800\n"
    "s_add_u32 s45, s57, 0x6c00\n"
    "buffer_load_dwordx4 v247, s[12:15], s57 offen lds\n"
    "buffer_load_dwordx4 v247, s[12:15], s43 offen offset:1024 lds\n"
    "buffer_load_dwordx4 v247, s[12:15], s44 offen offset:2048 lds\n"
    "s_mov_b32 exec_hi, 0\n"
    "buffer_load_dwordx4 v247, s[12:15], s45 offen offset:3072 lds\n"
    "s_mov_b32 exec_hi, -1\n"
    "s_add_u32 s57, s57, 0x80\n"
    "s_cmp_eq_u32 s57, 0x500\n"
    "s_cselect_b32 s57, 0, s57\n"
    "s_waitcnt vmcnt(13) lgkmcnt(0)\n"
    "s_barrier\n"
    "s_add_u32 m0, s55, 0x6000\n"
    "s_nop 0\n"
    "buffer_load_dwordx4 v244, s[8:11], s56 offen lds\n"
    "buffer_load_dwordx4 v245, s[8:11], s56 offen offset:1024 lds\n"
    "buffer_load_dwordx4 v246, s[8:11], s56 offen offset:2048 lds\n"
    "s_add_u32 s56, s56, 0x60000\n"
    "s_waitcnt vmcnt(13) lgkmcnt(0)\n"
    "s_barrier\n"
    "s_add_u32 m0, s55, 0x9000\n"
    "s_nop 0\n"
    "buffer_load_dwordx4 v244, s[8:11], s56 offen lds\n"
    "buffer_load_dwordx4 v245, s[8:11], s56 offen offset:1024 lds\n"
    "buffer_load_dwordx4 v246, s[8:11], s56 offen offset:2048 lds\n"
    "s_add_u32 s56, s56, 0x60000\n"
    "s_cmp_eq_u32 s56, 0x780000\n"
    "s_cselect_b32 s56, 0x0, s56\n"
    "s_waitcnt vmcnt(6) lgkmcnt(0)\n"
    "s_barrier\n"
    "s_mov_b32 m0, s55\n"
    "s_mov_b32 s56, 0x180\n"
    "buffer_load_dwordx4 v244, s[8:11], s56 offen lds\n"
    "buffer_load_dwordx4 v245, s[8:11], s56 offen offset:1024 lds\n"
    "buffer_load_dwordx4 v246, s[8:11], s56 offen offset:2048 lds\n"
    "s_add_u32 m0, s34, 0x3000\n"
    "s_mov_b32 s42, 0x3000\n"
    "buffer_load_dwordx4 v248, s[16:19], s42 offen nt lds\n"
    "buffer_load_dwordx4 v249, s[16:19], s42 offen offset:1024 nt lds\n"
    "buffer_load_dwordx4 v250, s[16:19], s42 offen offset:2048 nt lds\n"
    "s_mov_b32 m0, s54\n"
    "s_mov_b32 s42, 0x0\n"
    "buffer_load_dwordx4 v251, s[20:23], s42 offen nt lds\n"
    "buffer_load_dwordx4 v252, s[20:23], s42 offen offset:1024 nt lds\n"
    "buffer_load_dwordx4 v253, s[20:23], s42 offen offset:2048 nt lds\n"
    "s_waitcnt vmcnt(12) lgkmcnt(0)\n"
    "s_barrier\n"
    "s_add_u32 m0, s55, 0x3000\n"
    "s_mov_b32 s56, 0x60180\n"
    "buffer_load_dwordx4 v244, s[8:11], s56 offen lds\n"
    "buffer_load_dwordx4 v245, s[8:11], s56 offen offset:1024 lds\n"
    "buffer_load_dwordx4 v246, s[8:11], s56 offen offset:2048 lds\n"
    "s_waitcnt vmcnt(12) lgkmcnt(0)\n"
    "s_barrier\n"
    "s_add_u32 m0, s55, 0x6000\n"
    "s_mov_b32 s56, 0xc0180\n"
    "buffer_load_dwordx4 v244, s[8:11], s56 offen lds\n"
    "buffer_load_dwordx4 v245, s[8:11], s56 offen offset:1024 lds\n"
    "buffer_load_dwordx4 v246, s[8:11], s56 offen offset:2048 lds\n"
    "s_add_u32 m0, s34, 0x6000\n"
    "s_mov_b32 s42, 0x6000\n"
    "buffer_load_dwordx4 v248, s[16:19], s42 offen nt lds\n"
    "buffer_load_dwordx4 v249, s[16:19], s42 offen offset:1024 nt lds\n"
    "buffer_load_dwordx4 v250, s[16:19], s42 offen offset:2048 nt lds\n"
    "s_waitcnt vmcnt(15) lgkmcnt(0)\n"
    "s_barrier\n"
    "ds_read_b64_tr_b16 v[200:201], v176\n"
    "ds_read_b64_tr_b16 v[202:203], v176 offset:1536\n"
    "s_waitcnt lgkmcnt(0)\n"
    "v_mfma_f32_16x16x32_bf16 v[0:3], v[200:203], v[96:99], v[0:3]\n"
    "ds_read_b64_tr_b16 v[204:205], v177\n"
    "ds_read_b64_tr_b16 v[206:207], v177 offset:1536\n"
    "v_mfma_f32_16x16x32_bf16 v[4:7], v[200:203], v[100:103], v[4:7]\n"
    "ds_read_b64_tr_b16 v[208:209], v178\n"
    "s_waitcnt lgkmcnt(1)\n"
    "v_mfma_f32_16x16x32_bf16 v[8:11], v[204:207], v[96:99], v[8:11]\n"
    "ds_read_b64_tr_b16 v[210:211], v178 offset:1536\n"
    "ds_read_b64_tr_b16 v[212:213], v179\n"
    "v_mfma_f32_16x16x32_bf16 v[12:15], v[204:207], v[100:103], v[12:15]\n"
    "ds_read_b64_tr_b16 v[214:215], v179 offset:1536\n"
    "ds_read_b64_tr_b16 v[216:217], v176 offset:128\n"
    "ds_read_b64_tr_b16 v[218:219], v176 offset:1664\n"
    "ds_read_b64_tr_b16 v[220:221], v177 offset:128\n"
    "ds_read_b64_tr_b16 v[222:223], v177 offset:1664\n"
    "s_waitcnt vmcnt(6) lgkmcnt(0)\n"
    "s_barrier\n"
    "ds_read_b64_tr_b16 v[224:225], v178 offset:128\n"
    "ds_read_b64_tr_b16 v[226:227], v178 offset:1664\n"
    "v_mfma_f32_16x16x32_bf16 v[16:19], v[208:211], v[96:99], v[16:19]\n"
    "ds_read_b64_tr_b16 v[228:229], v179 offset:128\n"
    "ds_read_b64_tr_b16 v[230:231], v179 offset:1664\n"
    "v_mfma_f32_16x16x32_bf16 v[20:23], v[208:211], v[100:103], v[20:23]\n"
    "s_add_u32 m0, s55, 0x9000\n"
    "s_mov_b32 s56, 0x120180\n"
    "v_mfma_f32_16x16x32_bf16 v[24:27], v[212:215], v[96:99], v[24:27]\n"
    "buffer_load_dwordx4 v244, s[8:11], s56 offen lds\n"
    "v_mfma_f32_16x16x32_bf16 v[28:31], v[212:215], v[100:103], v[28:31]\n"
    "buffer_load_dwordx4 v245, s[8:11], s56 offen offset:1024 lds\n"
    "v_mfma_f32_16x16x32_bf16 v[32:35], v[216:219], v[96:99], v[32:35]\n"
    "buffer_load_dwordx4 v246, s[8:11], s56 offen offset:2048 lds\n"
    "v_mfma_f32_16x16x32_bf16 v[36:39], v[216:219], v[100:103], v[36:39]\n"
    "ds_read_b64_tr_b16 v[200:201], v176 offset:256\n"
    "ds_read_b64_tr_b16 v[202:203], v176 offset:1792\n"
    "v_mfma_f32_16x16x32_bf16 v[40:43], v[220:223], v[96:99], v[40:43]\n"
    "ds_read_b64_tr_b16 v[204:205], v177 offset:256\n"
    "ds_read_b64_tr_b16 v[206:207], v177 offset:1792\n"
    "v_mfma_f32_16x16x32_bf16 v[44:47], v[220:223], v[100:103], v[44:47]\n"
    "ds_read_b64_tr_b16 v[208:209], v178 offset:256\n"
    "s_waitcnt lgkmcnt(5)\n"
    "v_mfma_f32_16x16x32_bf16 v[48:51], v[224:227], v[96:99], v[48:51]\n"
    "ds_read_b64_tr_b16 v[210:211], v178 offset:1792\n"
    "ds_read_b64_tr_b16 v[212:213], v179 offset:256\n"
    "v_mfma_f32_16x16x32_bf16 v[52:55], v[224:227], v[100:103], v[52:55]\n"
    "ds_read_b64_tr_b16 v[214:215], v179 offset:1792\n"
    "ds_read_b64_tr_b16 v[216:217], v176 offset:12288\n"
    "v_mfma_f32_16x16x32_bf16 v[56:59], v[228:231], v[96:99], v[56:59]\n"
    "ds_read_b64_tr_b16 v[218:219], v176 offset:13824\n"
    "ds_read_b64_tr_b16 v[220:221], v177 offset:12288\n"
    "v_mfma_f32_16x16x32_bf16 v[60:63], v[228:231], v[100:103], v[60:63]\n"
    "ds_read_b64_tr_b16 v[222:223], v177 offset:13824\n"
    "s_waitcnt lgkmcnt(7)\n"
    "v_mfma_f32_16x16x32_bf16 v[64:67], v[200:203], v[96:99], v[64:67]\n"
    "ds_read_b64_tr_b16 v[224:225], v178 offset:12288\n"
    "ds_read_b64_tr_b16 v[226:227], v178 offset:13824\n"
    "v_mfma_f32_16x16x32_bf16 v[68:71], v[200:203], v[100:103], v[68:71]\n"
    "ds_read_b64_tr_b16 v[228:229], v179 offset:12288\n"
    "ds_read_b64_tr_b16 v[230:231], v179 offset:13824\n"
    "v_mfma_f32_16x16x32_bf16 v[72:75], v[204:207], v[96:99], v[72:75]\n"
    "v_mfma_f32_16x16x32_bf16 v[76:79], v[204:207], v[100:103], v[76:79]\n"
    "s_waitcnt lgkmcnt(4)\n"
    "v_mfma_f32_16x16x32_bf16 v[80:83], v[208:211], v[96:99], v[80:83]\n"
    "v_mfma_f32_16x16x32_bf16 v[84:87], v[208:211], v[100:103], v[84:87]\n"
    "v_mfma_f32_16x16x32_bf16 v[88:91], v[212:215], v[96:99], v[88:91]\n"
    "v_mfma_f32_16x16x32_bf16 v[92:95], v[212:215], v[100:103], v[92:95]\n"
    "s_waitcnt lgkmcnt(0)\n"
    "s_barrier\n"
    "ds_read_b64_tr_b16 v[200:201], v176 offset:12416\n"
    "ds_read_b64_tr_b16 v[202:203], v176 offset:13952\n"
    "v_mfma_f32_16x16x32_bf16 v[0:3], v[216:219], v[104:107], v[0:3]\n"
    "ds_read_b64_tr_b16 v[204:205], v177 offset:12416\n"
    "ds_read_b64_tr_b16 v[206:207], v177 offset:13952\n"
    "v_mfma_f32_16x16x32_bf16 v[4:7], v[216:219], v[108:111], v[4:7]\n"
    "ds_read_b64_tr_b16 v[208:209], v178 offset:12416\n"
    "ds_read_b64_tr_b16 v[210:211], v178 offset:13952\n"
    "v_mfma_f32_16x16x32_bf16 v[8:11], v[220:223], v[104:107], v[8:11]\n"
    "ds_read_b64_tr_b16 v[212:213], v179 offset:12416\n"
    "ds_read_b64_tr_b16 v[214:215], v179 offset:13952\n"
    "v_mfma_f32_16x16x32_bf16 v[12:15], v[220:223], v[108:111], v[12:15]\n"
    "ds_read_b64_tr_b16 v[216:217], v176 offset:12544\n"
    "ds_read_b64_tr_b16 v[218:219], v176 offset:14080\n"
    "v_mfma_f32_16x16x32_bf16 v[16:19], v[224:227], v[104:107], v[16:19]\n"
    "ds_read_b64_tr_b16 v[220:221], v177 offset:12544\n"
    "ds_read_b64_tr_b16 v[222:223], v177 offset:14080\n"
    "v_mfma_f32_16x16x32_bf16 v[20:23], v[224:227], v[108:111], v[20:23]\n"
    "v_mfma_f32_16x16x32_bf16 v[24:27], v[228:231], v[104:107], v[24:27]\n"
    "v_mfma_f32_16x16x32_bf16 v[28:31], v[228:231], v[108:111], v[28:31]\n"
    "s_waitcnt lgkmcnt(4)\n"
    "v_mfma_f32_16x16x32_bf16 v[32:35], v[200:203], v[104:107], v[32:35]\n"
    "v_mfma_f32_16x16x32_bf16 v[36:39], v[200:203], v[108:111], v[36:39]\n"
    "v_mfma_f32_16x16x32_bf16 v[40:43], v[204:207], v[104:107], v[40:43]\n"
    "v_mfma_f32_16x16x32_bf16 v[44:47], v[204:207], v[108:111], v[44:47]\n"
    "s_waitcnt vmcnt(6) lgkmcnt(0)\n"
    "s_barrier\n"
    "ds_read_b64_tr_b16 v[224:225], v178 offset:12544\n"
    "ds_read_b64_tr_b16 v[226:227], v178 offset:14080\n"
    "v_mfma_f32_16x16x32_bf16 v[48:51], v[208:211], v[104:107], v[48:51]\n"
    "ds_read_b64_tr_b16 v[228:229], v179 offset:12544\n"
    "ds_read_b64_tr_b16 v[230:231], v179 offset:14080\n"
    "v_mfma_f32_16x16x32_bf16 v[52:55], v[208:211], v[108:111], v[52:55]\n"
    "s_mov_b32 m0, s55\n"
    "s_mov_b32 s56, 0x180180\n"
    "v_mfma_f32_16x16x32_bf16 v[56:59], v[212:215], v[104:107], v[56:59]\n"
    "buffer_load_dwordx4 v244, s[8:11], s56 offen lds\n"
    "v_mfma_f32_16x16x32_bf16 v[60:63], v[212:215], v[108:111], v[60:63]\n"
    "buffer_load_dwordx4 v245, s[8:11], s56 offen offset:1024 lds\n"
    "v_mfma_f32_16x16x32_bf16 v[64:67], v[216:219], v[104:107], v[64:67]\n"
    "buffer_load_dwordx4 v246, s[8:11], s56 offen offset:2048 lds\n"
    "v_mfma_f32_16x16x32_bf16 v[68:71], v[216:219], v[108:111], v[68:71]\n"
    "ds_read_b64_tr_b16 v[200:201], v176 offset:24576\n"
    "ds_read_b64_tr_b16 v[202:203], v176 offset:26112\n"
    "v_mfma_f32_16x16x32_bf16 v[72:75], v[220:223], v[104:107], v[72:75]\n"
    "ds_read_b64_tr_b16 v[204:205], v177 offset:24576\n"
    "ds_read_b64_tr_b16 v[206:207], v177 offset:26112\n"
    "v_mfma_f32_16x16x32_bf16 v[76:79], v[220:223], v[108:111], v[76:79]\n"
    "ds_read_b64_tr_b16 v[208:209], v178 offset:24576\n"
    "s_waitcnt lgkmcnt(5)\n"
    "v_mfma_f32_16x16x32_bf16 v[80:83], v[224:227], v[104:107], v[80:83]\n"
    "ds_read_b64_tr_b16 v[210:211], v178 offset:26112\n"
    "ds_read_b64_tr_b16 v[212:213], v179 offset:24576\n"
    "v_mfma_f32_16x16x32_bf16 v[84:87], v[224:227], v[108:111], v[84:87]\n"
    "ds_read_b64_tr_b16 v[214:215], v179 offset:26112\n"
    "ds_read_b64_tr_b16 v[216:217], v176 offset:24704\n"
    "v_mfma_f32_16x16x32_bf16 v[88:91], v[228:231], v[104:107], v[88:91]\n"
    "ds_read_b64_tr_b16 v[218:219], v176 offset:26240\n"
    "ds_read_b64_tr_b16 v[220:221], v177 offset:24704\n"
    "v_mfma_f32_16x16x32_bf16 v[92:95], v[228:231], v[108:111], v[92:95]\n"
    "ds_read_b64_tr_b16 v[222:223], v177 offset:26240\n"
    "s_waitcnt lgkmcnt(7)\n"
    "v_mfma_f32_16x16x32_bf16 v[0:3], v[200:203], v[112:115], v[0:3]\n"
    "ds_read_b64_tr_b16 v[224:225], v178 offset:24704\n"
    "ds_read_b64_tr_b16 v[226:227], v178 offset:26240\n"
    "v_mfma_f32_16x16x32_bf16 v[4:7], v[200:203], v[116:119], v[4:7]\n"
    "ds_read_b64_tr_b16 v[228:229], v179 offset:24704\n"
    "ds_read_b64_tr_b16 v[230:231], v179 offset:26240\n"
    "v_mfma_f32_16x16x32_bf16 v[8:11], v[204:207], v[112:115], v[8:11]\n"
    "v_mfma_f32_16x16x32_bf16 v[12:15], v[204:207], v[116:119], v[12:15]\n"
    "s_waitcnt lgkmcnt(4)\n"
    "v_mfma_f32_16x16x32_bf16 v[16:19], v[208:211], v[112:115], v[16:19]\n"
    "v_mfma_f32_16x16x32_bf16 v[20:23], v[208:211], v[116:119], v[20:23]\n"
    "v_mfma_f32_16x16x32_bf16 v[24:27], v[212:215], v[112:115], v[24:27]\n"
    "v_mfma_f32_16x16x32_bf16 v[28:31], v[212:215], v[116:119], v[28:31]\n"
    "s_waitcnt vmcnt(3) lgkmcnt(0)\n"
    "s_barrier\n"
    "ds_read_b64_tr_b16 v[200:201], v176 offset:24832\n"
    "ds_read_b64_tr_b16 v[202:203], v176 offset:26368\n"
    "v_mfma_f32_16x16x32_bf16 v[32:35], v[216:219], v[112:115], v[32:35]\n"
    "ds_read_b64_tr_b16 v[204:205], v177 offset:24832\n"
    "ds_read_b64_tr_b16 v[206:207], v177 offset:26368\n"
    "v_mfma_f32_16x16x32_bf16 v[36:39], v[216:219], v[116:119], v[36:39]\n"
    "s_add_u32 m0, s55, 0x3000\n"
    "s_mov_b32 s56, 0x1e0180\n"
    "v_mfma_f32_16x16x32_bf16 v[40:43], v[220:223], v[112:115], v[40:43]\n"
    "buffer_load_dwordx4 v244, s[8:11], s56 offen lds\n"
    "v_mfma_f32_16x16x32_bf16 v[44:47], v[220:223], v[116:119], v[44:47]\n"
    "buffer_load_dwordx4 v245, s[8:11], s56 offen offset:1024 lds\n"
    "v_mfma_f32_16x16x32_bf16 v[48:51], v[224:227], v[112:115], v[48:51]\n"
    "buffer_load_dwordx4 v246, s[8:11], s56 offen offset:2048 lds\n"
    "v_mfma_f32_16x16x32_bf16 v[52:55], v[224:227], v[116:119], v[52:55]\n"
    "ds_read_b128 v[96:99], v180\n"
    "ds_read_b64_tr_b16 v[208:209], v178 offset:24832\n"
    "v_mfma_f32_16x16x32_bf16 v[56:59], v[228:231], v[112:115], v[56:59]\n"
    "s_mov_b64 exec, s[84:85]\n"
    "ds_read_b128 v[100:103], v181\n"
    "ds_read_b128 v[108:111], v183\n"
    "s_mov_b64 exec, -1\n"
    "v_mfma_f32_16x16x32_bf16 v[60:63], v[228:231], v[116:119], v[60:63]\n"
    "ds_read_b128 v[104:107], v182\n"
    "s_waitcnt lgkmcnt(5)\n"
    "v_mfma_f32_16x16x32_bf16 v[64:67], v[200:203], v[112:115], v[64:67]\n"
    "ds_read_b64_tr_b16 v[210:211], v178 offset:26368\n"
    "ds_read_b64_tr_b16 v[212:213], v179 offset:24832\n"
    "v_mfma_f32_16x16x32_bf16 v[68:71], v[200:203], v[116:119], v[68:71]\n"
    "ds_read_b64_tr_b16 v[214:215], v179 offset:26368\n"
    "ds_read_b64_tr_b16 v[216:217], v176 offset:36864\n"
    "v_mfma_f32_16x16x32_bf16 v[72:75], v[204:207], v[112:115], v[72:75]\n"
    "ds_read_b64_tr_b16 v[218:219], v176 offset:38400\n"
    "ds_read_b64_tr_b16 v[220:221], v177 offset:36864\n"
    "v_mfma_f32_16x16x32_bf16 v[76:79], v[204:207], v[116:119], v[76:79]\n"
    "ds_read_b64_tr_b16 v[222:223], v177 offset:38400\n"
    "s_waitcnt lgkmcnt(0)\n"
    "s_barrier\n"
    "ds_read_b64_tr_b16 v[224:225], v178 offset:36864\n"
    "ds_read_b64_tr_b16 v[226:227], v178 offset:38400\n"
    "v_mfma_f32_16x16x32_bf16 v[80:83], v[208:211], v[112:115], v[80:83]\n"
    "ds_read_b64_tr_b16 v[228:229], v179 offset:36864\n"
    "ds_read_b64_tr_b16 v[230:231], v179 offset:38400\n"
    "v_mfma_f32_16x16x32_bf16 v[84:87], v[208:211], v[116:119], v[84:87]\n"
    "s_mov_b32 s57, 0x300\n"
    "s_mov_b32 m0, s35\n"
    "v_mfma_f32_16x16x32_bf16 v[88:91], v[212:215], v[112:115], v[88:91]\n"
    "s_add_u32 s43, s57, 0x2400\n"
    "s_add_u32 s44, s57, 0x4800\n"
    "v_mfma_f32_16x16x32_bf16 v[92:95], v[212:215], v[116:119], v[92:95]\n"
    "s_add_u32 s45, s57, 0x6c00\n"
    "ds_read_b64_tr_b16 v[200:201], v176 offset:36992\n"
    "v_mfma_f32_16x16x32_bf16 v[0:3], v[216:219], v[120:123], v[0:3]\n"
    "buffer_load_dwordx4 v247, s[12:15], s57 offen lds\n"
    "v_mfma_f32_16x16x32_bf16 v[4:7], v[216:219], v[124:127], v[4:7]\n"
    "buffer_load_dwordx4 v247, s[12:15], s43 offen offset:1024 lds\n"
    "v_mfma_f32_16x16x32_bf16 v[8:11], v[220:223], v[120:123], v[8:11]\n"
    "buffer_load_dwordx4 v247, s[12:15], s44 offen offset:2048 lds\n"
    "v_mfma_f32_16x16x32_bf16 v[12:15], v[220:223], v[124:127], v[12:15]\n"
    "ds_read_b64_tr_b16 v[202:203], v176 offset:38528\n"
    "s_waitcnt lgkmcnt(1)\n"
    "v_mfma_f32_16x16x32_bf16 v[16:19], v[224:227], v[120:123], v[16:19]\n"
    "s_mov_b32 exec_hi, 0\n"
    "buffer_load_dwordx4 v247, s[12:15], s45 offen offset:3072 lds\n"
    "s_mov_b32 exec_hi, -1\n"
    "v_mfma_f32_16x16x32_bf16 v[20:23], v[224:227], v[124:127], v[20:23]\n"
    "ds_read_b64_tr_b16 v[204:205], v177 offset:36992\n"
    "ds_read_b64_tr_b16 v[206:207], v177 offset:38528\n"
    "v_mfma_f32_16x16x32_bf16 v[24:27], v[228:231], v[120:123], v[24:27]\n"
    "ds_read_b64_tr_b16 v[208:209], v178 offset:36992\n"
    "ds_read_b64_tr_b16 v[210:211], v178 offset:38528\n"
    "v_mfma_f32_16x16x32_bf16 v[28:31], v[228:231], v[124:127], v[28:31]\n"
    "ds_read_b64_tr_b16 v[212:213], v179 offset:36992\n"
    "s_waitcnt lgkmcnt(5)\n"
    "v_mfma_f32_16x16x32_bf16 v[32:35], v[200:203], v[120:123], v[32:35]\n"
    "ds_read_b64_tr_b16 v[214:215], v179 offset:38528\n"
    "ds_read_b64_tr_b16 v[216:217], v176 offset:37120\n"
    "v_mfma_f32_16x16x32_bf16 v[36:39], v[200:203], v[124:127], v[36:39]\n"
    "ds_read_b64_tr_b16 v[218:219], v176 offset:38656\n"
    "s_waitcnt lgkmcnt(6)\n"
    "v_mfma_f32_16x16x32_bf16 v[40:43], v[204:207], v[120:123], v[40:43]\n"
    "ds_read_b64_tr_b16 v[220:221], v177 offset:37120\n"
    "ds_read_b64_tr_b16 v[222:223], v177 offset:38656\n"
    "v_mfma_f32_16x16x32_bf16 v[44:47], v[204:207], v[124:127], v[44:47]\n"
    "ds_read_b64_tr_b16 v[224:225], v178 offset:37120\n"
    "s_waitcnt lgkmcnt(6)\n"
    "v_mfma_f32_16x16x32_bf16 v[48:51], v[208:211], v[120:123], v[48:51]\n"
    "ds_read_b64_tr_b16 v[226:227], v178 offset:38656\n"
    "ds_read_b64_tr_b16 v[228:229], v179 offset:37120\n"
    "v_mfma_f32_16x16x32_bf16 v[52:55], v[208:211], v[124:127], v[52:55]\n"
    "ds_read_b64_tr_b16 v[230:231], v179 offset:38656\n"
    "s_waitcnt lgkmcnt(6)\n"
    "v_mfma_f32_16x16x32_bf16 v[56:59], v[212:215], v[120:123], v[56:59]\n"
    "v_mfma_f32_16x16x32_bf16 v[60:63], v[212:215], v[124:127], v[60:63]\n"
    "s_waitcnt vmcnt(7) lgkmcnt(0)\n"
    "s_barrier\n"
    "ds_read_b64_tr_b16 v[200:201], v176\n"
    "ds_read_b64_tr_b16 v[202:203], v176 offset:1536\n"
    "v_mfma_f32_16x16x32_bf16 v[64:67], v[216:219], v[120:123], v[64:67]\n"
    "ds_read_b64_tr_b16 v[204:205], v177\n"
    "ds_read_b64_tr_b16 v[206:207], v177 offset:1536\n"
    "v_mfma_f32_16x16x32_bf16 v[68:71], v[216:219], v[124:127], v[68:71]\n"
    "s_add_u32 m0, s55, 0x6000\n"
    "s_mov_b32 s56, 0x240180\n"
    "v_mfma_f32_16x16x32_bf16 v[72:75], v[220:223], v[120:123], v[72:75]\n"
    "buffer_load_dwordx4 v244, s[8:11], s56 offen lds\n"
    "v_mfma_f32_16x16x32_bf16 v[76:79], v[220:223], v[124:127], v[76:79]\n"
    "buffer_load_dwordx4 v245, s[8:11], s56 offen offset:1024 lds\n"
    "v_mfma_f32_16x16x32_bf16 v[80:83], v[224:227], v[120:123], v[80:83]\n"
    "buffer_load_dwordx4 v246, s[8:11], s56 offen offset:2048 lds\n"
    "v_mfma_f32_16x16x32_bf16 v[84:87], v[224:227], v[124:127], v[84:87]\n"
    "ds_read_b64_tr_b16 v[208:209], v178\n"
    "ds_read_b64_tr_b16 v[210:211], v178 offset:1536\n"
    "v_mfma_f32_16x16x32_bf16 v[88:91], v[228:231], v[120:123], v[88:91]\n"
    "ds_read_b64_tr_b16 v[212:213], v179\n"
    "ds_read_b64_tr_b16 v[214:215], v179 offset:1536\n"
    "v_mfma_f32_16x16x32_bf16 v[92:95], v[228:231], v[124:127], v[92:95]\n"
    "ds_read_b64_tr_b16 v[216:217], v176 offset:128\n"
    "s_waitcnt lgkmcnt(5)\n"
    "v_mfma_f32_16x16x32_bf16 v[0:3], v[200:203], v[128:131], v[0:3]\n"
    "ds_read_b64_tr_b16 v[218:219], v176 offset:1664\n"
    "ds_read_b64_tr_b16 v[220:221], v177 offset:128\n"
    "v_mfma_f32_16x16x32_bf16 v[4:7], v[200:203], v[132:135], v[4:7]\n"
    "ds_read_b64_tr_b16 v[222:223], v177 offset:1664\n"
    "v_mfma_f32_16x16x32_bf16 v[8:11], v[204:207], v[128:131], v[8:11]\n"
    "v_mfma_f32_16x16x32_bf16 v[12:15], v[204:207], v[132:135], v[12:15]\n"
    "s_waitcnt vmcnt(7) lgkmcnt(0)\n"
    "s_barrier\n"
    "ds_read_b64_tr_b16 v[224:225], v178 offset:128\n"
    "ds_read_b64_tr_b16 v[226:227], v178 offset:1664\n"
    "v_mfma_f32_16x16x32_bf16 v[16:19], v[208:211], v[128:131], v[16:19]\n"
    "ds_read_b64_tr_b16 v[228:229], v179 offset:128\n"
    "ds_read_b64_tr_b16 v[230:231], v179 offset:1664\n"
    "v_mfma_f32_16x16x32_bf16 v[20:23], v[208:211], v[132:135], v[20:23]\n"
    "s_add_u32 m0, s55, 0x9000\n"
    "s_mov_b32 s56, 0x2a0180\n"
    "v_mfma_f32_16x16x32_bf16 v[24:27], v[212:215], v[128:131], v[24:27]\n"
    "buffer_load_dwordx4 v244, s[8:11], s56 offen lds\n"
    "v_mfma_f32_16x16x32_bf16 v[28:31], v[212:215], v[132:135], v[28:31]\n"
    "buffer_load_dwordx4 v245, s[8:11], s56 offen offset:1024 lds\n"
    "v_mfma_f32_16x16x32_bf16 v[32:35], v[216:219], v[128:131], v[32:35]\n"
    "buffer_load_dwordx4 v246, s[8:11], s56 offen offset:2048 lds\n"
    "v_mfma_f32_16x16x32_bf16 v[36:39], v[216:219], v[132:135], v[36:39]\n"
    "ds_read_b128 v[112:115], v180 offset:14336\n"
    "ds_read_b64_tr_b16 v[200:201], v176 offset:256\n"
    "v_mfma_f32_16x16x32_bf16 v[40:43], v[220:223], v[128:131], v[40:43]\n"
    "s_mov_b64 exec, s[84:85]\n"
    "ds_read_b128 v[116:119], v181 offset:14336\n"
    "ds_read_b128 v[124:127], v183 offset:14336\n"
    "s_mov_b64 exec, -1\n"
    "v_mfma_f32_16x16x32_bf16 v[44:47], v[220:223], v[132:135], v[44:47]\n"
    "ds_read_b128 v[120:123], v182 offset:14336\n"
    "s_waitcnt lgkmcnt(5)\n"
    "v_mfma_f32_16x16x32_bf16 v[48:51], v[224:227], v[128:131], v[48:51]\n"
    "ds_read_b64_tr_b16 v[202:203], v176 offset:1792\n"
    "ds_read_b64_tr_b16 v[204:205], v177 offset:256\n"
    "v_mfma_f32_16x16x32_bf16 v[52:55], v[224:227], v[132:135], v[52:55]\n"
    "ds_read_b64_tr_b16 v[206:207], v177 offset:1792\n"
    "ds_read_b64_tr_b16 v[208:209], v178 offset:256\n"
    "v_mfma_f32_16x16x32_bf16 v[56:59], v[228:231], v[128:131], v[56:59]\n"
    "ds_read_b64_tr_b16 v[210:211], v178 offset:1792\n"
    "ds_read_b64_tr_b16 v[212:213], v179 offset:256\n"
    "v_mfma_f32_16x16x32_bf16 v[60:63], v[228:231], v[132:135], v[60:63]\n"
    "ds_read_b64_tr_b16 v[214:215], v179 offset:1792\n"
    "s_waitcnt lgkmcnt(6)\n"
    "v_mfma_f32_16x16x32_bf16 v[64:67], v[200:203], v[128:131], v[64:67]\n"
    "ds_read_b64_tr_b16 v[216:217], v176 offset:12288\n"
    "ds_read_b64_tr_b16 v[218:219], v176 offset:13824\n"
    "v_mfma_f32_16x16x32_bf16 v[68:71], v[200:203], v[132:135], v[68:71]\n"
    "ds_read_b64_tr_b16 v[220:221], v177 offset:12288\n"
    "s_waitcnt lgkmcnt(6)\n"
    "v_mfma_f32_16x16x32_bf16 v[72:75], v[204:207], v[128:131], v[72:75]\n"
    "ds_read_b64_tr_b16 v[222:223], v177 offset:13824\n"
    "ds_read_b64_tr_b16 v[224:225], v178 offset:12288\n"
    "v_mfma_f32_16x16x32_bf16 v[76:79], v[204:207], v[132:135], v[76:79]\n"
    "ds_read_b64_tr_b16 v[226:227], v178 offset:13824\n"
    "s_waitcnt lgkmcnt(6)\n"
    "v_mfma_f32_16x16x32_bf16 v[80:83], v[208:211], v[128:131], v[80:83]\n"
    "ds_read_b64_tr_b16 v[228:229], v179 offset:12288\n"
    "ds_read_b64_tr_b16 v[230:231], v179 offset:13824\n"
    "v_mfma_f32_16x16x32_bf16 v[84:87], v[208:211], v[132:135], v[84:87]\n"
    "v_mfma_f32_16x16x32_bf16 v[88:91], v[212:215], v[128:131], v[88:91]\n"
    "v_mfma_f32_16x16x32_bf16 v[92:95], v[212:215], v[132:135], v[92:95]\n"
    "s_waitcnt lgkmcnt(0)\n"
    "s_barrier\n"
    "ds_read_b64_tr_b16 v[200:201], v176 offset:12416\n"
    "ds_read_b64_tr_b16 v[202:203], v176 offset:13952\n"
    "v_mfma_f32_16x16x32_bf16 v[0:3], v[216:219], v[136:139], v[0:3]\n"
    "ds_read_b64_tr_b16 v[204:205], v177 offset:12416\n"
    "ds_read_b64_tr_b16 v[206:207], v177 offset:13952\n"
    "v_mfma_f32_16x16x32_bf16 v[4:7], v[216:219], v[140:143], v[4:7]\n"
    "s_mov_b32 s57, 0x380\n"
    "s_add_u32 m0, s35, 0x3800\n"
    "v_mfma_f32_16x16x32_bf16 v[8:11], v[220:223], v[136:139], v[8:11]\n"
    "s_add_u32 s43, s57, 0x2400\n"
    "s_add_u32 s44, s57, 0x4800\n"
    "v_mfma_f32_16x16x32_bf16 v[12:15], v[220:223], v[140:143], v[12:15]\n"
    "s_add_u32 s45, s57, 0x6c00\n"
    "ds_read_b64_tr_b16 v[208:209], v178 offset:12416\n"
    "v_mfma_f32_16x16x32_bf16 v[16:19], v[224:227], v[136:139], v[16:19]\n"
    "buffer_load_dwordx4 v247, s[12:15], s57 offen lds\n"
    "v_mfma_f32_16x16x32_bf16 v[20:23], v[224:227], v[140:143], v[20:23]\n"
    "buffer_load_dwordx4 v247, s[12:15], s43 offen offset:1024 lds\n"
    "v_mfma_f32_16x16x32_bf16 v[24:27], v[228:231], v[136:139], v[24:27]\n"
    "buffer_load_dwordx4 v247, s[12:15], s44 offen offset:2048 lds\n"
    "v_mfma_f32_16x16x32_bf16 v[28:31], v[228:231], v[140:143], v[28:31]\n"
    "ds_read_b64_tr_b16 v[210:211], v178 offset:13952\n"
    "s_waitcnt lgkmcnt(1)\n"
    "v_mfma_f32_16x16x32_bf16 v[32:35], v[200:203], v[136:139], v[32:35]\n"
    "s_mov_b32 exec_hi, 0\n"
    "buffer_load_dwordx4 v247, s[12:15], s45 offen offset:3072 lds\n"
    "s_mov_b32 exec_hi, -1\n"
    "v_mfma_f32_16x16x32_bf16 v[36:39], v[200:203], v[140:143], v[36:39]\n"
    "ds_read_b64_tr_b16 v[212:213], v179 offset:12416\n"
    "ds_read_b64_tr_b16 v[214:215], v179 offset:13952\n"
    "v_mfma_f32_16x16x32_bf16 v[40:43], v[204:207], v[136:139], v[40:43]\n"
    "ds_read_b64_tr_b16 v[216:217], v176 offset:12544\n"
    "ds_read_b64_tr_b16 v[218:219], v176 offset:14080\n"
    "v_mfma_f32_16x16x32_bf16 v[44:47], v[204:207], v[140:143], v[44:47]\n"
    "ds_read_b64_tr_b16 v[220:221], v177 offset:12544\n"
    "ds_read_b64_tr_b16 v[222:223], v177 offset:14080\n"
    "s_waitcnt vmcnt(7) lgkmcnt(0)\n"
    "s_barrier\n"
    "ds_read_b64_tr_b16 v[224:225], v178 offset:12544\n"
    "ds_read_b64_tr_b16 v[226:227], v178 offset:14080\n"
    "v_mfma_f32_16x16x32_bf16 v[48:51], v[208:211], v[136:139], v[48:51]\n"
    "ds_read_b64_tr_b16 v[228:229], v179 offset:12544\n"
    "ds_read_b64_tr_b16 v[230:231], v179 offset:14080\n"
    "v_mfma_f32_16x16x32_bf16 v[52:55], v[208:211], v[140:143], v[52:55]\n"
    "s_mov_b32 m0, s55\n"
    "s_mov_b32 s56, 0x300180\n"
    "v_mfma_f32_16x16x32_bf16 v[56:59], v[212:215], v[136:139], v[56:59]\n"
    "buffer_load_dwordx4 v244, s[8:11], s56 offen lds\n"
    "v_mfma_f32_16x16x32_bf16 v[60:63], v[212:215], v[140:143], v[60:63]\n"
    "buffer_load_dwordx4 v245, s[8:11], s56 offen offset:1024 lds\n"
    "v_mfma_f32_16x16x32_bf16 v[64:67], v[216:219], v[136:139], v[64:67]\n"
    "buffer_load_dwordx4 v246, s[8:11], s56 offen offset:2048 lds\n"
    "v_mfma_f32_16x16x32_bf16 v[68:71], v[216:219], v[140:143], v[68:71]\n"
    "ds_read_b64_tr_b16 v[200:201], v176 offset:24576\n"
    "ds_read_b64_tr_b16 v[202:203], v176 offset:26112\n"
    "v_mfma_f32_16x16x32_bf16 v[72:75], v[220:223], v[136:139], v[72:75]\n"
    "ds_read_b64_tr_b16 v[204:205], v177 offset:24576\n"
    "ds_read_b64_tr_b16 v[206:207], v177 offset:26112\n"
    "v_mfma_f32_16x16x32_bf16 v[76:79], v[220:223], v[140:143], v[76:79]\n"
    "ds_read_b64_tr_b16 v[208:209], v178 offset:24576\n"
    "s_waitcnt lgkmcnt(5)\n"
    "v_mfma_f32_16x16x32_bf16 v[80:83], v[224:227], v[136:139], v[80:83]\n"
    "ds_read_b64_tr_b16 v[210:211], v178 offset:26112\n"
    "ds_read_b64_tr_b16 v[212:213], v179 offset:24576\n"
    "v_mfma_f32_16x16x32_bf16 v[84:87], v[224:227], v[140:143], v[84:87]\n"
    "ds_read_b64_tr_b16 v[214:215], v179 offset:26112\n"
    "ds_read_b64_tr_b16 v[216:217], v176 offset:24704\n"
    "v_mfma_f32_16x16x32_bf16 v[88:91], v[228:231], v[136:139], v[88:91]\n"
    "ds_read_b64_tr_b16 v[218:219], v176 offset:26240\n"
    "ds_read_b64_tr_b16 v[220:221], v177 offset:24704\n"
    "v_mfma_f32_16x16x32_bf16 v[92:95], v[228:231], v[140:143], v[92:95]\n"
    "ds_read_b64_tr_b16 v[222:223], v177 offset:26240\n"
    "s_waitcnt lgkmcnt(7)\n"
    "v_mfma_f32_16x16x32_bf16 v[0:3], v[200:203], v[144:147], v[0:3]\n"
    "ds_read_b64_tr_b16 v[224:225], v178 offset:24704\n"
    "ds_read_b64_tr_b16 v[226:227], v178 offset:26240\n"
    "v_mfma_f32_16x16x32_bf16 v[4:7], v[200:203], v[148:151], v[4:7]\n"
    "ds_read_b64_tr_b16 v[228:229], v179 offset:24704\n"
    "ds_read_b64_tr_b16 v[230:231], v179 offset:26240\n"
    "v_mfma_f32_16x16x32_bf16 v[8:11], v[204:207], v[144:147], v[8:11]\n"
    "v_mfma_f32_16x16x32_bf16 v[12:15], v[204:207], v[148:151], v[12:15]\n"
    "s_waitcnt lgkmcnt(4)\n"
    "v_mfma_f32_16x16x32_bf16 v[16:19], v[208:211], v[144:147], v[16:19]\n"
    "v_mfma_f32_16x16x32_bf16 v[20:23], v[208:211], v[148:151], v[20:23]\n"
    "v_mfma_f32_16x16x32_bf16 v[24:27], v[212:215], v[144:147], v[24:27]\n"
    "v_mfma_f32_16x16x32_bf16 v[28:31], v[212:215], v[148:151], v[28:31]\n"
    "s_waitcnt vmcnt(7) lgkmcnt(0)\n"
    "s_barrier\n"
    "ds_read_b64_tr_b16 v[200:201], v176 offset:24832\n"
    "ds_read_b64_tr_b16 v[202:203], v176 offset:26368\n"
    "v_mfma_f32_16x16x32_bf16 v[32:35], v[216:219], v[144:147], v[32:35]\n"
    "ds_read_b64_tr_b16 v[204:205], v177 offset:24832\n"
    "ds_read_b64_tr_b16 v[206:207], v177 offset:26368\n"
    "v_mfma_f32_16x16x32_bf16 v[36:39], v[216:219], v[148:151], v[36:39]\n"
    "s_add_u32 m0, s55, 0x3000\n"
    "s_mov_b32 s56, 0x360180\n"
    "v_mfma_f32_16x16x32_bf16 v[40:43], v[220:223], v[144:147], v[40:43]\n"
    "buffer_load_dwordx4 v244, s[8:11], s56 offen lds\n"
    "v_mfma_f32_16x16x32_bf16 v[44:47], v[220:223], v[148:151], v[44:47]\n"
    "buffer_load_dwordx4 v245, s[8:11], s56 offen offset:1024 lds\n"
    "v_mfma_f32_16x16x32_bf16 v[48:51], v[224:227], v[144:147], v[48:51]\n"
    "buffer_load_dwordx4 v246, s[8:11], s56 offen offset:2048 lds\n"
    "v_mfma_f32_16x16x32_bf16 v[52:55], v[224:227], v[148:151], v[52:55]\n"
    "ds_read_b64_tr_b16 v[208:209], v178 offset:24832\n"
    "ds_read_b64_tr_b16 v[210:211], v178 offset:26368\n"
    "v_mfma_f32_16x16x32_bf16 v[56:59], v[228:231], v[144:147], v[56:59]\n"
    "ds_read_b64_tr_b16 v[212:213], v179 offset:24832\n"
    "ds_read_b64_tr_b16 v[214:215], v179 offset:26368\n"
    "v_mfma_f32_16x16x32_bf16 v[60:63], v[228:231], v[148:151], v[60:63]\n"
    "ds_read_b64_tr_b16 v[216:217], v176 offset:36864\n"
    "s_waitcnt lgkmcnt(5)\n"
    "v_mfma_f32_16x16x32_bf16 v[64:67], v[200:203], v[144:147], v[64:67]\n"
    "ds_read_b64_tr_b16 v[218:219], v176 offset:38400\n"
    "ds_read_b64_tr_b16 v[220:221], v177 offset:36864\n"
    "v_mfma_f32_16x16x32_bf16 v[68:71], v[200:203], v[148:151], v[68:71]\n"
    "ds_read_b64_tr_b16 v[222:223], v177 offset:38400\n"
    "v_mfma_f32_16x16x32_bf16 v[72:75], v[204:207], v[144:147], v[72:75]\n"
    "v_mfma_f32_16x16x32_bf16 v[76:79], v[204:207], v[148:151], v[76:79]\n"
    "s_waitcnt lgkmcnt(0)\n"
    "s_barrier\n"
    "ds_read_b64_tr_b16 v[224:225], v178 offset:36864\n"
    "ds_read_b64_tr_b16 v[226:227], v178 offset:38400\n"
    "v_mfma_f32_16x16x32_bf16 v[80:83], v[208:211], v[144:147], v[80:83]\n"
    "ds_read_b64_tr_b16 v[228:229], v179 offset:36864\n"
    "ds_read_b64_tr_b16 v[230:231], v179 offset:38400\n"
    "v_mfma_f32_16x16x32_bf16 v[84:87], v[208:211], v[148:151], v[84:87]\n"
    "ds_read_b64_tr_b16 v[200:201], v176 offset:36992\n"
    "ds_read_b64_tr_b16 v[202:203], v176 offset:38528\n"
    "v_mfma_f32_16x16x32_bf16 v[88:91], v[212:215], v[144:147], v[88:91]\n"
    "ds_read_b64_tr_b16 v[204:205], v177 offset:36992\n"
    "ds_read_b64_tr_b16 v[206:207], v177 offset:38528\n"
    "v_mfma_f32_16x16x32_bf16 v[92:95], v[212:215], v[148:151], v[92:95]\n"
    "ds_read_b64_tr_b16 v[208:209], v178 offset:36992\n"
    "ds_read_b64_tr_b16 v[210:211], v178 offset:38528\n"
    "v_mfma_f32_16x16x32_bf16 v[0:3], v[216:219], v[152:155], v[0:3]\n"
    "ds_read_b64_tr_b16 v[212:213], v179 offset:36992\n"
    "ds_read_b64_tr_b16 v[214:215], v179 offset:38528\n"
    "v_mfma_f32_16x16x32_bf16 v[4:7], v[216:219], v[156:159], v[4:7]\n"
    "ds_read_b64_tr_b16 v[216:217], v176 offset:37120\n"
    "ds_read_b64_tr_b16 v[218:219], v176 offset:38656\n"
    "v_mfma_f32_16x16x32_bf16 v[8:11], v[220:223], v[152:155], v[8:11]\n"
    "v_mfma_f32_16x16x32_bf16 v[12:15], v[220:223], v[156:159], v[12:15]\n"
    "ds_read_b64_tr_b16 v[220:221], v177 offset:37120\n"
    "s_waitcnt lgkmcnt(5)\n"
    "v_mfma_f32_16x16x32_bf16 v[16:19], v[224:227], v[152:155], v[16:19]\n"
    "ds_read_b64_tr_b16 v[222:223], v177 offset:38656\n"
    "v_mfma_f32_16x16x32_bf16 v[20:23], v[224:227], v[156:159], v[20:23]\n"
    "ds_read_b64_tr_b16 v[224:225], v178 offset:37120\n"
    "ds_read_b64_tr_b16 v[226:227], v178 offset:38656\n"
    "v_mfma_f32_16x16x32_bf16 v[24:27], v[228:231], v[152:155], v[24:27]\n"
    "v_mfma_f32_16x16x32_bf16 v[28:31], v[228:231], v[156:159], v[28:31]\n"
    "ds_read_b64_tr_b16 v[228:229], v179 offset:37120\n"
    "ds_read_b64_tr_b16 v[230:231], v179 offset:38656\n"
    "v_mfma_f32_16x16x32_bf16 v[32:35], v[200:203], v[152:155], v[32:35]\n"
    "v_mfma_f32_16x16x32_bf16 v[36:39], v[200:203], v[156:159], v[36:39]\n"
    "v_mfma_f32_16x16x32_bf16 v[40:43], v[204:207], v[152:155], v[40:43]\n"
    "v_mfma_f32_16x16x32_bf16 v[44:47], v[204:207], v[156:159], v[44:47]\n"
    "v_mfma_f32_16x16x32_bf16 v[48:51], v[208:211], v[152:155], v[48:51]\n"
    "v_mfma_f32_16x16x32_bf16 v[52:55], v[208:211], v[156:159], v[52:55]\n"
    "s_waitcnt lgkmcnt(0)\n"
    "v_mfma_f32_16x16x32_bf16 v[56:59], v[212:215], v[152:155], v[56:59]\n"
    "v_mfma_f32_16x16x32_bf16 v[60:63], v[212:215], v[156:159], v[60:63]\n"
    "s_waitcnt vmcnt(3) lgkmcnt(0)\n"
    "s_barrier\n"
    "ds_read_b64_tr_b16 v[200:201], v176\n"
    "ds_read_b64_tr_b16 v[202:203], v176 offset:1536\n"
    "v_mfma_f32_16x16x32_bf16 v[64:67], v[216:219], v[152:155], v[64:67]\n"
    "ds_read_b64_tr_b16 v[204:205], v177\n"
    "ds_read_b64_tr_b16 v[206:207], v177 offset:1536\n"
    "v_mfma_f32_16x16x32_bf16 v[68:71], v[216:219], v[156:159], v[68:71]\n"
    "s_add_u32 m0, s55, 0x6000\n"
    "s_mov_b32 s56, 0x3c0180\n"
    "v_mfma_f32_16x16x32_bf16 v[72:75], v[220:223], v[152:155], v[72:75]\n"
    "buffer_load_dwordx4 v244, s[8:11], s56 offen lds\n"
    "v_mfma_f32_16x16x32_bf16 v[76:79], v[220:223], v[156:159], v[76:79]\n"
    "buffer_load_dwordx4 v245, s[8:11], s56 offen offset:1024 lds\n"
    "v_mfma_f32_16x16x32_bf16 v[80:83], v[224:227], v[152:155], v[80:83]\n"
    "buffer_load_dwordx4 v246, s[8:11], s56 offen offset:2048 lds\n"
    "v_mfma_f32_16x16x32_bf16 v[84:87], v[224:227], v[156:159], v[84:87]\n"
    "ds_read_b64_tr_b16 v[208:209], v178\n"
    "ds_read_b64_tr_b16 v[210:211], v178 offset:1536\n"
    "v_mfma_f32_16x16x32_bf16 v[88:91], v[228:231], v[152:155], v[88:91]\n"
    "ds_read_b64_tr_b16 v[212:213], v179\n"
    "ds_read_b64_tr_b16 v[214:215], v179 offset:1536\n"
    "v_mfma_f32_16x16x32_bf16 v[92:95], v[228:231], v[156:159], v[92:95]\n"
    "ds_read_b64_tr_b16 v[216:217], v176 offset:128\n"
    "s_waitcnt lgkmcnt(5)\n"
    "v_mfma_f32_16x16x32_bf16 v[0:3], v[200:203], v[160:163], v[0:3]\n"
    "ds_read_b64_tr_b16 v[218:219], v176 offset:1664\n"
    "ds_read_b64_tr_b16 v[220:221], v177 offset:128\n"
    "v_mfma_f32_16x16x32_bf16 v[4:7], v[200:203], v[164:167], v[4:7]\n"
    "ds_read_b64_tr_b16 v[222:223], v177 offset:1664\n"
    "v_mfma_f32_16x16x32_bf16 v[8:11], v[204:207], v[160:163], v[8:11]\n"
    "v_mfma_f32_16x16x32_bf16 v[12:15], v[204:207], v[164:167], v[12:15]\n"
    "s_waitcnt vmcnt(3) lgkmcnt(0)\n"
    "s_barrier\n"
    "ds_read_b64_tr_b16 v[224:225], v178 offset:128\n"
    "ds_read_b64_tr_b16 v[226:227], v178 offset:1664\n"
    "v_mfma_f32_16x16x32_bf16 v[16:19], v[208:211], v[160:163], v[16:19]\n"
    "ds_read_b64_tr_b16 v[228:229], v179 offset:128\n"
    "ds_read_b64_tr_b16 v[230:231], v179 offset:1664\n"
    "v_mfma_f32_16x16x32_bf16 v[20:23], v[208:211], v[164:167], v[20:23]\n"
    "s_add_u32 m0, s55, 0x9000\n"
    "s_mov_b32 s56, 0x420180\n"
    "v_mfma_f32_16x16x32_bf16 v[24:27], v[212:215], v[160:163], v[24:27]\n"
    "buffer_load_dwordx4 v244, s[8:11], s56 offen lds\n"
    "v_mfma_f32_16x16x32_bf16 v[28:31], v[212:215], v[164:167], v[28:31]\n"
    "buffer_load_dwordx4 v245, s[8:11], s56 offen offset:1024 lds\n"
    "v_mfma_f32_16x16x32_bf16 v[32:35], v[216:219], v[160:163], v[32:35]\n"
    "buffer_load_dwordx4 v246, s[8:11], s56 offen offset:2048 lds\n"
    "v_mfma_f32_16x16x32_bf16 v[36:39], v[216:219], v[164:167], v[36:39]\n"
    "ds_read_b128 v[144:147], v180\n"
    "ds_read_b64_tr_b16 v[200:201], v176 offset:256\n"
    "v_mfma_f32_16x16x32_bf16 v[40:43], v[220:223], v[160:163], v[40:43]\n"
    "s_mov_b64 exec, s[84:85]\n"
    "ds_read_b128 v[148:151], v181\n"
    "ds_read_b128 v[156:159], v183\n"
    "s_mov_b64 exec, -1\n"
    "v_mfma_f32_16x16x32_bf16 v[44:47], v[220:223], v[164:167], v[44:47]\n"
    "ds_read_b128 v[152:155], v182\n"
    "s_waitcnt lgkmcnt(5)\n"
    "v_mfma_f32_16x16x32_bf16 v[48:51], v[224:227], v[160:163], v[48:51]\n"
    "ds_read_b64_tr_b16 v[202:203], v176 offset:1792\n"
    "ds_read_b64_tr_b16 v[204:205], v177 offset:256\n"
    "v_mfma_f32_16x16x32_bf16 v[52:55], v[224:227], v[164:167], v[52:55]\n"
    "ds_read_b64_tr_b16 v[206:207], v177 offset:1792\n"
    "ds_read_b64_tr_b16 v[208:209], v178 offset:256\n"
    "v_mfma_f32_16x16x32_bf16 v[56:59], v[228:231], v[160:163], v[56:59]\n"
    "ds_read_b64_tr_b16 v[210:211], v178 offset:1792\n"
    "ds_read_b64_tr_b16 v[212:213], v179 offset:256\n"
    "v_mfma_f32_16x16x32_bf16 v[60:63], v[228:231], v[164:167], v[60:63]\n"
    "ds_read_b64_tr_b16 v[214:215], v179 offset:1792\n"
    "s_waitcnt lgkmcnt(6)\n"
    "v_mfma_f32_16x16x32_bf16 v[64:67], v[200:203], v[160:163], v[64:67]\n"
    "ds_read_b64_tr_b16 v[216:217], v176 offset:12288\n"
    "ds_read_b64_tr_b16 v[218:219], v176 offset:13824\n"
    "v_mfma_f32_16x16x32_bf16 v[68:71], v[200:203], v[164:167], v[68:71]\n"
    "ds_read_b64_tr_b16 v[220:221], v177 offset:12288\n"
    "s_waitcnt lgkmcnt(6)\n"
    "v_mfma_f32_16x16x32_bf16 v[72:75], v[204:207], v[160:163], v[72:75]\n"
    "ds_read_b64_tr_b16 v[222:223], v177 offset:13824\n"
    "ds_read_b64_tr_b16 v[224:225], v178 offset:12288\n"
    "v_mfma_f32_16x16x32_bf16 v[76:79], v[204:207], v[164:167], v[76:79]\n"
    "ds_read_b64_tr_b16 v[226:227], v178 offset:13824\n"
    "s_waitcnt lgkmcnt(6)\n"
    "v_mfma_f32_16x16x32_bf16 v[80:83], v[208:211], v[160:163], v[80:83]\n"
    "ds_read_b64_tr_b16 v[228:229], v179 offset:12288\n"
    "ds_read_b64_tr_b16 v[230:231], v179 offset:13824\n"
    "v_mfma_f32_16x16x32_bf16 v[84:87], v[208:211], v[164:167], v[84:87]\n"
    "v_mfma_f32_16x16x32_bf16 v[88:91], v[212:215], v[160:163], v[88:91]\n"
    "v_mfma_f32_16x16x32_bf16 v[92:95], v[212:215], v[164:167], v[92:95]\n"
    "s_waitcnt lgkmcnt(0)\n"
    "s_barrier\n"
    "ds_read_b64_tr_b16 v[200:201], v176 offset:12416\n"
    "ds_read_b64_tr_b16 v[202:203], v176 offset:13952\n"
    "v_mfma_f32_16x16x32_bf16 v[0:3], v[216:219], v[168:171], v[0:3]\n"
    "ds_read_b64_tr_b16 v[204:205], v177 offset:12416\n"
    "ds_read_b64_tr_b16 v[206:207], v177 offset:13952\n"
    "v_mfma_f32_16x16x32_bf16 v[4:7], v[216:219], v[172:175], v[4:7]\n"
    "ds_read_b64_tr_b16 v[208:209], v178 offset:12416\n"
    "ds_read_b64_tr_b16 v[210:211], v178 offset:13952\n"
    "v_mfma_f32_16x16x32_bf16 v[8:11], v[220:223], v[168:171], v[8:11]\n"
    "ds_read_b64_tr_b16 v[212:213], v179 offset:12416\n"
    "ds_read_b64_tr_b16 v[214:215], v179 offset:13952\n"
    "v_mfma_f32_16x16x32_bf16 v[12:15], v[220:223], v[172:175], v[12:15]\n"
    "ds_read_b64_tr_b16 v[216:217], v176 offset:12544\n"
    "ds_read_b64_tr_b16 v[218:219], v176 offset:14080\n"
    "v_mfma_f32_16x16x32_bf16 v[16:19], v[224:227], v[168:171], v[16:19]\n"
    "ds_read_b64_tr_b16 v[220:221], v177 offset:12544\n"
    "ds_read_b64_tr_b16 v[222:223], v177 offset:14080\n"
    "v_mfma_f32_16x16x32_bf16 v[20:23], v[224:227], v[172:175], v[20:23]\n"
    "v_mfma_f32_16x16x32_bf16 v[24:27], v[228:231], v[168:171], v[24:27]\n"
    "v_mfma_f32_16x16x32_bf16 v[28:31], v[228:231], v[172:175], v[28:31]\n"
    "s_waitcnt lgkmcnt(4)\n"
    "v_mfma_f32_16x16x32_bf16 v[32:35], v[200:203], v[168:171], v[32:35]\n"
    "v_mfma_f32_16x16x32_bf16 v[36:39], v[200:203], v[172:175], v[36:39]\n"
    "v_mfma_f32_16x16x32_bf16 v[40:43], v[204:207], v[168:171], v[40:43]\n"
    "v_mfma_f32_16x16x32_bf16 v[44:47], v[204:207], v[172:175], v[44:47]\n"
    "s_waitcnt vmcnt(3) lgkmcnt(0)\n"
    "s_barrier\n"
    "ds_read_b64_tr_b16 v[224:225], v178 offset:12544\n"
    "ds_read_b64_tr_b16 v[226:227], v178 offset:14080\n"
    "v_mfma_f32_16x16x32_bf16 v[48:51], v[208:211], v[168:171], v[48:51]\n"
    "ds_read_b64_tr_b16 v[228:229], v179 offset:12544\n"
    "ds_read_b64_tr_b16 v[230:231], v179 offset:14080\n"
    "v_mfma_f32_16x16x32_bf16 v[52:55], v[208:211], v[172:175], v[52:55]\n"
    "s_mov_b32 m0, s55\n"
    "s_mov_b32 s56, 0x600180\n"
    "v_mfma_f32_16x16x32_bf16 v[56:59], v[212:215], v[168:171], v[56:59]\n"
    "buffer_load_dwordx4 v244, s[8:11], s56 offen lds\n"
    "v_mfma_f32_16x16x32_bf16 v[60:63], v[212:215], v[172:175], v[60:63]\n"
    "buffer_load_dwordx4 v245, s[8:11], s56 offen offset:1024 lds\n"
    "v_mfma_f32_16x16x32_bf16 v[64:67], v[216:219], v[168:171], v[64:67]\n"
    "buffer_load_dwordx4 v246, s[8:11], s56 offen offset:2048 lds\n"
    "v_mfma_f32_16x16x32_bf16 v[68:71], v[216:219], v[172:175], v[68:71]\n"
    "ds_read_b64_tr_b16 v[200:201], v176 offset:24576\n"
    "ds_read_b64_tr_b16 v[202:203], v176 offset:26112\n"
    "v_mfma_f32_16x16x32_bf16 v[72:75], v[220:223], v[168:171], v[72:75]\n"
    "ds_read_b64_tr_b16 v[204:205], v177 offset:24576\n"
    "ds_read_b64_tr_b16 v[206:207], v177 offset:26112\n"
    "v_mfma_f32_16x16x32_bf16 v[76:79], v[220:223], v[172:175], v[76:79]\n"
    "ds_read_b64_tr_b16 v[208:209], v178 offset:24576\n"
    "s_waitcnt lgkmcnt(5)\n"
    "v_mfma_f32_16x16x32_bf16 v[80:83], v[224:227], v[168:171], v[80:83]\n"
    "ds_read_b64_tr_b16 v[210:211], v178 offset:26112\n"
    "ds_read_b64_tr_b16 v[212:213], v179 offset:24576\n"
    "v_mfma_f32_16x16x32_bf16 v[84:87], v[224:227], v[172:175], v[84:87]\n"
    "ds_read_b64_tr_b16 v[214:215], v179 offset:26112\n"
    "ds_read_b64_tr_b16 v[216:217], v176 offset:24704\n"
    "v_mfma_f32_16x16x32_bf16 v[88:91], v[228:231], v[168:171], v[88:91]\n"
    "ds_read_b64_tr_b16 v[218:219], v176 offset:26240\n"
    "ds_read_b64_tr_b16 v[220:221], v177 offset:24704\n"
    "v_mfma_f32_16x16x32_bf16 v[92:95], v[228:231], v[172:175], v[92:95]\n"
    "ds_read_b64_tr_b16 v[222:223], v177 offset:26240\n"
    "s_waitcnt lgkmcnt(7)\n"
    "v_mfma_f32_16x16x32_bf16 v[0:3], v[200:203], v[184:187], v[0:3]\n"
    "ds_read_b64_tr_b16 v[224:225], v178 offset:24704\n"
    "ds_read_b64_tr_b16 v[226:227], v178 offset:26240\n"
    "v_mfma_f32_16x16x32_bf16 v[4:7], v[200:203], v[188:191], v[4:7]\n"
    "ds_read_b64_tr_b16 v[228:229], v179 offset:24704\n"
    "ds_read_b64_tr_b16 v[230:231], v179 offset:26240\n"
    "v_mfma_f32_16x16x32_bf16 v[8:11], v[204:207], v[184:187], v[8:11]\n"
    "v_mfma_f32_16x16x32_bf16 v[12:15], v[204:207], v[188:191], v[12:15]\n"
    "s_waitcnt lgkmcnt(4)\n"
    "v_mfma_f32_16x16x32_bf16 v[16:19], v[208:211], v[184:187], v[16:19]\n"
    "v_mfma_f32_16x16x32_bf16 v[20:23], v[208:211], v[188:191], v[20:23]\n"
    "v_mfma_f32_16x16x32_bf16 v[24:27], v[212:215], v[184:187], v[24:27]\n"
    "v_mfma_f32_16x16x32_bf16 v[28:31], v[212:215], v[188:191], v[28:31]\n"
    "s_waitcnt vmcnt(3) lgkmcnt(0)\n"
    "s_barrier\n"
    "ds_read_b64_tr_b16 v[200:201], v176 offset:24832\n"
    "ds_read_b64_tr_b16 v[202:203], v176 offset:26368\n"
    "v_mfma_f32_16x16x32_bf16 v[32:35], v[216:219], v[184:187], v[32:35]\n"
    "ds_read_b64_tr_b16 v[204:205], v177 offset:24832\n"
    "ds_read_b64_tr_b16 v[206:207], v177 offset:26368\n"
    "v_mfma_f32_16x16x32_bf16 v[36:39], v[216:219], v[188:191], v[36:39]\n"
    "s_add_u32 m0, s55, 0x3000\n"
    "s_mov_b32 s56, 0x660180\n"
    "v_mfma_f32_16x16x32_bf16 v[40:43], v[220:223], v[184:187], v[40:43]\n"
    "buffer_load_dwordx4 v244, s[8:11], s56 offen lds\n"
    "v_mfma_f32_16x16x32_bf16 v[44:47], v[220:223], v[188:191], v[44:47]\n"
    "buffer_load_dwordx4 v245, s[8:11], s56 offen offset:1024 lds\n"
    "v_mfma_f32_16x16x32_bf16 v[48:51], v[224:227], v[184:187], v[48:51]\n"
    "buffer_load_dwordx4 v246, s[8:11], s56 offen offset:2048 lds\n"
    "v_mfma_f32_16x16x32_bf16 v[52:55], v[224:227], v[188:191], v[52:55]\n"
    "ds_read_b128 v[160:163], v180 offset:14336\n"
    "ds_read_b64_tr_b16 v[208:209], v178 offset:24832\n"
    "v_mfma_f32_16x16x32_bf16 v[56:59], v[228:231], v[184:187], v[56:59]\n"
    "s_mov_b64 exec, s[84:85]\n"
    "ds_read_b128 v[164:167], v181 offset:14336\n"
    "ds_read_b128 v[172:175], v183 offset:14336\n"
    "s_mov_b64 exec, -1\n"
    "v_mfma_f32_16x16x32_bf16 v[60:63], v[228:231], v[188:191], v[60:63]\n"
    "ds_read_b128 v[168:171], v182 offset:14336\n"
    "s_waitcnt lgkmcnt(5)\n"
    "v_mfma_f32_16x16x32_bf16 v[64:67], v[200:203], v[184:187], v[64:67]\n"
    "ds_read_b64_tr_b16 v[210:211], v178 offset:26368\n"
    "ds_read_b64_tr_b16 v[212:213], v179 offset:24832\n"
    "v_mfma_f32_16x16x32_bf16 v[68:71], v[200:203], v[188:191], v[68:71]\n"
    "ds_read_b64_tr_b16 v[214:215], v179 offset:26368\n"
    "ds_read_b64_tr_b16 v[216:217], v176 offset:36864\n"
    "v_mfma_f32_16x16x32_bf16 v[72:75], v[204:207], v[184:187], v[72:75]\n"
    "ds_read_b64_tr_b16 v[218:219], v176 offset:38400\n"
    "ds_read_b64_tr_b16 v[220:221], v177 offset:36864\n"
    "v_mfma_f32_16x16x32_bf16 v[76:79], v[204:207], v[188:191], v[76:79]\n"
    "ds_read_b64_tr_b16 v[222:223], v177 offset:38400\n"
    "s_waitcnt lgkmcnt(0)\n"
    "s_barrier\n"
    "ds_read_b64_tr_b16 v[224:225], v178 offset:36864\n"
    "ds_read_b64_tr_b16 v[226:227], v178 offset:38400\n"
    "v_mfma_f32_16x16x32_bf16 v[80:83], v[208:211], v[184:187], v[80:83]\n"
    "ds_read_b64_tr_b16 v[228:229], v179 offset:36864\n"
    "ds_read_b64_tr_b16 v[230:231], v179 offset:38400\n"
    "v_mfma_f32_16x16x32_bf16 v[84:87], v[208:211], v[188:191], v[84:87]\n"
    "ds_read_b64_tr_b16 v[200:201], v176 offset:36992\n"
    "ds_read_b64_tr_b16 v[202:203], v176 offset:38528\n"
    "v_mfma_f32_16x16x32_bf16 v[88:91], v[212:215], v[184:187], v[88:91]\n"
    "ds_read_b64_tr_b16 v[204:205], v177 offset:36992\n"
    "ds_read_b64_tr_b16 v[206:207], v177 offset:38528\n"
    "v_mfma_f32_16x16x32_bf16 v[92:95], v[212:215], v[188:191], v[92:95]\n"
    "ds_read_b64_tr_b16 v[208:209], v178 offset:36992\n"
    "ds_read_b64_tr_b16 v[210:211], v178 offset:38528\n"
    "v_mfma_f32_16x16x32_bf16 v[0:3], v[216:219], v[192:195], v[0:3]\n"
    "ds_read_b64_tr_b16 v[212:213], v179 offset:36992\n"
    "ds_read_b64_tr_b16 v[214:215], v179 offset:38528\n"
    "v_mfma_f32_16x16x32_bf16 v[4:7], v[216:219], v[196:199], v[4:7]\n"
    "ds_read_b64_tr_b16 v[216:217], v176 offset:37120\n"
    "ds_read_b64_tr_b16 v[218:219], v176 offset:38656\n"
    "v_mfma_f32_16x16x32_bf16 v[8:11], v[220:223], v[192:195], v[8:11]\n"
    "v_mfma_f32_16x16x32_bf16 v[12:15], v[220:223], v[196:199], v[12:15]\n"
    "ds_read_b64_tr_b16 v[220:221], v177 offset:37120\n"
    "s_waitcnt lgkmcnt(5)\n"
    "v_mfma_f32_16x16x32_bf16 v[16:19], v[224:227], v[192:195], v[16:19]\n"
    "ds_read_b64_tr_b16 v[222:223], v177 offset:38656\n"
    "v_mfma_f32_16x16x32_bf16 v[20:23], v[224:227], v[196:199], v[20:23]\n"
    "ds_read_b64_tr_b16 v[224:225], v178 offset:37120\n"
    "ds_read_b64_tr_b16 v[226:227], v178 offset:38656\n"
    "v_mfma_f32_16x16x32_bf16 v[24:27], v[228:231], v[192:195], v[24:27]\n"
    "v_mfma_f32_16x16x32_bf16 v[28:31], v[228:231], v[196:199], v[28:31]\n"
    "ds_read_b64_tr_b16 v[228:229], v179 offset:37120\n"
    "ds_read_b64_tr_b16 v[230:231], v179 offset:38656\n"
    "v_mfma_f32_16x16x32_bf16 v[32:35], v[200:203], v[192:195], v[32:35]\n"
    "v_mfma_f32_16x16x32_bf16 v[36:39], v[200:203], v[196:199], v[36:39]\n"
    "v_mfma_f32_16x16x32_bf16 v[40:43], v[204:207], v[192:195], v[40:43]\n"
    "v_mfma_f32_16x16x32_bf16 v[44:47], v[204:207], v[196:199], v[44:47]\n"
    "v_mfma_f32_16x16x32_bf16 v[48:51], v[208:211], v[192:195], v[48:51]\n"
    "v_mfma_f32_16x16x32_bf16 v[52:55], v[208:211], v[196:199], v[52:55]\n"
    "s_waitcnt lgkmcnt(0)\n"
    "v_mfma_f32_16x16x32_bf16 v[56:59], v[212:215], v[192:195], v[56:59]\n"
    "v_mfma_f32_16x16x32_bf16 v[60:63], v[212:215], v[196:199], v[60:63]\n"
    "s_waitcnt vmcnt(3) lgkmcnt(0)\n"
    "s_barrier\n"
    "ds_read_b64_tr_b16 v[200:201], v176\n"
    "ds_read_b64_tr_b16 v[202:203], v176 offset:1536\n"
    "v_mfma_f32_16x16x32_bf16 v[64:67], v[216:219], v[192:195], v[64:67]\n"
    "ds_read_b64_tr_b16 v[204:205], v177\n"
    "ds_read_b64_tr_b16 v[206:207], v177 offset:1536\n"
    "v_mfma_f32_16x16x32_bf16 v[68:71], v[216:219], v[196:199], v[68:71]\n"
    "s_add_u32 m0, s55, 0x6000\n"
    "s_mov_b32 s56, 0x6c0180\n"
    "v_mfma_f32_16x16x32_bf16 v[72:75], v[220:223], v[192:195], v[72:75]\n"
    "buffer_load_dwordx4 v244, s[8:11], s56 offen lds\n"
    "v_mfma_f32_16x16x32_bf16 v[76:79], v[220:223], v[196:199], v[76:79]\n"
    "buffer_load_dwordx4 v245, s[8:11], s56 offen offset:1024 lds\n"
    "v_mfma_f32_16x16x32_bf16 v[80:83], v[224:227], v[192:195], v[80:83]\n"
    "buffer_load_dwordx4 v246, s[8:11], s56 offen offset:2048 lds\n"
    "v_mfma_f32_16x16x32_bf16 v[84:87], v[224:227], v[196:199], v[84:87]\n"
    "ds_read_b64_tr_b16 v[208:209], v178\n"
    "ds_read_b64_tr_b16 v[210:211], v178 offset:1536\n"
    "v_mfma_f32_16x16x32_bf16 v[88:91], v[228:231], v[192:195], v[88:91]\n"
    "ds_read_b64_tr_b16 v[212:213], v179\n"
    "ds_read_b64_tr_b16 v[214:215], v179 offset:1536\n"
    "v_mfma_f32_16x16x32_bf16 v[92:95], v[228:231], v[196:199], v[92:95]\n"
    "ds_read_b64_tr_b16 v[216:217], v176 offset:128\n"
    "s_waitcnt lgkmcnt(5)\n"
    "v_mfma_f32_16x16x32_bf16 v[0:3], v[200:203], v[96:99], v[0:3]\n"
    "ds_read_b64_tr_b16 v[218:219], v176 offset:1664\n"
    "ds_read_b64_tr_b16 v[220:221], v177 offset:128\n"
    "v_mfma_f32_16x16x32_bf16 v[4:7], v[200:203], v[100:103], v[4:7]\n"
    "ds_read_b64_tr_b16 v[222:223], v177 offset:1664\n"
    "v_mfma_f32_16x16x32_bf16 v[8:11], v[204:207], v[96:99], v[8:11]\n"
    "v_mfma_f32_16x16x32_bf16 v[12:15], v[204:207], v[100:103], v[12:15]\n"
    "s_waitcnt vmcnt(3) lgkmcnt(0)\n"
    "s_barrier\n"
    "ds_read_b64_tr_b16 v[224:225], v178 offset:128\n"
    "ds_read_b64_tr_b16 v[226:227], v178 offset:1664\n"
    "v_mfma_f32_16x16x32_bf16 v[16:19], v[208:211], v[96:99], v[16:19]\n"
    "ds_read_b64_tr_b16 v[228:229], v179 offset:128\n"
    "ds_read_b64_tr_b16 v[230:231], v179 offset:1664\n"
    "v_mfma_f32_16x16x32_bf16 v[20:23], v[208:211], v[100:103], v[20:23]\n"
    "s_add_u32 m0, s55, 0x9000\n"
    "s_mov_b32 s56, 0x720180\n"
    "v_mfma_f32_16x16x32_bf16 v[24:27], v[212:215], v[96:99], v[24:27]\n"
    "buffer_load_dwordx4 v244, s[8:11], s56 offen lds\n"
    "v_mfma_f32_16x16x32_bf16 v[28:31], v[212:215], v[100:103], v[28:31]\n"
    "buffer_load_dwordx4 v245, s[8:11], s56 offen offset:1024 lds\n"
    "v_mfma_f32_16x16x32_bf16 v[32:35], v[216:219], v[96:99], v[32:35]\n"
    "buffer_load_dwordx4 v246, s[8:11], s56 offen offset:2048 lds\n"
    "v_mfma_f32_16x16x32_bf16 v[36:39], v[216:219], v[100:103], v[36:39]\n"
    "ds_read_b64_tr_b16 v[200:201], v176 offset:256\n"
    "ds_read_b64_tr_b16 v[202:203], v176 offset:1792\n"
    "v_mfma_f32_16x16x32_bf16 v[40:43], v[220:223], v[96:99], v[40:43]\n"
    "ds_read_b64_tr_b16 v[204:205], v177 offset:256\n"
    "ds_read_b64_tr_b16 v[206:207], v177 offset:1792\n"
    "v_mfma_f32_16x16x32_bf16 v[44:47], v[220:223], v[100:103], v[44:47]\n"
    "ds_read_b64_tr_b16 v[208:209], v178 offset:256\n"
    "s_waitcnt lgkmcnt(5)\n"
    "v_mfma_f32_16x16x32_bf16 v[48:51], v[224:227], v[96:99], v[48:51]\n"
    "ds_read_b64_tr_b16 v[210:211], v178 offset:1792\n"
    "ds_read_b64_tr_b16 v[212:213], v179 offset:256\n"
    "v_mfma_f32_16x16x32_bf16 v[52:55], v[224:227], v[100:103], v[52:55]\n"
    "ds_read_b64_tr_b16 v[214:215], v179 offset:1792\n"
    "ds_read_b64_tr_b16 v[216:217], v176 offset:12288\n"
    "v_mfma_f32_16x16x32_bf16 v[56:59], v[228:231], v[96:99], v[56:59]\n"
    "ds_read_b64_tr_b16 v[218:219], v176 offset:13824\n"
    "ds_read_b64_tr_b16 v[220:221], v177 offset:12288\n"
    "v_mfma_f32_16x16x32_bf16 v[60:63], v[228:231], v[100:103], v[60:63]\n"
    "ds_read_b64_tr_b16 v[222:223], v177 offset:13824\n"
    "s_waitcnt lgkmcnt(7)\n"
    "v_mfma_f32_16x16x32_bf16 v[64:67], v[200:203], v[96:99], v[64:67]\n"
    "ds_read_b64_tr_b16 v[224:225], v178 offset:12288\n"
    "ds_read_b64_tr_b16 v[226:227], v178 offset:13824\n"
    "v_mfma_f32_16x16x32_bf16 v[68:71], v[200:203], v[100:103], v[68:71]\n"
    "ds_read_b64_tr_b16 v[228:229], v179 offset:12288\n"
    "ds_read_b64_tr_b16 v[230:231], v179 offset:13824\n"
    "v_mfma_f32_16x16x32_bf16 v[72:75], v[204:207], v[96:99], v[72:75]\n"
    "v_mfma_f32_16x16x32_bf16 v[76:79], v[204:207], v[100:103], v[76:79]\n"
    "s_waitcnt lgkmcnt(4)\n"
    "v_mfma_f32_16x16x32_bf16 v[80:83], v[208:211], v[96:99], v[80:83]\n"
    "v_mfma_f32_16x16x32_bf16 v[84:87], v[208:211], v[100:103], v[84:87]\n"
    "v_mfma_f32_16x16x32_bf16 v[88:91], v[212:215], v[96:99], v[88:91]\n"
    "v_mfma_f32_16x16x32_bf16 v[92:95], v[212:215], v[100:103], v[92:95]\n"
    "s_waitcnt lgkmcnt(0)\n"
    "s_barrier\n"
    "ds_read_b64_tr_b16 v[200:201], v176 offset:12416\n"
    "ds_read_b64_tr_b16 v[202:203], v176 offset:13952\n"
    "v_mfma_f32_16x16x32_bf16 v[0:3], v[216:219], v[104:107], v[0:3]\n"
    "ds_read_b64_tr_b16 v[204:205], v177 offset:12416\n"
    "ds_read_b64_tr_b16 v[206:207], v177 offset:13952\n"
    "v_mfma_f32_16x16x32_bf16 v[4:7], v[216:219], v[108:111], v[4:7]\n"
    "ds_read_b64_tr_b16 v[208:209], v178 offset:12416\n"
    "ds_read_b64_tr_b16 v[210:211], v178 offset:13952\n"
    "v_mfma_f32_16x16x32_bf16 v[8:11], v[220:223], v[104:107], v[8:11]\n"
    "ds_read_b64_tr_b16 v[212:213], v179 offset:12416\n"
    "ds_read_b64_tr_b16 v[214:215], v179 offset:13952\n"
    "v_mfma_f32_16x16x32_bf16 v[12:15], v[220:223], v[108:111], v[12:15]\n"
    "ds_read_b64_tr_b16 v[216:217], v176 offset:12544\n"
    "ds_read_b64_tr_b16 v[218:219], v176 offset:14080\n"
    "v_mfma_f32_16x16x32_bf16 v[16:19], v[224:227], v[104:107], v[16:19]\n"
    "ds_read_b64_tr_b16 v[220:221], v177 offset:12544\n"
    "ds_read_b64_tr_b16 v[222:223], v177 offset:14080\n"
    "v_mfma_f32_16x16x32_bf16 v[20:23], v[224:227], v[108:111], v[20:23]\n"
    "v_mfma_f32_16x16x32_bf16 v[24:27], v[228:231], v[104:107], v[24:27]\n"
    "v_mfma_f32_16x16x32_bf16 v[28:31], v[228:231], v[108:111], v[28:31]\n"
    "s_waitcnt lgkmcnt(4)\n"
    "v_mfma_f32_16x16x32_bf16 v[32:35], v[200:203], v[104:107], v[32:35]\n"
    "v_mfma_f32_16x16x32_bf16 v[36:39], v[200:203], v[108:111], v[36:39]\n"
    "v_mfma_f32_16x16x32_bf16 v[40:43], v[204:207], v[104:107], v[40:43]\n"
    "v_mfma_f32_16x16x32_bf16 v[44:47], v[204:207], v[108:111], v[44:47]\n"
    "s_waitcnt vmcnt(3) lgkmcnt(0)\n"
    "s_barrier\n"
    "ds_read_b64_tr_b16 v[224:225], v178 offset:12544\n"
    "ds_read_b64_tr_b16 v[226:227], v178 offset:14080\n"
    "v_mfma_f32_16x16x32_bf16 v[48:51], v[208:211], v[104:107], v[48:51]\n"
    "ds_read_b64_tr_b16 v[228:229], v179 offset:12544\n"
    "ds_read_b64_tr_b16 v[230:231], v179 offset:14080\n"
    "v_mfma_f32_16x16x32_bf16 v[52:55], v[208:211], v[108:111], v[52:55]\n"
    "s_mov_b32 m0, s55\n"
    "s_mov_b32 s56, 0x480180\n"
    "v_mfma_f32_16x16x32_bf16 v[56:59], v[212:215], v[104:107], v[56:59]\n"
    "buffer_load_dwordx4 v244, s[8:11], s56 offen lds\n"
    "v_mfma_f32_16x16x32_bf16 v[60:63], v[212:215], v[108:111], v[60:63]\n"
    "buffer_load_dwordx4 v245, s[8:11], s56 offen offset:1024 lds\n"
    "v_mfma_f32_16x16x32_bf16 v[64:67], v[216:219], v[104:107], v[64:67]\n"
    "buffer_load_dwordx4 v246, s[8:11], s56 offen offset:2048 lds\n"
    "v_mfma_f32_16x16x32_bf16 v[68:71], v[216:219], v[108:111], v[68:71]\n"
    "ds_read_b64_tr_b16 v[200:201], v176 offset:24576\n"
    "ds_read_b64_tr_b16 v[202:203], v176 offset:26112\n"
    "v_mfma_f32_16x16x32_bf16 v[72:75], v[220:223], v[104:107], v[72:75]\n"
    "ds_read_b64_tr_b16 v[204:205], v177 offset:24576\n"
    "ds_read_b64_tr_b16 v[206:207], v177 offset:26112\n"
    "v_mfma_f32_16x16x32_bf16 v[76:79], v[220:223], v[108:111], v[76:79]\n"
    "ds_read_b64_tr_b16 v[208:209], v178 offset:24576\n"
    "s_waitcnt lgkmcnt(5)\n"
    "v_mfma_f32_16x16x32_bf16 v[80:83], v[224:227], v[104:107], v[80:83]\n"
    "ds_read_b64_tr_b16 v[210:211], v178 offset:26112\n"
    "ds_read_b64_tr_b16 v[212:213], v179 offset:24576\n"
    "v_mfma_f32_16x16x32_bf16 v[84:87], v[224:227], v[108:111], v[84:87]\n"
    "ds_read_b64_tr_b16 v[214:215], v179 offset:26112\n"
    "ds_read_b64_tr_b16 v[216:217], v176 offset:24704\n"
    "v_mfma_f32_16x16x32_bf16 v[88:91], v[228:231], v[104:107], v[88:91]\n"
    "ds_read_b64_tr_b16 v[218:219], v176 offset:26240\n"
    "ds_read_b64_tr_b16 v[220:221], v177 offset:24704\n"
    "v_mfma_f32_16x16x32_bf16 v[92:95], v[228:231], v[108:111], v[92:95]\n"
    "ds_read_b64_tr_b16 v[222:223], v177 offset:26240\n"
    "s_waitcnt lgkmcnt(7)\n"
    "v_mfma_f32_16x16x32_bf16 v[0:3], v[200:203], v[112:115], v[0:3]\n"
    "ds_read_b64_tr_b16 v[224:225], v178 offset:24704\n"
    "ds_read_b64_tr_b16 v[226:227], v178 offset:26240\n"
    "v_mfma_f32_16x16x32_bf16 v[4:7], v[200:203], v[116:119], v[4:7]\n"
    "ds_read_b64_tr_b16 v[228:229], v179 offset:24704\n"
    "ds_read_b64_tr_b16 v[230:231], v179 offset:26240\n"
    "v_mfma_f32_16x16x32_bf16 v[8:11], v[204:207], v[112:115], v[8:11]\n"
    "v_mfma_f32_16x16x32_bf16 v[12:15], v[204:207], v[116:119], v[12:15]\n"
    "s_waitcnt lgkmcnt(4)\n"
    "v_mfma_f32_16x16x32_bf16 v[16:19], v[208:211], v[112:115], v[16:19]\n"
    "v_mfma_f32_16x16x32_bf16 v[20:23], v[208:211], v[116:119], v[20:23]\n"
    "v_mfma_f32_16x16x32_bf16 v[24:27], v[212:215], v[112:115], v[24:27]\n"
    "v_mfma_f32_16x16x32_bf16 v[28:31], v[212:215], v[116:119], v[28:31]\n"
    "s_waitcnt vmcnt(3) lgkmcnt(0)\n"
    "s_barrier\n"
    "ds_read_b64_tr_b16 v[200:201], v176 offset:24832\n"
    "ds_read_b64_tr_b16 v[202:203], v176 offset:26368\n"
    "v_mfma_f32_16x16x32_bf16 v[32:35], v[216:219], v[112:115], v[32:35]\n"
    "ds_read_b64_tr_b16 v[204:205], v177 offset:24832\n"
    "ds_read_b64_tr_b16 v[206:207], v177 offset:26368\n"
    "v_mfma_f32_16x16x32_bf16 v[36:39], v[216:219], v[116:119], v[36:39]\n"
    "s_add_u32 m0, s55, 0x3000\n"
    "s_mov_b32 s56, 0x4e0180\n"
    "v_mfma_f32_16x16x32_bf16 v[40:43], v[220:223], v[112:115], v[40:43]\n"
    "buffer_load_dwordx4 v244, s[8:11], s56 offen lds\n"
    "v_mfma_f32_16x16x32_bf16 v[44:47], v[220:223], v[116:119], v[44:47]\n"
    "buffer_load_dwordx4 v245, s[8:11], s56 offen offset:1024 lds\n"
    "v_mfma_f32_16x16x32_bf16 v[48:51], v[224:227], v[112:115], v[48:51]\n"
    "buffer_load_dwordx4 v246, s[8:11], s56 offen offset:2048 lds\n"
    "v_mfma_f32_16x16x32_bf16 v[52:55], v[224:227], v[116:119], v[52:55]\n"
    "ds_read_b64_tr_b16 v[208:209], v178 offset:24832\n"
    "ds_read_b64_tr_b16 v[210:211], v178 offset:26368\n"
    "v_mfma_f32_16x16x32_bf16 v[56:59], v[228:231], v[112:115], v[56:59]\n"
    "ds_read_b64_tr_b16 v[212:213], v179 offset:24832\n"
    "ds_read_b64_tr_b16 v[214:215], v179 offset:26368\n"
    "v_mfma_f32_16x16x32_bf16 v[60:63], v[228:231], v[116:119], v[60:63]\n"
    "ds_read_b64_tr_b16 v[216:217], v176 offset:36864\n"
    "s_waitcnt lgkmcnt(5)\n"
    "v_mfma_f32_16x16x32_bf16 v[64:67], v[200:203], v[112:115], v[64:67]\n"
    "ds_read_b64_tr_b16 v[218:219], v176 offset:38400\n"
    "ds_read_b64_tr_b16 v[220:221], v177 offset:36864\n"
    "v_mfma_f32_16x16x32_bf16 v[68:71], v[200:203], v[116:119], v[68:71]\n"
    "ds_read_b64_tr_b16 v[222:223], v177 offset:38400\n"
    "v_mfma_f32_16x16x32_bf16 v[72:75], v[204:207], v[112:115], v[72:75]\n"
    "v_mfma_f32_16x16x32_bf16 v[76:79], v[204:207], v[116:119], v[76:79]\n"
    "s_waitcnt lgkmcnt(0)\n"
    "s_barrier\n"
    "ds_read_b64_tr_b16 v[224:225], v178 offset:36864\n"
    "ds_read_b64_tr_b16 v[226:227], v178 offset:38400\n"
    "v_mfma_f32_16x16x32_bf16 v[80:83], v[208:211], v[112:115], v[80:83]\n"
    "ds_read_b64_tr_b16 v[228:229], v179 offset:36864\n"
    "ds_read_b64_tr_b16 v[230:231], v179 offset:38400\n"
    "v_mfma_f32_16x16x32_bf16 v[84:87], v[208:211], v[116:119], v[84:87]\n"
    "ds_read_b64_tr_b16 v[200:201], v176 offset:36992\n"
    "ds_read_b64_tr_b16 v[202:203], v176 offset:38528\n"
    "v_mfma_f32_16x16x32_bf16 v[88:91], v[212:215], v[112:115], v[88:91]\n"
    "ds_read_b64_tr_b16 v[204:205], v177 offset:36992\n"
    "ds_read_b64_tr_b16 v[206:207], v177 offset:38528\n"
    "v_mfma_f32_16x16x32_bf16 v[92:95], v[212:215], v[116:119], v[92:95]\n"
    "ds_read_b64_tr_b16 v[208:209], v178 offset:36992\n"
    "ds_read_b64_tr_b16 v[210:211], v178 offset:38528\n"
    "v_mfma_f32_16x16x32_bf16 v[0:3], v[216:219], v[120:123], v[0:3]\n"
    "ds_read_b64_tr_b16 v[212:213], v179 offset:36992\n"
    "ds_read_b64_tr_b16 v[214:215], v179 offset:38528\n"
    "v_mfma_f32_16x16x32_bf16 v[4:7], v[216:219], v[124:127], v[4:7]\n"
    "ds_read_b64_tr_b16 v[216:217], v176 offset:37120\n"
    "ds_read_b64_tr_b16 v[218:219], v176 offset:38656\n"
    "v_mfma_f32_16x16x32_bf16 v[8:11], v[220:223], v[120:123], v[8:11]\n"
    "v_mfma_f32_16x16x32_bf16 v[12:15], v[220:223], v[124:127], v[12:15]\n"
    "ds_read_b64_tr_b16 v[220:221], v177 offset:37120\n"
    "s_waitcnt lgkmcnt(5)\n"
    "v_mfma_f32_16x16x32_bf16 v[16:19], v[224:227], v[120:123], v[16:19]\n"
    "ds_read_b64_tr_b16 v[222:223], v177 offset:38656\n"
    "v_mfma_f32_16x16x32_bf16 v[20:23], v[224:227], v[124:127], v[20:23]\n"
    "ds_read_b64_tr_b16 v[224:225], v178 offset:37120\n"
    "ds_read_b64_tr_b16 v[226:227], v178 offset:38656\n"
    "v_mfma_f32_16x16x32_bf16 v[24:27], v[228:231], v[120:123], v[24:27]\n"
    "v_mfma_f32_16x16x32_bf16 v[28:31], v[228:231], v[124:127], v[28:31]\n"
    "ds_read_b64_tr_b16 v[228:229], v179 offset:37120\n"
    "ds_read_b64_tr_b16 v[230:231], v179 offset:38656\n"
    "v_mfma_f32_16x16x32_bf16 v[32:35], v[200:203], v[120:123], v[32:35]\n"
    "v_mfma_f32_16x16x32_bf16 v[36:39], v[200:203], v[124:127], v[36:39]\n"
    "v_mfma_f32_16x16x32_bf16 v[40:43], v[204:207], v[120:123], v[40:43]\n"
    "v_mfma_f32_16x16x32_bf16 v[44:47], v[204:207], v[124:127], v[44:47]\n"
    "v_mfma_f32_16x16x32_bf16 v[48:51], v[208:211], v[120:123], v[48:51]\n"
    "v_mfma_f32_16x16x32_bf16 v[52:55], v[208:211], v[124:127], v[52:55]\n"
    "s_waitcnt lgkmcnt(0)\n"
    "v_mfma_f32_16x16x32_bf16 v[56:59], v[212:215], v[120:123], v[56:59]\n"
    "v_mfma_f32_16x16x32_bf16 v[60:63], v[212:215], v[124:127], v[60:63]\n"
    "s_waitcnt vmcnt(3) lgkmcnt(0)\n"
    "s_barrier\n"
    "ds_read_b64_tr_b16 v[200:201], v176\n"
    "ds_read_b64_tr_b16 v[202:203], v176 offset:1536\n"
    "v_mfma_f32_16x16x32_bf16 v[64:67], v[216:219], v[120:123], v[64:67]\n"
    "ds_read_b64_tr_b16 v[204:205], v177\n"
    "ds_read_b64_tr_b16 v[206:207], v177 offset:1536\n"
    "v_mfma_f32_16x16x32_bf16 v[68:71], v[216:219], v[124:127], v[68:71]\n"
    "s_add_u32 m0, s55, 0x6000\n"
    "s_mov_b32 s56, 0x540180\n"
    "v_mfma_f32_16x16x32_bf16 v[72:75], v[220:223], v[120:123], v[72:75]\n"
    "buffer_load_dwordx4 v244, s[8:11], s56 offen lds\n"
    "v_mfma_f32_16x16x32_bf16 v[76:79], v[220:223], v[124:127], v[76:79]\n"
    "buffer_load_dwordx4 v245, s[8:11], s56 offen offset:1024 lds\n"
    "v_mfma_f32_16x16x32_bf16 v[80:83], v[224:227], v[120:123], v[80:83]\n"
    "buffer_load_dwordx4 v246, s[8:11], s56 offen offset:2048 lds\n"
    "v_mfma_f32_16x16x32_bf16 v[84:87], v[224:227], v[124:127], v[84:87]\n"
    "ds_read_b64_tr_b16 v[208:209], v178\n"
    "ds_read_b64_tr_b16 v[210:211], v178 offset:1536\n"
    "v_mfma_f32_16x16x32_bf16 v[88:91], v[228:231], v[120:123], v[88:91]\n"
    "ds_read_b64_tr_b16 v[212:213], v179\n"
    "ds_read_b64_tr_b16 v[214:215], v179 offset:1536\n"
    "v_mfma_f32_16x16x32_bf16 v[92:95], v[228:231], v[124:127], v[92:95]\n"
    "ds_read_b64_tr_b16 v[216:217], v176 offset:128\n"
    "s_waitcnt lgkmcnt(5)\n"
    "v_mfma_f32_16x16x32_bf16 v[0:3], v[200:203], v[144:147], v[0:3]\n"
    "ds_read_b64_tr_b16 v[218:219], v176 offset:1664\n"
    "ds_read_b64_tr_b16 v[220:221], v177 offset:128\n"
    "v_mfma_f32_16x16x32_bf16 v[4:7], v[200:203], v[148:151], v[4:7]\n"
    "ds_read_b64_tr_b16 v[222:223], v177 offset:1664\n"
    "v_mfma_f32_16x16x32_bf16 v[8:11], v[204:207], v[144:147], v[8:11]\n"
    "v_mfma_f32_16x16x32_bf16 v[12:15], v[204:207], v[148:151], v[12:15]\n"
    "s_waitcnt vmcnt(3) lgkmcnt(0)\n"
    "s_barrier\n"
    "ds_read_b64_tr_b16 v[224:225], v178 offset:128\n"
    "ds_read_b64_tr_b16 v[226:227], v178 offset:1664\n"
    "v_mfma_f32_16x16x32_bf16 v[16:19], v[208:211], v[144:147], v[16:19]\n"
    "ds_read_b64_tr_b16 v[228:229], v179 offset:128\n"
    "ds_read_b64_tr_b16 v[230:231], v179 offset:1664\n"
    "v_mfma_f32_16x16x32_bf16 v[20:23], v[208:211], v[148:151], v[20:23]\n"
    "s_add_u32 m0, s55, 0x9000\n"
    "s_mov_b32 s56, 0x5a0180\n"
    "v_mfma_f32_16x16x32_bf16 v[24:27], v[212:215], v[144:147], v[24:27]\n"
    "buffer_load_dwordx4 v244, s[8:11], s56 offen lds\n"
    "v_mfma_f32_16x16x32_bf16 v[28:31], v[212:215], v[148:151], v[28:31]\n"
    "buffer_load_dwordx4 v245, s[8:11], s56 offen offset:1024 lds\n"
    "v_mfma_f32_16x16x32_bf16 v[32:35], v[216:219], v[144:147], v[32:35]\n"
    "buffer_load_dwordx4 v246, s[8:11], s56 offen offset:2048 lds\n"
    "v_mfma_f32_16x16x32_bf16 v[36:39], v[216:219], v[148:151], v[36:39]\n"
    "ds_read_b64_tr_b16 v[200:201], v176 offset:256\n"
    "ds_read_b64_tr_b16 v[202:203], v176 offset:1792\n"
    "v_mfma_f32_16x16x32_bf16 v[40:43], v[220:223], v[144:147], v[40:43]\n"
    "ds_read_b64_tr_b16 v[204:205], v177 offset:256\n"
    "ds_read_b64_tr_b16 v[206:207], v177 offset:1792\n"
    "v_mfma_f32_16x16x32_bf16 v[44:47], v[220:223], v[148:151], v[44:47]\n"
    "ds_read_b64_tr_b16 v[208:209], v178 offset:256\n"
    "s_waitcnt lgkmcnt(5)\n"
    "v_mfma_f32_16x16x32_bf16 v[48:51], v[224:227], v[144:147], v[48:51]\n"
    "ds_read_b64_tr_b16 v[210:211], v178 offset:1792\n"
    "ds_read_b64_tr_b16 v[212:213], v179 offset:256\n"
    "v_mfma_f32_16x16x32_bf16 v[52:55], v[224:227], v[148:151], v[52:55]\n"
    "ds_read_b64_tr_b16 v[214:215], v179 offset:1792\n"
    "ds_read_b64_tr_b16 v[216:217], v176 offset:12288\n"
    "v_mfma_f32_16x16x32_bf16 v[56:59], v[228:231], v[144:147], v[56:59]\n"
    "ds_read_b64_tr_b16 v[218:219], v176 offset:13824\n"
    "ds_read_b64_tr_b16 v[220:221], v177 offset:12288\n"
    "v_mfma_f32_16x16x32_bf16 v[60:63], v[228:231], v[148:151], v[60:63]\n"
    "ds_read_b64_tr_b16 v[222:223], v177 offset:13824\n"
    "s_waitcnt lgkmcnt(7)\n"
    "v_mfma_f32_16x16x32_bf16 v[64:67], v[200:203], v[144:147], v[64:67]\n"
    "ds_read_b64_tr_b16 v[224:225], v178 offset:12288\n"
    "ds_read_b64_tr_b16 v[226:227], v178 offset:13824\n"
    "v_mfma_f32_16x16x32_bf16 v[68:71], v[200:203], v[148:151], v[68:71]\n"
    "ds_read_b64_tr_b16 v[228:229], v179 offset:12288\n"
    "ds_read_b64_tr_b16 v[230:231], v179 offset:13824\n"
    "v_mfma_f32_16x16x32_bf16 v[72:75], v[204:207], v[144:147], v[72:75]\n"
    "v_mfma_f32_16x16x32_bf16 v[76:79], v[204:207], v[148:151], v[76:79]\n"
    "s_waitcnt lgkmcnt(4)\n"
    "v_mfma_f32_16x16x32_bf16 v[80:83], v[208:211], v[144:147], v[80:83]\n"
    "v_mfma_f32_16x16x32_bf16 v[84:87], v[208:211], v[148:151], v[84:87]\n"
    "v_mfma_f32_16x16x32_bf16 v[88:91], v[212:215], v[144:147], v[88:91]\n"
    "v_mfma_f32_16x16x32_bf16 v[92:95], v[212:215], v[148:151], v[92:95]\n"
    "s_waitcnt lgkmcnt(0)\n"
    "s_barrier\n"
    "ds_read_b64_tr_b16 v[200:201], v176 offset:12416\n"
    "ds_read_b64_tr_b16 v[202:203], v176 offset:13952\n"
    "v_mfma_f32_16x16x32_bf16 v[0:3], v[216:219], v[152:155], v[0:3]\n"
    "ds_read_b64_tr_b16 v[204:205], v177 offset:12416\n"
    "ds_read_b64_tr_b16 v[206:207], v177 offset:13952\n"
    "v_mfma_f32_16x16x32_bf16 v[4:7], v[216:219], v[156:159], v[4:7]\n"
    "ds_read_b64_tr_b16 v[208:209], v178 offset:12416\n"
    "ds_read_b64_tr_b16 v[210:211], v178 offset:13952\n"
    "v_mfma_f32_16x16x32_bf16 v[8:11], v[220:223], v[152:155], v[8:11]\n"
    "ds_read_b64_tr_b16 v[212:213], v179 offset:12416\n"
    "ds_read_b64_tr_b16 v[214:215], v179 offset:13952\n"
    "v_mfma_f32_16x16x32_bf16 v[12:15], v[220:223], v[156:159], v[12:15]\n"
    "ds_read_b64_tr_b16 v[216:217], v176 offset:12544\n"
    "ds_read_b64_tr_b16 v[218:219], v176 offset:14080\n"
    "v_mfma_f32_16x16x32_bf16 v[16:19], v[224:227], v[152:155], v[16:19]\n"
    "ds_read_b64_tr_b16 v[220:221], v177 offset:12544\n"
    "ds_read_b64_tr_b16 v[222:223], v177 offset:14080\n"
    "v_mfma_f32_16x16x32_bf16 v[20:23], v[224:227], v[156:159], v[20:23]\n"
    "v_mfma_f32_16x16x32_bf16 v[24:27], v[228:231], v[152:155], v[24:27]\n"
    "v_mfma_f32_16x16x32_bf16 v[28:31], v[228:231], v[156:159], v[28:31]\n"
    "s_waitcnt lgkmcnt(4)\n"
    "v_mfma_f32_16x16x32_bf16 v[32:35], v[200:203], v[152:155], v[32:35]\n"
    "v_mfma_f32_16x16x32_bf16 v[36:39], v[200:203], v[156:159], v[36:39]\n"
    "v_mfma_f32_16x16x32_bf16 v[40:43], v[204:207], v[152:155], v[40:43]\n"
    "v_mfma_f32_16x16x32_bf16 v[44:47], v[204:207], v[156:159], v[44:47]\n"
    "s_waitcnt vmcnt(3) lgkmcnt(0)\n"
    "s_barrier\n"
    "ds_read_b64_tr_b16 v[224:225], v178 offset:12544\n"
    "ds_read_b64_tr_b16 v[226:227], v178 offset:14080\n"
    "v_mfma_f32_16x16x32_bf16 v[48:51], v[208:211], v[152:155], v[48:51]\n"
    "ds_read_b64_tr_b16 v[228:229], v179 offset:12544\n"
    "ds_read_b64_tr_b16 v[230:231], v179 offset:14080\n"
    "v_mfma_f32_16x16x32_bf16 v[52:55], v[208:211], v[156:159], v[52:55]\n"
    "ds_read_b64_tr_b16 v[200:201], v176 offset:24576\n"
    "ds_read_b64_tr_b16 v[202:203], v176 offset:26112\n"
    "v_mfma_f32_16x16x32_bf16 v[56:59], v[212:215], v[152:155], v[56:59]\n"
    "ds_read_b64_tr_b16 v[204:205], v177 offset:24576\n"
    "ds_read_b64_tr_b16 v[206:207], v177 offset:26112\n"
    "v_mfma_f32_16x16x32_bf16 v[60:63], v[212:215], v[156:159], v[60:63]\n"
    "ds_read_b64_tr_b16 v[208:209], v178 offset:24576\n"
    "ds_read_b64_tr_b16 v[210:211], v178 offset:26112\n"
    "v_mfma_f32_16x16x32_bf16 v[64:67], v[216:219], v[152:155], v[64:67]\n"
    "ds_read_b64_tr_b16 v[212:213], v179 offset:24576\n"
    "ds_read_b64_tr_b16 v[214:215], v179 offset:26112\n"
    "v_mfma_f32_16x16x32_bf16 v[68:71], v[216:219], v[156:159], v[68:71]\n"
    "ds_read_b64_tr_b16 v[216:217], v176 offset:24704\n"
    "ds_read_b64_tr_b16 v[218:219], v176 offset:26240\n"
    "v_mfma_f32_16x16x32_bf16 v[72:75], v[220:223], v[152:155], v[72:75]\n"
    "v_mfma_f32_16x16x32_bf16 v[76:79], v[220:223], v[156:159], v[76:79]\n"
    "ds_read_b64_tr_b16 v[220:221], v177 offset:24704\n"
    "s_waitcnt lgkmcnt(5)\n"
    "v_mfma_f32_16x16x32_bf16 v[80:83], v[224:227], v[152:155], v[80:83]\n"
    "ds_read_b64_tr_b16 v[222:223], v177 offset:26240\n"
    "v_mfma_f32_16x16x32_bf16 v[84:87], v[224:227], v[156:159], v[84:87]\n"
    "ds_read_b64_tr_b16 v[224:225], v178 offset:24704\n"
    "ds_read_b64_tr_b16 v[226:227], v178 offset:26240\n"
    "v_mfma_f32_16x16x32_bf16 v[88:91], v[228:231], v[152:155], v[88:91]\n"
    "v_mfma_f32_16x16x32_bf16 v[92:95], v[228:231], v[156:159], v[92:95]\n"
    "ds_read_b64_tr_b16 v[228:229], v179 offset:24704\n"
    "ds_read_b64_tr_b16 v[230:231], v179 offset:26240\n"
    "v_mfma_f32_16x16x32_bf16 v[0:3], v[200:203], v[160:163], v[0:3]\n"
    "v_mfma_f32_16x16x32_bf16 v[4:7], v[200:203], v[164:167], v[4:7]\n"
    "v_mfma_f32_16x16x32_bf16 v[8:11], v[204:207], v[160:163], v[8:11]\n"
    "v_mfma_f32_16x16x32_bf16 v[12:15], v[204:207], v[164:167], v[12:15]\n"
    "v_mfma_f32_16x16x32_bf16 v[16:19], v[208:211], v[160:163], v[16:19]\n"
    "v_mfma_f32_16x16x32_bf16 v[20:23], v[208:211], v[164:167], v[20:23]\n"
    "s_waitcnt lgkmcnt(0)\n"
    "v_mfma_f32_16x16x32_bf16 v[24:27], v[212:215], v[160:163], v[24:27]\n"
    "v_mfma_f32_16x16x32_bf16 v[28:31], v[212:215], v[164:167], v[28:31]\n"
    "s_waitcnt vmcnt(0) lgkmcnt(0)\n"
    "s_barrier\n"
    "ds_read_b64_tr_b16 v[200:201], v176 offset:24832\n"
    "ds_read_b64_tr_b16 v[202:203], v176 offset:26368\n"
    "v_mfma_f32_16x16x32_bf16 v[32:35], v[216:219], v[160:163], v[32:35]\n"
    "ds_read_b64_tr_b16 v[204:205], v177 offset:24832\n"
    "ds_read_b64_tr_b16 v[206:207], v177 offset:26368\n"
    "v_mfma_f32_16x16x32_bf16 v[36:39], v[216:219], v[164:167], v[36:39]\n"
    "ds_read_b64_tr_b16 v[208:209], v178 offset:24832\n"
    "ds_read_b64_tr_b16 v[210:211], v178 offset:26368\n"
    "v_mfma_f32_16x16x32_bf16 v[40:43], v[220:223], v[160:163], v[40:43]\n"
    "ds_read_b64_tr_b16 v[212:213], v179 offset:24832\n"
    "ds_read_b64_tr_b16 v[214:215], v179 offset:26368\n"
    "v_mfma_f32_16x16x32_bf16 v[44:47], v[220:223], v[164:167], v[44:47]\n"
    "ds_read_b64_tr_b16 v[216:217], v176 offset:36864\n"
    "ds_read_b64_tr_b16 v[218:219], v176 offset:38400\n"
    "v_mfma_f32_16x16x32_bf16 v[48:51], v[224:227], v[160:163], v[48:51]\n"
    "ds_read_b64_tr_b16 v[220:221], v177 offset:36864\n"
    "ds_read_b64_tr_b16 v[222:223], v177 offset:38400\n"
    "v_mfma_f32_16x16x32_bf16 v[52:55], v[224:227], v[164:167], v[52:55]\n"
    "v_mfma_f32_16x16x32_bf16 v[56:59], v[228:231], v[160:163], v[56:59]\n"
    "v_mfma_f32_16x16x32_bf16 v[60:63], v[228:231], v[164:167], v[60:63]\n"
    "s_waitcnt lgkmcnt(4)\n"
    "v_mfma_f32_16x16x32_bf16 v[64:67], v[200:203], v[160:163], v[64:67]\n"
    "v_mfma_f32_16x16x32_bf16 v[68:71], v[200:203], v[164:167], v[68:71]\n"
    "v_mfma_f32_16x16x32_bf16 v[72:75], v[204:207], v[160:163], v[72:75]\n"
    "v_mfma_f32_16x16x32_bf16 v[76:79], v[204:207], v[164:167], v[76:79]\n"
    "s_waitcnt lgkmcnt(0)\n"
    "s_barrier\n"
    "ds_read_b64_tr_b16 v[224:225], v178 offset:36864\n"
    "ds_read_b64_tr_b16 v[226:227], v178 offset:38400\n"
    "v_mfma_f32_16x16x32_bf16 v[80:83], v[208:211], v[160:163], v[80:83]\n"
    "ds_read_b64_tr_b16 v[228:229], v179 offset:36864\n"
    "ds_read_b64_tr_b16 v[230:231], v179 offset:38400\n"
    "v_mfma_f32_16x16x32_bf16 v[84:87], v[208:211], v[164:167], v[84:87]\n"
    "ds_read_b64_tr_b16 v[200:201], v176 offset:36992\n"
    "ds_read_b64_tr_b16 v[202:203], v176 offset:38528\n"
    "v_mfma_f32_16x16x32_bf16 v[88:91], v[212:215], v[160:163], v[88:91]\n"
    "ds_read_b64_tr_b16 v[204:205], v177 offset:36992\n"
    "ds_read_b64_tr_b16 v[206:207], v177 offset:38528\n"
    "v_mfma_f32_16x16x32_bf16 v[92:95], v[212:215], v[164:167], v[92:95]\n"
    "ds_read_b64_tr_b16 v[208:209], v178 offset:36992\n"
    "ds_read_b64_tr_b16 v[210:211], v178 offset:38528\n"
    "v_mfma_f32_16x16x32_bf16 v[0:3], v[216:219], v[168:171], v[0:3]\n"
    "ds_read_b64_tr_b16 v[212:213], v179 offset:36992\n"
    "ds_read_b64_tr_b16 v[214:215], v179 offset:38528\n"
    "v_mfma_f32_16x16x32_bf16 v[4:7], v[216:219], v[172:175], v[4:7]\n"
    "ds_read_b64_tr_b16 v[216:217], v176 offset:37120\n"
    "ds_read_b64_tr_b16 v[218:219], v176 offset:38656\n"
    "v_mfma_f32_16x16x32_bf16 v[8:11], v[220:223], v[168:171], v[8:11]\n"
    "v_mfma_f32_16x16x32_bf16 v[12:15], v[220:223], v[172:175], v[12:15]\n"
    "ds_read_b64_tr_b16 v[220:221], v177 offset:37120\n"
    "s_waitcnt lgkmcnt(5)\n"
    "v_mfma_f32_16x16x32_bf16 v[16:19], v[224:227], v[168:171], v[16:19]\n"
    "ds_read_b64_tr_b16 v[222:223], v177 offset:38656\n"
    "v_mfma_f32_16x16x32_bf16 v[20:23], v[224:227], v[172:175], v[20:23]\n"
    "ds_read_b64_tr_b16 v[224:225], v178 offset:37120\n"
    "ds_read_b64_tr_b16 v[226:227], v178 offset:38656\n"
    "v_mfma_f32_16x16x32_bf16 v[24:27], v[228:231], v[168:171], v[24:27]\n"
    "v_mfma_f32_16x16x32_bf16 v[28:31], v[228:231], v[172:175], v[28:31]\n"
    "ds_read_b64_tr_b16 v[228:229], v179 offset:37120\n"
    "ds_read_b64_tr_b16 v[230:231], v179 offset:38656\n"
    "v_mfma_f32_16x16x32_bf16 v[32:35], v[200:203], v[168:171], v[32:35]\n"
    "v_mfma_f32_16x16x32_bf16 v[36:39], v[200:203], v[172:175], v[36:39]\n"
    "v_mfma_f32_16x16x32_bf16 v[40:43], v[204:207], v[168:171], v[40:43]\n"
    "v_mfma_f32_16x16x32_bf16 v[44:47], v[204:207], v[172:175], v[44:47]\n"
    "v_mfma_f32_16x16x32_bf16 v[48:51], v[208:211], v[168:171], v[48:51]\n"
    "v_mfma_f32_16x16x32_bf16 v[52:55], v[208:211], v[172:175], v[52:55]\n"
    "s_waitcnt lgkmcnt(0)\n"
    "v_mfma_f32_16x16x32_bf16 v[56:59], v[212:215], v[168:171], v[56:59]\n"
    "v_mfma_f32_16x16x32_bf16 v[60:63], v[212:215], v[172:175], v[60:63]\n"
    "s_waitcnt vmcnt(0) lgkmcnt(0)\n"
    "s_barrier\n"
    "s_add_u32 m0, s34, 0x12000\n"
    "s_add_u32 s42, s63, 0x0\n"
    "s_add_u32 s43, s63, 0x2400\n"
    "s_add_u32 s44, s63, 0x4800\n"
    "buffer_load_dwordx4 v254, s[24:27], s42 offen lds\n"
    "buffer_load_dwordx4 v254, s[24:27], s43 offen offset:1024 lds\n"
    "buffer_load_dwordx4 v254, s[24:27], s44 offen offset:2048 lds\n"
    "s_add_u32 m0, s34, 0x15000\n"
    "s_add_u32 s42, s63, 0x1e000\n"
    "s_add_u32 s43, s63, 0x20400\n"
    "s_add_u32 s44, s63, 0x22800\n"
    "buffer_load_dwordx4 v254, s[24:27], s42 offen lds\n"
    "buffer_load_dwordx4 v254, s[24:27], s43 offen offset:1024 lds\n"
    "buffer_load_dwordx4 v254, s[24:27], s44 offen offset:2048 lds\n"
    "v_mfma_f32_16x16x32_bf16 v[64:67], v[216:219], v[168:171], v[64:67]\n"
    "v_pk_mul_f32 v[0:1], v[0:1], s[48:49]\n"
    "v_pk_mul_f32 v[2:3], v[2:3], s[48:49]\n"
    "v_mfma_f32_16x16x32_bf16 v[68:71], v[216:219], v[172:175], v[68:71]\n"
    "v_pk_mul_f32 v[8:9], v[8:9], s[48:49]\n"
    "v_pk_mul_f32 v[10:11], v[10:11], s[48:49]\n"
    "v_mfma_f32_16x16x32_bf16 v[72:75], v[220:223], v[168:171], v[72:75]\n"
    "v_cvt_pk_bf16_f32 v0, v0, v1\n"
    "v_cvt_pk_bf16_f32 v2, v2, v3\n"
    "v_mfma_f32_16x16x32_bf16 v[76:79], v[220:223], v[172:175], v[76:79]\n"
    "v_cvt_pk_bf16_f32 v8, v8, v9\n"
    "v_cvt_pk_bf16_f32 v10, v10, v11\n"
    "v_mfma_f32_16x16x32_bf16 v[80:83], v[224:227], v[168:171], v[80:83]\n"
    "v_cndmask_b32_e64 v96, v0, v8, s[50:51]\n"
    "v_cndmask_b32_e64 v97, v2, v10, s[50:51]\n"
    "v_mfma_f32_16x16x32_bf16 v[84:87], v[224:227], v[172:175], v[84:87]\n"
    "v_cndmask_b32_e64 v98, v8, v0, s[50:51]\n"
    "v_cndmask_b32_e64 v99, v10, v2, s[50:51]\n"
    "v_mfma_f32_16x16x32_bf16 v[88:91], v[228:231], v[168:171], v[88:91]\n"
    "v_pk_mul_f32 v[4:5], v[4:5], s[80:81]\n"
    "v_pk_mul_f32 v[6:7], v[6:7], s[80:81]\n"
    "v_mfma_f32_16x16x32_bf16 v[92:95], v[228:231], v[172:175], v[92:95]\n"
    "v_pk_mul_f32 v[12:13], v[12:13], s[80:81]\n"
    "v_pk_mul_f32 v[14:15], v[14:15], s[80:81]\n"
    "v_cvt_pk_bf16_f32 v4, v4, v5\n"
    "v_cvt_pk_bf16_f32 v6, v6, v7\n"
    "v_cvt_pk_bf16_f32 v12, v12, v13\n"
    "v_cvt_pk_bf16_f32 v14, v14, v15\n"
    "v_cndmask_b32_e64 v100, v4, v12, s[50:51]\n"
    "v_cndmask_b32_e64 v101, v6, v14, s[50:51]\n"
    "v_cndmask_b32_e64 v102, v12, v4, s[50:51]\n"
    "v_cndmask_b32_e64 v103, v14, v6, s[50:51]\n"
    "v_pk_mul_f32 v[16:17], v[16:17], s[48:49]\n"
    "v_pk_mul_f32 v[18:19], v[18:19], s[48:49]\n"
    "v_pk_mul_f32 v[24:25], v[24:25], s[48:49]\n"
    "v_pk_mul_f32 v[26:27], v[26:27], s[48:49]\n"
    "v_cvt_pk_bf16_f32 v16, v16, v17\n"
    "v_cvt_pk_bf16_f32 v18, v18, v19\n"
    "v_cvt_pk_bf16_f32 v24, v24, v25\n"
    "v_cvt_pk_bf16_f32 v26, v26, v27\n"
    "v_cndmask_b32_e64 v104, v16, v24, s[50:51]\n"
    "v_cndmask_b32_e64 v105, v18, v26, s[50:51]\n"
    "v_cndmask_b32_e64 v106, v24, v16, s[50:51]\n"
    "v_cndmask_b32_e64 v107, v26, v18, s[50:51]\n"
    "v_pk_mul_f32 v[20:21], v[20:21], s[80:81]\n"
    "v_pk_mul_f32 v[22:23], v[22:23], s[80:81]\n"
    "v_pk_mul_f32 v[28:29], v[28:29], s[80:81]\n"
    "v_pk_mul_f32 v[30:31], v[30:31], s[80:81]\n"
    "v_cvt_pk_bf16_f32 v20, v20, v21\n"
    "v_cvt_pk_bf16_f32 v22, v22, v23\n"
    "v_cvt_pk_bf16_f32 v28, v28, v29\n"
    "v_cvt_pk_bf16_f32 v30, v30, v31\n"
    "v_cndmask_b32_e64 v108, v20, v28, s[50:51]\n"
    "v_cndmask_b32_e64 v109, v22, v30, s[50:51]\n"
    "v_cndmask_b32_e64 v110, v28, v20, s[50:51]\n"
    "v_cndmask_b32_e64 v111, v30, v22, s[50:51]\n"
    "v_pk_mul_f32 v[32:33], v[32:33], s[48:49]\n"
    "v_pk_mul_f32 v[34:35], v[34:35], s[48:49]\n"
    "v_pk_mul_f32 v[40:41], v[40:41], s[48:49]\n"
    "v_pk_mul_f32 v[42:43], v[42:43], s[48:49]\n"
    "v_cvt_pk_bf16_f32 v32, v32, v33\n"
    "v_cvt_pk_bf16_f32 v34, v34, v35\n"
    "v_cvt_pk_bf16_f32 v40, v40, v41\n"
    "v_cvt_pk_bf16_f32 v42, v42, v43\n"
    "v_cndmask_b32_e64 v112, v32, v40, s[50:51]\n"
    "v_cndmask_b32_e64 v113, v34, v42, s[50:51]\n"
    "v_cndmask_b32_e64 v114, v40, v32, s[50:51]\n"
    "v_cndmask_b32_e64 v115, v42, v34, s[50:51]\n"
    "v_pk_mul_f32 v[36:37], v[36:37], s[80:81]\n"
    "v_pk_mul_f32 v[38:39], v[38:39], s[80:81]\n"
    "v_pk_mul_f32 v[44:45], v[44:45], s[80:81]\n"
    "v_pk_mul_f32 v[46:47], v[46:47], s[80:81]\n"
    "v_cvt_pk_bf16_f32 v36, v36, v37\n"
    "v_cvt_pk_bf16_f32 v38, v38, v39\n"
    "v_cvt_pk_bf16_f32 v44, v44, v45\n"
    "v_cvt_pk_bf16_f32 v46, v46, v47\n"
    "v_cndmask_b32_e64 v116, v36, v44, s[50:51]\n"
    "v_cndmask_b32_e64 v117, v38, v46, s[50:51]\n"
    "v_cndmask_b32_e64 v118, v44, v36, s[50:51]\n"
    "v_cndmask_b32_e64 v119, v46, v38, s[50:51]\n"
    "v_pk_mul_f32 v[48:49], v[48:49], s[48:49]\n"
    "v_pk_mul_f32 v[50:51], v[50:51], s[48:49]\n"
    "v_pk_mul_f32 v[56:57], v[56:57], s[48:49]\n"
    "v_pk_mul_f32 v[58:59], v[58:59], s[48:49]\n"
    "v_cvt_pk_bf16_f32 v48, v48, v49\n"
    "v_cvt_pk_bf16_f32 v50, v50, v51\n"
    "v_cvt_pk_bf16_f32 v56, v56, v57\n"
    "v_cvt_pk_bf16_f32 v58, v58, v59\n"
    "v_cndmask_b32_e64 v120, v48, v56, s[50:51]\n"
    "v_cndmask_b32_e64 v121, v50, v58, s[50:51]\n"
    "v_cndmask_b32_e64 v122, v56, v48, s[50:51]\n"
    "v_cndmask_b32_e64 v123, v58, v50, s[50:51]\n"
    "v_pk_mul_f32 v[52:53], v[52:53], s[80:81]\n"
    "v_pk_mul_f32 v[54:55], v[54:55], s[80:81]\n"
    "v_pk_mul_f32 v[60:61], v[60:61], s[80:81]\n"
    "v_pk_mul_f32 v[62:63], v[62:63], s[80:81]\n"
    "v_cvt_pk_bf16_f32 v52, v52, v53\n"
    "v_cvt_pk_bf16_f32 v54, v54, v55\n"
    "v_cvt_pk_bf16_f32 v60, v60, v61\n"
    "v_cvt_pk_bf16_f32 v62, v62, v63\n"
    "v_cndmask_b32_e64 v124, v52, v60, s[50:51]\n"
    "v_cndmask_b32_e64 v125, v54, v62, s[50:51]\n"
    "v_cndmask_b32_e64 v126, v60, v52, s[50:51]\n"
    "v_cndmask_b32_e64 v127, v62, v54, s[50:51]\n"
    "v_pk_mul_f32 v[64:65], v[64:65], s[48:49]\n"
    "v_pk_mul_f32 v[66:67], v[66:67], s[48:49]\n"
    "v_pk_mul_f32 v[72:73], v[72:73], s[48:49]\n"
    "v_pk_mul_f32 v[74:75], v[74:75], s[48:49]\n"
    "v_cvt_pk_bf16_f32 v64, v64, v65\n"
    "v_cvt_pk_bf16_f32 v66, v66, v67\n"
    "v_cvt_pk_bf16_f32 v72, v72, v73\n"
    "v_cvt_pk_bf16_f32 v74, v74, v75\n"
    "v_cndmask_b32_e64 v128, v64, v72, s[50:51]\n"
    "v_cndmask_b32_e64 v129, v66, v74, s[50:51]\n"
    "v_cndmask_b32_e64 v130, v72, v64, s[50:51]\n"
    "v_cndmask_b32_e64 v131, v74, v66, s[50:51]\n"
    "v_pk_mul_f32 v[68:69], v[68:69], s[80:81]\n"
    "v_pk_mul_f32 v[70:71], v[70:71], s[80:81]\n"
    "v_pk_mul_f32 v[76:77], v[76:77], s[80:81]\n"
    "v_pk_mul_f32 v[78:79], v[78:79], s[80:81]\n"
    "v_cvt_pk_bf16_f32 v68, v68, v69\n"
    "v_cvt_pk_bf16_f32 v70, v70, v71\n"
    "v_cvt_pk_bf16_f32 v76, v76, v77\n"
    "v_cvt_pk_bf16_f32 v78, v78, v79\n"
    "v_cndmask_b32_e64 v132, v68, v76, s[50:51]\n"
    "v_cndmask_b32_e64 v133, v70, v78, s[50:51]\n"
    "v_cndmask_b32_e64 v134, v76, v68, s[50:51]\n"
    "v_cndmask_b32_e64 v135, v78, v70, s[50:51]\n"
    "v_pk_mul_f32 v[80:81], v[80:81], s[48:49]\n"
    "v_pk_mul_f32 v[82:83], v[82:83], s[48:49]\n"
    "v_pk_mul_f32 v[88:89], v[88:89], s[48:49]\n"
    "v_pk_mul_f32 v[90:91], v[90:91], s[48:49]\n"
    "v_cvt_pk_bf16_f32 v80, v80, v81\n"
    "v_cvt_pk_bf16_f32 v82, v82, v83\n"
    "v_cvt_pk_bf16_f32 v88, v88, v89\n"
    "v_cvt_pk_bf16_f32 v90, v90, v91\n"
    "v_cndmask_b32_e64 v136, v80, v88, s[50:51]\n"
    "v_cndmask_b32_e64 v137, v82, v90, s[50:51]\n"
    "v_cndmask_b32_e64 v138, v88, v80, s[50:51]\n"
    "v_cndmask_b32_e64 v139, v90, v82, s[50:51]\n"
    "v_pk_mul_f32 v[84:85], v[84:85], s[80:81]\n"
    "v_pk_mul_f32 v[86:87], v[86:87], s[80:81]\n"
    "v_pk_mul_f32 v[92:93], v[92:93], s[80:81]\n"
    "v_pk_mul_f32 v[94:95], v[94:95], s[80:81]\n"
    "v_cvt_pk_bf16_f32 v84, v84, v85\n"
    "v_cvt_pk_bf16_f32 v86, v86, v87\n"
    "v_cvt_pk_bf16_f32 v92, v92, v93\n"
    "v_cvt_pk_bf16_f32 v94, v94, v95\n"
    "v_cndmask_b32_e64 v140, v84, v92, s[50:51]\n"
    "v_cndmask_b32_e64 v141, v86, v94, s[50:51]\n"
    "v_cndmask_b32_e64 v142, v92, v84, s[50:51]\n"
    "v_cndmask_b32_e64 v143, v94, v86, s[50:51]\n"
    "s_waitcnt lgkmcnt(0)\n"
    "s_barrier\n"
    "s_add_u32 m0, s34, 0x18000\n"
    "s_add_u32 s42, s64, 0x0\n"
    "s_add_u32 s43, s64, 0x2400\n"
    "s_add_u32 s44, s64, 0x4800\n"
    "buffer_load_dwordx4 v254, s[24:27], s42 offen lds\n"
    "buffer_load_dwordx4 v254, s[24:27], s43 offen offset:1024 lds\n"
    "buffer_load_dwordx4 v254, s[24:27], s44 offen offset:2048 lds\n"
    "s_add_u32 m0, s34, 0x1b000\n"
    "s_add_u32 s42, s64, 0x1e000\n"
    "s_add_u32 s43, s64, 0x20400\n"
    "s_add_u32 s44, s64, 0x22800\n"
    "buffer_load_dwordx4 v254, s[24:27], s42 offen lds\n"
    "buffer_load_dwordx4 v254, s[24:27], s43 offen offset:1024 lds\n"
    "buffer_load_dwordx4 v254, s[24:27], s44 offen offset:2048 lds\n"
    "s_mov_b32 s36, 0x3000\n"
    "s_mov_b32 s37, 0x6000\n"
    "s_mov_b32 s38, 0\n"
    "s_mov_b32 s39, 0x36000\n"
    "s_mov_b32 s40, 0x30000\n"
    ".Lfz_ATT:\n"
    "v_add_u32_e32 v232, s36, v238\n"
    "v_xor_b32_e32 v233, 64, v232\n"
    "s_waitcnt lgkmcnt(0)\n"
    "s_barrier\n"
    "ds_read_b128 v[184:187], v232\n"
    "ds_read_b128 v[188:191], v232 offset:6144\n"
    "ds_read_b128 v[192:195], v233\n"
    "ds_read_b128 v[196:199], v233 offset:6144\n"
    "ds_read_b128 v[200:203], v232 offset:128\n"
    "ds_read_b128 v[204:207], v232 offset:6272\n"
    "ds_read_b128 v[208:211], v233 offset:128\n"
    "ds_read_b128 v[212:215], v233 offset:6272\n"
    "s_waitcnt lgkmcnt(7)\n"
    "v_mfma_f32_16x16x32_bf16 v[160:163], v[184:187], v[96:99], 0\n"
    "v_mov_b64_e32 v[0:1], 0\n"
    "v_mov_b64_e32 v[2:3], 0\n"
    "v_mfma_f32_16x16x32_bf16 v[164:167], v[184:187], v[100:103], 0\n"
    "s_add_u32 m0, s54, s37\n"
    "s_waitcnt lgkmcnt(6)\n"
    "v_mfma_f32_16x16x32_bf16 v[168:171], v[188:191], v[96:99], 0\n"
    "s_addk_i32 s40, 0x3000\n"
    "ds_read_b128 v[184:187], v232 offset:256\n"
    "v_mfma_f32_16x16x32_bf16 v[172:175], v[188:191], v[100:103], 0\n"
    "ds_read_b128 v[188:191], v232 offset:6400\n"
    "s_waitcnt lgkmcnt(2)\n"
    "v_mfma_f32_16x16x32_bf16 v[160:163], v[192:195], v[104:107], v[160:163]\n"
    "buffer_load_dwordx4 v251, s[20:23], s40 offen nt lds\n"
    "v_mfma_f32_16x16x32_bf16 v[164:167], v[192:195], v[108:111], v[164:167]\n"
    "buffer_load_dwordx4 v252, s[20:23], s40 offen offset:1024 nt lds\n"
    "v_mfma_f32_16x16x32_bf16 v[168:171], v[196:199], v[104:107], v[168:171]\n"
    "buffer_load_dwordx4 v253, s[20:23], s40 offen offset:2048 nt lds\n"
    "v_mfma_f32_16x16x32_bf16 v[172:175], v[196:199], v[108:111], v[172:175]\n"
    "ds_read_b128 v[192:195], v233 offset:256\n"
    "ds_read_b128 v[196:199], v233 offset:6400\n"
    "v_mfma_f32_16x16x32_bf16 v[160:163], v[200:203], v[112:115], v[160:163]\n"
    "v_mov_b64_e32 v[4:5], 0\n"
    "v_mov_b64_e32 v[6:7], 0\n"
    "v_mfma_f32_16x16x32_bf16 v[164:167], v[200:203], v[116:119], v[164:167]\n"
    "v_mov_b64_e32 v[8:9], 0\n"
    "v_mov_b64_e32 v[10:11], 0\n"
    "v_mfma_f32_16x16x32_bf16 v[168:171], v[204:207], v[112:115], v[168:171]\n"
    "v_mov_b64_e32 v[12:13], 0\n"
    "v_mov_b64_e32 v[14:15], 0\n"
    "v_mfma_f32_16x16x32_bf16 v[172:175], v[204:207], v[116:119], v[172:175]\n"
    "v_mov_b64_e32 v[16:17], 0\n"
    "v_mov_b64_e32 v[18:19], 0\n"
    "v_mfma_f32_16x16x32_bf16 v[160:163], v[208:211], v[120:123], v[160:163]\n"
    "v_mov_b64_e32 v[20:21], 0\n"
    "v_mov_b64_e32 v[22:23], 0\n"
    "v_mfma_f32_16x16x32_bf16 v[164:167], v[208:211], v[124:127], v[164:167]\n"
    "v_mov_b64_e32 v[24:25], 0\n"
    "v_mov_b64_e32 v[26:27], 0\n"
    "v_mfma_f32_16x16x32_bf16 v[168:171], v[212:215], v[120:123], v[168:171]\n"
    "v_mov_b64_e32 v[28:29], 0\n"
    "v_mov_b64_e32 v[30:31], 0\n"
    "v_mfma_f32_16x16x32_bf16 v[172:175], v[212:215], v[124:127], v[172:175]\n"
    "v_mov_b64_e32 v[32:33], 0\n"
    "v_mov_b64_e32 v[34:35], 0\n"
    "v_mov_b64_e32 v[36:37], 0\n"
    "v_mov_b64_e32 v[38:39], 0\n"
    "v_mov_b64_e32 v[40:41], 0\n"
    "v_mov_b64_e32 v[42:43], 0\n"
    "v_mov_b64_e32 v[44:45], 0\n"
    "v_mov_b64_e32 v[46:47], 0\n"
    "v_mov_b64_e32 v[48:49], 0\n"
    "v_mov_b64_e32 v[50:51], 0\n"
    "v_mov_b64_e32 v[52:53], 0\n"
    "v_mov_b64_e32 v[54:55], 0\n"
    "v_mov_b64_e32 v[56:57], 0\n"
    "v_mov_b64_e32 v[58:59], 0\n"
    "v_mov_b64_e32 v[60:61], 0\n"
    "v_mov_b64_e32 v[62:63], 0\n"
    "v_mov_b64_e32 v[64:65], 0\n"
    "v_mov_b64_e32 v[66:67], 0\n"
    "v_mov_b64_e32 v[68:69], 0\n"
    "v_mov_b64_e32 v[70:71], 0\n"
    "v_mov_b64_e32 v[72:73], 0\n"
    "v_mov_b64_e32 v[74:75], 0\n"
    "v_mov_b64_e32 v[76:77], 0\n"
    "v_mov_b64_e32 v[78:79], 0\n"
    "v_mov_b64_e32 v[80:81], 0\n"
    "v_mov_b64_e32 v[82:83], 0\n"
    "v_mov_b64_e32 v[84:85], 0\n"
    "v_mov_b64_e32 v[86:87], 0\n"
    "v_mov_b64_e32 v[88:89], 0\n"
    "v_mov_b64_e32 v[90:91], 0\n"
    "v_mov_b64_e32 v[92:93], 0\n"
    "v_mov_b64_e32 v[94:95], 0\n"
    "v_mov_b64_e32 v[176:177], 0\n"
    "v_mov_b64_e32 v[178:179], 0\n"
    "v_mov_b64_e32 v[180:181], 0\n"
    "v_mov_b64_e32 v[182:183], 0\n"
    "v_mov_b64_e32 v[224:225], 0\n"
    "v_mov_b32_e32 v225, s83\n"
    "s_waitcnt vmcnt(3) lgkmcnt(0)\n"
    "s_barrier\n"
    "v_mfma_f32_16x16x32_bf16 v[160:163], v[184:187], v[128:131], v[160:163]\n"
    "s_add_u32 m0, s34, s36\n"
    "s_addk_i32 s39, 0x3000\n"
    "v_mfma_f32_16x16x32_bf16 v[164:167], v[184:187], v[132:135], v[164:167]\n"
    "buffer_load_dwordx4 v248, s[16:19], s39 offen nt lds\n"
    "v_mfma_f32_16x16x32_bf16 v[168:171], v[188:191], v[128:131], v[168:171]\n"
    "buffer_load_dwordx4 v249, s[16:19], s39 offen offset:1024 nt lds\n"
    "v_mfma_f32_16x16x32_bf16 v[172:175], v[188:191], v[132:135], v[172:175]\n"
    "buffer_load_dwordx4 v250, s[16:19], s39 offen offset:2048 nt lds\n"
    "v_mfma_f32_16x16x32_bf16 v[160:163], v[192:195], v[136:139], v[160:163]\n"
    "v_add_u32_e32 v232, s37, v238\n"
    "v_xor_b32_e32 v233, 64, v232\n"
    "v_mfma_f32_16x16x32_bf16 v[164:167], v[192:195], v[140:143], v[164:167]\n"
    "v_mfma_f32_16x16x32_bf16 v[168:171], v[196:199], v[136:139], v[168:171]\n"
    "v_mfma_f32_16x16x32_bf16 v[172:175], v[196:199], v[140:143], v[172:175]\n"
    "s_nop 5\n"
    "v_max3_f32 v226, v160, v161, v162\n"
    "v_max3_f32 v227, v164, v165, v166\n"
    "v_max3_f32 v226, v226, v163, v168\n"
    "v_max3_f32 v227, v227, v167, v172\n"
    "v_max3_f32 v226, v226, v169, v170\n"
    "v_max3_f32 v227, v227, v173, v174\n"
    "v_max_f32_e32 v226, v226, v171\n"
    "v_max_f32_e32 v227, v227, v175\n"
    "v_mov_b32_e32 v228, v226\n"
    "v_mov_b32_e32 v229, v227\n"
    "s_nop 0\n"
    "v_permlane32_swap_b32_e32 v226, v228\n"
    "v_permlane32_swap_b32_e32 v227, v229\n"
    "v_max_f32_e32 v226, v226, v228\n"
    "v_max_f32_e32 v227, v227, v229\n"
    "v_mov_b32_e32 v228, v226\n"
    "v_mov_b32_e32 v229, v227\n"
    "s_nop 0\n"
    "v_permlane16_swap_b32_e32 v226, v228\n"
    "v_permlane16_swap_b32_e32 v227, v229\n"
    "v_max_f32_e32 v226, v226, v228\n"
    "v_max_f32_e32 v227, v227, v229\n"
    "v_add_f32_e32 v227, s82, v227\n"
    "v_sub_f32_e32 v160, v160, v226\n"
    "v_sub_f32_e32 v161, v161, v226\n"
    "v_sub_f32_e32 v162, v162, v226\n"
    "v_sub_f32_e32 v163, v163, v226\n"
    "v_sub_f32_e32 v168, v168, v226\n"
    "v_sub_f32_e32 v169, v169, v226\n"
    "v_sub_f32_e32 v170, v170, v226\n"
    "v_sub_f32_e32 v171, v171, v226\n"
    "v_sub_f32_e32 v216, 0, v226\n"
    "v_sub_f32_e32 v217, 0, v226\n"
    "v_sub_f32_e32 v218, 0, v226\n"
    "v_sub_f32_e32 v219, 0, v226\n"
    "v_sub_f32_e32 v164, v164, v227\n"
    "v_sub_f32_e32 v165, v165, v227\n"
    "v_sub_f32_e32 v166, v166, v227\n"
    "v_sub_f32_e32 v167, v167, v227\n"
    "v_sub_f32_e32 v172, v172, v227\n"
    "v_sub_f32_e32 v173, v173, v227\n"
    "v_sub_f32_e32 v174, v174, v227\n"
    "v_sub_f32_e32 v175, v175, v227\n"
    "v_sub_f32_e32 v220, 0, v227\n"
    "v_sub_f32_e32 v221, 0, v227\n"
    "v_sub_f32_e32 v222, 0, v227\n"
    "v_sub_f32_e32 v223, 0, v227\n"
    "s_mov_b64 vcc, 0\n"
    "s_mov_b32 s41, 8\n"
    "s_branch .Lfz_H2\n"
    ".Lfz_H1:\n"
    "s_waitcnt vmcnt(12) lgkmcnt(0)\n"
    "s_barrier\n"
    "ds_read_b128 v[184:187], v232\n"
    "ds_read_b128 v[188:191], v232 offset:6144\n"
    "ds_read_b128 v[192:195], v233\n"
    "ds_read_b128 v[196:199], v233 offset:6144\n"
    "v_mfma_f32_16x16x32_bf16 v[64:67], v[200:203], v[176:179], v[64:67]\n"
    "v_add_u32_e32 v234, s36, v239\n"
    "v_xor_b32_e64 v235, 32, v234\n"
    "v_mfma_f32_16x16x32_bf16 v[68:71], v[200:203], v[180:183], v[68:71]\n"
    "ds_read_b128 v[200:203], v232 offset:128\n"
    "s_add_u32 m0, s54, s38\n"
    "v_mfma_f32_16x16x32_bf16 v[72:75], v[204:207], v[176:179], v[72:75]\n"
    "s_addk_i32 s40, 0x3000\n"
    "v_xor_b32_e64 v236, 64, v234\n"
    "v_mfma_f32_16x16x32_bf16 v[76:79], v[204:207], v[180:183], v[76:79]\n"
    "ds_read_b128 v[204:207], v232 offset:6272\n"
    "v_xor_b32_e64 v237, 32, v236\n"
    "v_mfma_f32_16x16x32_bf16 v[80:83], v[208:211], v[176:179], v[80:83]\n"
    "buffer_load_dwordx4 v251, s[20:23], s40 offen nt lds\n"
    "v_mfma_f32_16x16x32_bf16 v[84:87], v[208:211], v[180:183], v[84:87]\n"
    "ds_read_b128 v[208:211], v233 offset:128\n"
    "v_mfma_f32_16x16x32_bf16 v[88:91], v[212:215], v[176:179], v[88:91]\n"
    "buffer_load_dwordx4 v252, s[20:23], s40 offen offset:1024 nt lds\n"
    "v_mfma_f32_16x16x32_bf16 v[92:95], v[212:215], v[180:183], v[92:95]\n"
    "ds_read_b128 v[212:215], v233 offset:6272\n"
    "buffer_load_dwordx4 v253, s[20:23], s40 offen offset:2048 nt lds\n"
    "s_cbranch_vccnz .Lfz_R1\n"
    ".Lfz_RR1:\n"
    "s_waitcnt lgkmcnt(2)\n"
    "v_mfma_f32_16x16x32_bf16 v[160:163], v[184:187], v[96:99], v[216:219]\n"
    "v_exp_f32_e64 v144, v144\n"
    "v_mfma_f32_16x16x32_bf16 v[164:167], v[184:187], v[100:103], v[220:223]\n"
    "ds_read_b128 v[184:187], v232 offset:256\n"
    "v_exp_f32_e64 v145, v145\n"
    "v_mfma_f32_16x16x32_bf16 v[168:171], v[188:191], v[96:99], v[216:219]\n"
    "v_exp_f32_e32 v148, v148\n"
    "v_add_f32_e32 v224, v144, v224\n"
    "v_mfma_f32_16x16x32_bf16 v[172:175], v[188:191], v[100:103], v[220:223]\n"
    "ds_read_b128 v[188:191], v232 offset:6400\n"
    "v_exp_f32_e64 v149, v149\n"
    "v_mfma_f32_16x16x32_bf16 v[160:163], v[192:195], v[104:107], v[160:163]\n"
    "v_add_f32_e64 v224, v145, v224\n"
    "v_cvt_pk_bf16_f32 v176, v144, v145\n"
    "v_mfma_f32_16x16x32_bf16 v[164:167], v[192:195], v[108:111], v[164:167]\n"
    "ds_read_b128 v[192:195], v233 offset:256\n"
    "v_exp_f32_e64 v146, v146\n"
    "v_mfma_f32_16x16x32_bf16 v[168:171], v[196:199], v[104:107], v[168:171]\n"
    "v_add_f32_e32 v225, v148, v225\n"
    "v_exp_f32_e32 v147, v147\n"
    "v_mfma_f32_16x16x32_bf16 v[172:175], v[196:199], v[108:111], v[172:175]\n"
    "ds_read_b128 v[196:199], v233 offset:6400\n"
    "v_add_f32_e64 v225, v149, v225\n"
    "v_cvt_pk_bf16_f32 v180, v148, v149\n"
    "v_mfma_f32_16x16x32_bf16 v[160:163], v[200:203], v[112:115], v[160:163]\n"
    "v_exp_f32_e32 v150, v150\n"
    "v_add_f32_e32 v224, v146, v224\n"
    "v_mfma_f32_16x16x32_bf16 v[164:167], v[200:203], v[116:119], v[164:167]\n"
    "v_exp_f32_e32 v151, v151\n"
    "v_add_f32_e32 v224, v147, v224\n"
    "v_mfma_f32_16x16x32_bf16 v[168:171], v[204:207], v[112:115], v[168:171]\n"
    "v_cvt_pk_bf16_f32 v177, v146, v147\n"
    "v_exp_f32_e64 v152, v152\n"
    "v_mfma_f32_16x16x32_bf16 v[172:175], v[204:207], v[116:119], v[172:175]\n"
    "v_add_f32_e32 v225, v150, v225\n"
    "s_waitcnt lgkmcnt(0)\n"
    "v_mfma_f32_16x16x32_bf16 v[160:163], v[208:211], v[120:123], v[160:163]\n"
    "v_exp_f32_e32 v153, v153\n"
    "v_add_f32_e32 v225, v151, v225\n"
    "v_mfma_f32_16x16x32_bf16 v[164:167], v[208:211], v[124:127], v[164:167]\n"
    "v_cvt_pk_bf16_f32 v181, v150, v151\n"
    "v_exp_f32_e64 v156, v156\n"
    "v_mfma_f32_16x16x32_bf16 v[168:171], v[212:215], v[120:123], v[168:171]\n"
    "v_add_f32_e32 v224, v152, v224\n"
    "v_exp_f32_e32 v157, v157\n"
    "v_mfma_f32_16x16x32_bf16 v[172:175], v[212:215], v[124:127], v[172:175]\n"
    "v_add_f32_e64 v224, v153, v224\n"
    "v_cvt_pk_bf16_f32 v178, v152, v153\n"
    ".Lfz_RT1:\n"
    "s_memrealtime s[52:53]\n"
    "s_waitcnt lgkmcnt(0)\n"
    "s_sub_u32 s58, s73, s52\n"
    "s_cmp_gt_i32 s58, 0\n"
    "s_cbranch_scc1 .Lfz_RT1\n"
    "s_max_i32 s99, s99, s58\n"
    "s_sub_i32 s61, s61, s99\n"
    "s_movk_i32 s99, -55\n"
    "s_addk_i32 s61, -25\n"
    "s_max_i32 s61, s61, 0\n"
    "s_cmpk_gt_i32 s61, 90\n"
    "s_cselect_b32 s60, s62, s60\n"
    "s_max_i32 s58, s58, -4\n"
    "s_add_u32 s73, s52, s58\n"
    "s_add_u32 s73, s73, s60\n"
    "s_waitcnt vmcnt(12) lgkmcnt(0)\n"
    "s_barrier\n"
    "ds_read_b64_tr_b16 v[200:201], v234\n"
    "ds_read_b64_tr_b16 v[202:203], v234 offset:6144\n"
    "ds_read_b64_tr_b16 v[204:205], v235\n"
    "ds_read_b64_tr_b16 v[206:207], v235 offset:6144\n"
    "v_mfma_f32_16x16x32_bf16 v[160:163], v[184:187], v[128:131], v[160:163]\n"
    "ds_read_b64_tr_b16 v[208:209], v236\n"
    "ds_read_b64_tr_b16 v[210:211], v236 offset:6144\n"
    "v_mfma_f32_16x16x32_bf16 v[164:167], v[184:187], v[132:135], v[164:167]\n"
    "ds_read_b64_tr_b16 v[212:213], v237\n"
    "ds_read_b64_tr_b16 v[214:215], v237 offset:6144\n"
    "v_mfma_f32_16x16x32_bf16 v[168:171], v[188:191], v[128:131], v[168:171]\n"
    "v_exp_f32_e64 v154, v154\n"
    "v_mfma_f32_16x16x32_bf16 v[172:175], v[188:191], v[132:135], v[172:175]\n"
    "v_add_f32_e64 v225, v156, v225\n"
    "ds_read_b64_tr_b16 v[184:185], v234 offset:128\n"
    "v_mfma_f32_16x16x32_bf16 v[160:163], v[192:195], v[136:139], v[160:163]\n"
    "v_exp_f32_e64 v155, v155\n"
    "v_mfma_f32_16x16x32_bf16 v[164:167], v[192:195], v[140:143], v[164:167]\n"
    "v_add_f32_e64 v225, v157, v225\n"
    "v_cvt_pk_bf16_f32 v182, v156, v157\n"
    "v_exp_f32_e32 v158, v158\n"
    "v_add_f32_e32 v224, v154, v224\n"
    "v_exp_f32_e32 v159, v159\n"
    "v_add_f32_e32 v224, v155, v224\n"
    "v_cvt_pk_bf16_f32 v179, v154, v155\n"
    "v_add_f32_e32 v225, v158, v225\n"
    "v_add_f32_e32 v225, v159, v225\n"
    "v_cvt_pk_bf16_f32 v183, v158, v159\n"
    "v_mfma_f32_16x16x32_bf16 v[168:171], v[196:199], v[136:139], v[168:171]\n"
    "ds_read_b64_tr_b16 v[186:187], v234 offset:6272\n"
    "ds_read_b64_tr_b16 v[188:189], v235 offset:128\n"
    "v_mfma_f32_16x16x32_bf16 v[172:175], v[196:199], v[140:143], v[172:175]\n"
    "ds_read_b64_tr_b16 v[190:191], v235 offset:6272\n"
    "s_waitcnt lgkmcnt(3)\n"
    "v_mfma_f32_16x16x32_bf16 v[0:3], v[200:203], v[176:179], v[0:3]\n"
    "ds_read_b64_tr_b16 v[192:193], v236 offset:128\n"
    "ds_read_b64_tr_b16 v[194:195], v236 offset:6272\n"
    "v_mfma_f32_16x16x32_bf16 v[4:7], v[200:203], v[180:183], v[4:7]\n"
    "ds_read_b64_tr_b16 v[196:197], v237 offset:128\n"
    "ds_read_b64_tr_b16 v[198:199], v237 offset:6272\n"
    "v_mfma_f32_16x16x32_bf16 v[8:11], v[204:207], v[176:179], v[8:11]\n"
    "ds_read_b64_tr_b16 v[200:201], v234 offset:256\n"
    "ds_read_b64_tr_b16 v[202:203], v234 offset:6400\n"
    "v_mfma_f32_16x16x32_bf16 v[12:15], v[204:207], v[180:183], v[12:15]\n"
    "ds_read_b64_tr_b16 v[204:205], v235 offset:256\n"
    "ds_read_b64_tr_b16 v[206:207], v235 offset:6400\n"
    "v_mfma_f32_16x16x32_bf16 v[16:19], v[208:211], v[176:179], v[16:19]\n"
    "s_add_u32 m0, s34, s37\n"
    "s_addk_i32 s39, 0x3000\n"
    "v_mfma_f32_16x16x32_bf16 v[20:23], v[208:211], v[180:183], v[20:23]\n"
    "ds_read_b64_tr_b16 v[208:209], v236 offset:256\n"
    "ds_read_b64_tr_b16 v[210:211], v236 offset:6400\n"
    "v_mfma_f32_16x16x32_bf16 v[24:27], v[212:215], v[176:179], v[24:27]\n"
    "buffer_load_dwordx4 v248, s[16:19], s39 offen nt lds\n"
    "v_mfma_f32_16x16x32_bf16 v[28:31], v[212:215], v[180:183], v[28:31]\n"
    "ds_read_b64_tr_b16 v[212:213], v237 offset:256\n"
    "s_waitcnt lgkmcnt(3)\n"
    "v_mfma_f32_16x16x32_bf16 v[32:35], v[184:187], v[176:179], v[32:35]\n"
    "ds_read_b64_tr_b16 v[214:215], v237 offset:6400\n"
    "v_max3_f32 v226, v160, v161, v162\n"
    "v_mfma_f32_16x16x32_bf16 v[36:39], v[184:187], v[180:183], v[36:39]\n"
    "buffer_load_dwordx4 v249, s[16:19], s39 offen offset:1024 nt lds\n"
    "v_mfma_f32_16x16x32_bf16 v[40:43], v[188:191], v[176:179], v[40:43]\n"
    "buffer_load_dwordx4 v250, s[16:19], s39 offen offset:2048 nt lds\n"
    "v_mfma_f32_16x16x32_bf16 v[44:47], v[188:191], v[180:183], v[44:47]\n"
    "v_add_u32_e32 v232, s38, v238\n"
    "v_xor_b32_e32 v233, 64, v232\n"
    "v_mfma_f32_16x16x32_bf16 v[48:51], v[192:195], v[176:179], v[48:51]\n"
    "s_mov_b32 s58, s36\n"
    "s_mov_b32 s36, s37\n"
    "v_mfma_f32_16x16x32_bf16 v[52:55], v[192:195], v[180:183], v[52:55]\n"
    "s_mov_b32 s37, s38\n"
    "s_mov_b32 s38, s58\n"
    "v_mfma_f32_16x16x32_bf16 v[56:59], v[196:199], v[176:179], v[56:59]\n"
    "v_max3_f32 v227, v164, v165, v166\n"
    "v_max3_f32 v226, v226, v163, v168\n"
    "v_mfma_f32_16x16x32_bf16 v[60:63], v[196:199], v[180:183], v[60:63]\n"
    "v_max3_f32 v227, v227, v167, v172\n"
    "v_max3_f32 v226, v226, v169, v170\n"
    "v_max3_f32 v227, v227, v173, v174\n"
    "v_max_f32_e32 v226, v226, v171\n"
    "v_max_f32_e32 v227, v227, v175\n"
    "v_max_f32_e32 v228, v226, v227\n"
    "v_cmp_lt_f32_e32 vcc, s46, v228\n"
    ".Lfz_H2:\n"
    "s_waitcnt vmcnt(12) lgkmcnt(0)\n"
    "s_barrier\n"
    "ds_read_b128 v[184:187], v232\n"
    "ds_read_b128 v[188:191], v232 offset:6144\n"
    "ds_read_b128 v[192:195], v233\n"
    "ds_read_b128 v[196:199], v233 offset:6144\n"
    "v_mfma_f32_16x16x32_bf16 v[64:67], v[200:203], v[176:179], v[64:67]\n"
    "v_add_u32_e32 v234, s36, v239\n"
    "v_xor_b32_e32 v235, 32, v234\n"
    "v_mfma_f32_16x16x32_bf16 v[68:71], v[200:203], v[180:183], v[68:71]\n"
    "ds_read_b128 v[200:203], v232 offset:128\n"
    "s_add_u32 m0, s54, s38\n"
    "v_mfma_f32_16x16x32_bf16 v[72:75], v[204:207], v[176:179], v[72:75]\n"
    "s_addk_i32 s40, 0x3000\n"
    "v_xor_b32_e64 v236, 64, v234\n"
    "v_mfma_f32_16x16x32_bf16 v[76:79], v[204:207], v[180:183], v[76:79]\n"
    "ds_read_b128 v[204:207], v232 offset:6272\n"
    "v_xor_b32_e64 v237, 32, v236\n"
    "v_mfma_f32_16x16x32_bf16 v[80:83], v[208:211], v[176:179], v[80:83]\n"
    "buffer_load_dwordx4 v251, s[20:23], s40 offen nt lds\n"
    "v_mfma_f32_16x16x32_bf16 v[84:87], v[208:211], v[180:183], v[84:87]\n"
    "ds_read_b128 v[208:211], v233 offset:128\n"
    "v_mfma_f32_16x16x32_bf16 v[88:91], v[212:215], v[176:179], v[88:91]\n"
    "buffer_load_dwordx4 v252, s[20:23], s40 offen offset:1024 nt lds\n"
    "v_mfma_f32_16x16x32_bf16 v[92:95], v[212:215], v[180:183], v[92:95]\n"
    "ds_read_b128 v[212:215], v233 offset:6272\n"
    "buffer_load_dwordx4 v253, s[20:23], s40 offen offset:2048 nt lds\n"
    "s_cbranch_vccnz .Lfz_R2\n"
    ".Lfz_RR2:\n"
    "s_waitcnt lgkmcnt(2)\n"
    "v_mfma_f32_16x16x32_bf16 v[144:147], v[184:187], v[96:99], v[216:219]\n"
    "v_exp_f32_e64 v160, v160\n"
    "v_mfma_f32_16x16x32_bf16 v[148:151], v[184:187], v[100:103], v[220:223]\n"
    "ds_read_b128 v[184:187], v232 offset:256\n"
    "v_exp_f32_e64 v161, v161\n"
    "v_mfma_f32_16x16x32_bf16 v[152:155], v[188:191], v[96:99], v[216:219]\n"
    "v_exp_f32_e32 v164, v164\n"
    "v_add_f32_e32 v224, v160, v224\n"
    "v_mfma_f32_16x16x32_bf16 v[156:159], v[188:191], v[100:103], v[220:223]\n"
    "ds_read_b128 v[188:191], v232 offset:6400\n"
    "v_exp_f32_e64 v165, v165\n"
    "v_mfma_f32_16x16x32_bf16 v[144:147], v[192:195], v[104:107], v[144:147]\n"
    "v_add_f32_e64 v224, v161, v224\n"
    "v_cvt_pk_bf16_f32 v176, v160, v161\n"
    "v_mfma_f32_16x16x32_bf16 v[148:151], v[192:195], v[108:111], v[148:151]\n"
    "ds_read_b128 v[192:195], v233 offset:256\n"
    "v_exp_f32_e64 v162, v162\n"
    "v_mfma_f32_16x16x32_bf16 v[152:155], v[196:199], v[104:107], v[152:155]\n"
    "v_add_f32_e32 v225, v164, v225\n"
    "v_exp_f32_e32 v163, v163\n"
    "v_mfma_f32_16x16x32_bf16 v[156:159], v[196:199], v[108:111], v[156:159]\n"
    "ds_read_b128 v[196:199], v233 offset:6400\n"
    "v_add_f32_e64 v225, v165, v225\n"
    "v_cvt_pk_bf16_f32 v180, v164, v165\n"
    "v_mfma_f32_16x16x32_bf16 v[144:147], v[200:203], v[112:115], v[144:147]\n"
    "v_exp_f32_e32 v166, v166\n"
    "v_add_f32_e32 v224, v162, v224\n"
    "v_mfma_f32_16x16x32_bf16 v[148:151], v[200:203], v[116:119], v[148:151]\n"
    "v_exp_f32_e32 v167, v167\n"
    "v_add_f32_e32 v224, v163, v224\n"
    "v_mfma_f32_16x16x32_bf16 v[152:155], v[204:207], v[112:115], v[152:155]\n"
    "v_cvt_pk_bf16_f32 v177, v162, v163\n"
    "v_exp_f32_e64 v168, v168\n"
    "v_mfma_f32_16x16x32_bf16 v[156:159], v[204:207], v[116:119], v[156:159]\n"
    "v_add_f32_e32 v225, v166, v225\n"
    "s_waitcnt lgkmcnt(0)\n"
    "v_mfma_f32_16x16x32_bf16 v[144:147], v[208:211], v[120:123], v[144:147]\n"
    "v_exp_f32_e32 v169, v169\n"
    "v_add_f32_e32 v225, v167, v225\n"
    "v_mfma_f32_16x16x32_bf16 v[148:151], v[208:211], v[124:127], v[148:151]\n"
    "v_cvt_pk_bf16_f32 v181, v166, v167\n"
    "v_exp_f32_e64 v172, v172\n"
    "v_mfma_f32_16x16x32_bf16 v[152:155], v[212:215], v[120:123], v[152:155]\n"
    "v_add_f32_e32 v224, v168, v224\n"
    "v_exp_f32_e32 v173, v173\n"
    "v_mfma_f32_16x16x32_bf16 v[156:159], v[212:215], v[124:127], v[156:159]\n"
    "v_add_f32_e64 v224, v169, v224\n"
    "v_cvt_pk_bf16_f32 v178, v168, v169\n"
    ".Lfz_RT2:\n"
    "s_memrealtime s[52:53]\n"
    "s_waitcnt lgkmcnt(0)\n"
    "s_sub_u32 s58, s73, s52\n"
    "s_cmp_gt_i32 s58, 0\n"
    "s_cbranch_scc1 .Lfz_RT2\n"
    "s_max_i32 s99, s99, s58\n"
    "s_sub_i32 s61, s61, s99\n"
    "s_movk_i32 s99, -55\n"
    "s_addk_i32 s61, -25\n"
    "s_max_i32 s61, s61, 0\n"
    "s_cmpk_gt_i32 s61, 90\n"
    "s_cselect_b32 s60, s62, s60\n"
    "s_max_i32 s58, s58, -4\n"
    "s_add_u32 s73, s52, s58\n"
    "s_add_u32 s73, s73, s60\n"
    "s_waitcnt vmcnt(12) lgkmcnt(0)\n"
    "s_barrier\n"
    "ds_read_b64_tr_b16 v[200:201], v234\n"
    "ds_read_b64_tr_b16 v[202:203], v234 offset:6144\n"
    "ds_read_b64_tr_b16 v[204:205], v235\n"
    "ds_read_b64_tr_b16 v[206:207], v235 offset:6144\n"
    "v_mfma_f32_16x16x32_bf16 v[144:147], v[184:187], v[128:131], v[144:147]\n"
    "ds_read_b64_tr_b16 v[208:209], v236\n"
    "ds_read_b64_tr_b16 v[210:211], v236 offset:6144\n"
    "v_mfma_f32_16x16x32_bf16 v[148:151], v[184:187], v[132:135], v[148:151]\n"
    "ds_read_b64_tr_b16 v[212:213], v237\n"
    "ds_read_b64_tr_b16 v[214:215], v237 offset:6144\n"
    "v_mfma_f32_16x16x32_bf16 v[152:155], v[188:191], v[128:131], v[152:155]\n"
    "v_exp_f32_e64 v170, v170\n"
    "v_mfma_f32_16x16x32_bf16 v[156:159], v[188:191], v[132:135], v[156:159]\n"
    "v_add_f32_e64 v225, v172, v225\n"
    "ds_read_b64_tr_b16 v[184:185], v234 offset:128\n"
    "v_mfma_f32_16x16x32_bf16 v[144:147], v[192:195], v[136:139], v[144:147]\n"
    "v_exp_f32_e64 v171, v171\n"
    "v_mfma_f32_16x16x32_bf16 v[148:151], v[192:195], v[140:143], v[148:151]\n"
    "v_add_f32_e64 v225, v173, v225\n"
    "v_cvt_pk_bf16_f32 v182, v172, v173\n"
    "v_exp_f32_e32 v174, v174\n"
    "v_add_f32_e32 v224, v170, v224\n"
    "v_exp_f32_e32 v175, v175\n"
    "v_add_f32_e32 v224, v171, v224\n"
    "v_cvt_pk_bf16_f32 v179, v170, v171\n"
    "v_add_f32_e32 v225, v174, v225\n"
    "v_add_f32_e32 v225, v175, v225\n"
    "v_cvt_pk_bf16_f32 v183, v174, v175\n"
    "v_mfma_f32_16x16x32_bf16 v[152:155], v[196:199], v[136:139], v[152:155]\n"
    "ds_read_b64_tr_b16 v[186:187], v234 offset:6272\n"
    "ds_read_b64_tr_b16 v[188:189], v235 offset:128\n"
    "v_mfma_f32_16x16x32_bf16 v[156:159], v[196:199], v[140:143], v[156:159]\n"
    "ds_read_b64_tr_b16 v[190:191], v235 offset:6272\n"
    "s_waitcnt lgkmcnt(3)\n"
    "v_mfma_f32_16x16x32_bf16 v[0:3], v[200:203], v[176:179], v[0:3]\n"
    "ds_read_b64_tr_b16 v[192:193], v236 offset:128\n"
    "ds_read_b64_tr_b16 v[194:195], v236 offset:6272\n"
    "v_mfma_f32_16x16x32_bf16 v[4:7], v[200:203], v[180:183], v[4:7]\n"
    "ds_read_b64_tr_b16 v[196:197], v237 offset:128\n"
    "ds_read_b64_tr_b16 v[198:199], v237 offset:6272\n"
    "v_mfma_f32_16x16x32_bf16 v[8:11], v[204:207], v[176:179], v[8:11]\n"
    "ds_read_b64_tr_b16 v[200:201], v234 offset:256\n"
    "ds_read_b64_tr_b16 v[202:203], v234 offset:6400\n"
    "v_mfma_f32_16x16x32_bf16 v[12:15], v[204:207], v[180:183], v[12:15]\n"
    "ds_read_b64_tr_b16 v[204:205], v235 offset:256\n"
    "ds_read_b64_tr_b16 v[206:207], v235 offset:6400\n"
    "v_mfma_f32_16x16x32_bf16 v[16:19], v[208:211], v[176:179], v[16:19]\n"
    "s_add_u32 m0, s34, s37\n"
    "s_addk_i32 s39, 0x3000\n"
    "v_mfma_f32_16x16x32_bf16 v[20:23], v[208:211], v[180:183], v[20:23]\n"
    "ds_read_b64_tr_b16 v[208:209], v236 offset:256\n"
    "ds_read_b64_tr_b16 v[210:211], v236 offset:6400\n"
    "v_mfma_f32_16x16x32_bf16 v[24:27], v[212:215], v[176:179], v[24:27]\n"
    "buffer_load_dwordx4 v248, s[16:19], s39 offen nt lds\n"
    "v_mfma_f32_16x16x32_bf16 v[28:31], v[212:215], v[180:183], v[28:31]\n"
    "ds_read_b64_tr_b16 v[212:213], v237 offset:256\n"
    "s_waitcnt lgkmcnt(3)\n"
    "v_mfma_f32_16x16x32_bf16 v[32:35], v[184:187], v[176:179], v[32:35]\n"
    "ds_read_b64_tr_b16 v[214:215], v237 offset:6400\n"
    "v_max3_f32 v226, v144, v145, v146\n"
    "v_mfma_f32_16x16x32_bf16 v[36:39], v[184:187], v[180:183], v[36:39]\n"
    "buffer_load_dwordx4 v249, s[16:19], s39 offen offset:1024 nt lds\n"
    "v_mfma_f32_16x16x32_bf16 v[40:43], v[188:191], v[176:179], v[40:43]\n"
    "buffer_load_dwordx4 v250, s[16:19], s39 offen offset:2048 nt lds\n"
    "v_mfma_f32_16x16x32_bf16 v[44:47], v[188:191], v[180:183], v[44:47]\n"
    "v_add_u32_e32 v232, s38, v238\n"
    "v_xor_b32_e32 v233, 64, v232\n"
    "v_mfma_f32_16x16x32_bf16 v[48:51], v[192:195], v[176:179], v[48:51]\n"
    "s_mov_b32 s58, s36\n"
    "s_mov_b32 s36, s37\n"
    "v_mfma_f32_16x16x32_bf16 v[52:55], v[192:195], v[180:183], v[52:55]\n"
    "s_mov_b32 s37, s38\n"
    "s_mov_b32 s38, s58\n"
    "v_mfma_f32_16x16x32_bf16 v[56:59], v[196:199], v[176:179], v[56:59]\n"
    "s_sub_u32 s41, s41, 1\n"
    "s_cmp_lg_u32 s41, 0\n"
    "v_mfma_f32_16x16x32_bf16 v[60:63], v[196:199], v[180:183], v[60:63]\n"
    "v_max3_f32 v227, v148, v149, v150\n"
    "v_max3_f32 v226, v226, v147, v152\n"
    "v_max3_f32 v227, v227, v151, v156\n"
    "v_max3_f32 v226, v226, v153, v154\n"
    "v_max3_f32 v227, v227, v157, v158\n"
    "v_max_f32_e32 v226, v226, v155\n"
    "v_max_f32_e32 v227, v227, v159\n"
    "v_max_f32_e32 v228, v226, v227\n"
    "v_cmp_lt_f32_e32 vcc, s46, v228\n"
    "s_cbranch_scc1 .Lfz_H1\n"
    "s_waitcnt vmcnt(12) lgkmcnt(0)\n"
    "s_barrier\n"
    "v_mfma_f32_16x16x32_bf16 v[64:67], v[200:203], v[176:179], v[64:67]\n"
    "v_add_u32_e32 v234, s36, v239\n"
    "v_xor_b32_e64 v235, 32, v234\n"
    "v_mfma_f32_16x16x32_bf16 v[68:71], v[200:203], v[180:183], v[68:71]\n"
    "v_xor_b32_e32 v236, 64, v234\n"
    "v_xor_b32_e32 v237, 32, v236\n"
    "v_mfma_f32_16x16x32_bf16 v[72:75], v[204:207], v[176:179], v[72:75]\n"
    "v_mfma_f32_16x16x32_bf16 v[76:79], v[204:207], v[180:183], v[76:79]\n"
    "v_mfma_f32_16x16x32_bf16 v[80:83], v[208:211], v[176:179], v[80:83]\n"
    "v_mfma_f32_16x16x32_bf16 v[84:87], v[208:211], v[180:183], v[84:87]\n"
    "v_mfma_f32_16x16x32_bf16 v[88:91], v[212:215], v[176:179], v[88:91]\n"
    "v_mfma_f32_16x16x32_bf16 v[92:95], v[212:215], v[180:183], v[92:95]\n"
    "s_cbranch_vccnz .Lfz_R3\n"
    ".Lfz_RR3:\n"
    "v_exp_f32_e32 v144, v144\n"
    "v_exp_f32_e32 v145, v145\n"
    "v_exp_f32_e32 v148, v148\n"
    "v_add_f32_e32 v224, v144, v224\n"
    "v_exp_f32_e32 v149, v149\n"
    "v_add_f32_e32 v224, v145, v224\n"
    "v_exp_f32_e32 v146, v146\n"
    "v_add_f32_e32 v225, v148, v225\n"
    "v_exp_f32_e32 v147, v147\n"
    "v_add_f32_e32 v225, v149, v225\n"
    "v_exp_f32_e32 v150, v150\n"
    "v_add_f32_e32 v224, v146, v224\n"
    "v_exp_f32_e32 v151, v151\n"
    "v_add_f32_e32 v224, v147, v224\n"
    "v_exp_f32_e32 v152, v152\n"
    "v_add_f32_e32 v225, v150, v225\n"
    "v_exp_f32_e32 v153, v153\n"
    "v_add_f32_e32 v225, v151, v225\n"
    "v_exp_f32_e32 v156, v156\n"
    "v_add_f32_e32 v224, v152, v224\n"
    "v_exp_f32_e32 v157, v157\n"
    "v_add_f32_e32 v224, v153, v224\n"
    "v_exp_f32_e32 v154, v154\n"
    "v_add_f32_e32 v225, v156, v225\n"
    "v_exp_f32_e32 v155, v155\n"
    "v_add_f32_e32 v225, v157, v225\n"
    "v_exp_f32_e32 v158, v158\n"
    "v_add_f32_e32 v224, v154, v224\n"
    "v_exp_f32_e32 v159, v159\n"
    "v_add_f32_e32 v224, v155, v224\n"
    "v_add_f32_e32 v225, v158, v225\n"
    "v_add_f32_e32 v225, v159, v225\n"
    "v_mov_b32_e32 v230, v224\n"
    "v_mov_b32_e32 v231, v225\n"
    "s_nop 0\n"
    "v_permlane32_swap_b32_e32 v224, v230\n"
    "v_permlane32_swap_b32_e32 v225, v231\n"
    "v_add_f32_e32 v224, v224, v230\n"
    "v_add_f32_e32 v225, v225, v231\n"
    "v_mov_b32_e32 v230, v224\n"
    "v_mov_b32_e32 v231, v225\n"
    "s_nop 0\n"
    "v_permlane16_swap_b32_e32 v224, v230\n"
    "v_permlane16_swap_b32_e32 v225, v231\n"
    "v_add_f32_e32 v224, v224, v230\n"
    "v_add_f32_e32 v225, v225, v231\n"
    "v_rcp_f32_e32 v226, v224\n"
    "v_rcp_f32_e32 v228, v225\n"
    "s_nop 0\n"
    "v_pk_mul_f32 v[144:145], v[144:145], v[226:227] op_sel_hi:[1,0]\n"
    "v_pk_mul_f32 v[146:147], v[146:147], v[226:227] op_sel_hi:[1,0]\n"
    "v_pk_mul_f32 v[152:153], v[152:153], v[226:227] op_sel_hi:[1,0]\n"
    "v_pk_mul_f32 v[154:155], v[154:155], v[226:227] op_sel_hi:[1,0]\n"
    "v_pk_mul_f32 v[148:149], v[148:149], v[228:229] op_sel_hi:[1,0]\n"
    "v_pk_mul_f32 v[150:151], v[150:151], v[228:229] op_sel_hi:[1,0]\n"
    "v_pk_mul_f32 v[156:157], v[156:157], v[228:229] op_sel_hi:[1,0]\n"
    "v_pk_mul_f32 v[158:159], v[158:159], v[228:229] op_sel_hi:[1,0]\n"
    "v_cvt_pk_bf16_f32 v176, v144, v145\n"
    "v_cvt_pk_bf16_f32 v177, v146, v147\n"
    "v_cvt_pk_bf16_f32 v178, v152, v153\n"
    "v_cvt_pk_bf16_f32 v179, v154, v155\n"
    "v_cvt_pk_bf16_f32 v180, v148, v149\n"
    "v_cvt_pk_bf16_f32 v181, v150, v151\n"
    "v_cvt_pk_bf16_f32 v182, v156, v157\n"
    "v_cvt_pk_bf16_f32 v183, v158, v159\n"
    "v_pk_mul_f32 v[32:33], v[32:33], v[226:227] op_sel_hi:[1,0]\n"
    "v_pk_mul_f32 v[34:35], v[34:35], v[226:227] op_sel_hi:[1,0]\n"
    "v_pk_mul_f32 v[36:37], v[36:37], v[228:229] op_sel_hi:[1,0]\n"
    "v_pk_mul_f32 v[38:39], v[38:39], v[228:229] op_sel_hi:[1,0]\n"
    "v_pk_mul_f32 v[40:41], v[40:41], v[226:227] op_sel_hi:[1,0]\n"
    "v_pk_mul_f32 v[42:43], v[42:43], v[226:227] op_sel_hi:[1,0]\n"
    "v_pk_mul_f32 v[44:45], v[44:45], v[228:229] op_sel_hi:[1,0]\n"
    "v_pk_mul_f32 v[46:47], v[46:47], v[228:229] op_sel_hi:[1,0]\n"
    "v_pk_mul_f32 v[48:49], v[48:49], v[226:227] op_sel_hi:[1,0]\n"
    "v_pk_mul_f32 v[50:51], v[50:51], v[226:227] op_sel_hi:[1,0]\n"
    "v_pk_mul_f32 v[52:53], v[52:53], v[228:229] op_sel_hi:[1,0]\n"
    "v_pk_mul_f32 v[54:55], v[54:55], v[228:229] op_sel_hi:[1,0]\n"
    "v_pk_mul_f32 v[56:57], v[56:57], v[226:227] op_sel_hi:[1,0]\n"
    "v_pk_mul_f32 v[58:59], v[58:59], v[226:227] op_sel_hi:[1,0]\n"
    "v_pk_mul_f32 v[60:61], v[60:61], v[228:229] op_sel_hi:[1,0]\n"
    "v_pk_mul_f32 v[62:63], v[62:63], v[228:229] op_sel_hi:[1,0]\n"
    "v_pk_mul_f32 v[64:65], v[64:65], v[226:227] op_sel_hi:[1,0]\n"
    "v_pk_mul_f32 v[66:67], v[66:67], v[226:227] op_sel_hi:[1,0]\n"
    "v_pk_mul_f32 v[68:69], v[68:69], v[228:229] op_sel_hi:[1,0]\n"
    "v_pk_mul_f32 v[70:71], v[70:71], v[228:229] op_sel_hi:[1,0]\n"
    "v_pk_mul_f32 v[72:73], v[72:73], v[226:227] op_sel_hi:[1,0]\n"
    "v_pk_mul_f32 v[74:75], v[74:75], v[226:227] op_sel_hi:[1,0]\n"
    "v_pk_mul_f32 v[76:77], v[76:77], v[228:229] op_sel_hi:[1,0]\n"
    "v_pk_mul_f32 v[78:79], v[78:79], v[228:229] op_sel_hi:[1,0]\n"
    "v_pk_mul_f32 v[80:81], v[80:81], v[226:227] op_sel_hi:[1,0]\n"
    "v_pk_mul_f32 v[82:83], v[82:83], v[226:227] op_sel_hi:[1,0]\n"
    "v_pk_mul_f32 v[84:85], v[84:85], v[228:229] op_sel_hi:[1,0]\n"
    "v_pk_mul_f32 v[86:87], v[86:87], v[228:229] op_sel_hi:[1,0]\n"
    "v_pk_mul_f32 v[88:89], v[88:89], v[226:227] op_sel_hi:[1,0]\n"
    "v_pk_mul_f32 v[90:91], v[90:91], v[226:227] op_sel_hi:[1,0]\n"
    "v_pk_mul_f32 v[92:93], v[92:93], v[228:229] op_sel_hi:[1,0]\n"
    "v_pk_mul_f32 v[94:95], v[94:95], v[228:229] op_sel_hi:[1,0]\n"
    "s_waitcnt vmcnt(9) lgkmcnt(0)\n"
    "s_barrier\n"
    "ds_read_b64_tr_b16 v[200:201], v234\n"
    "ds_read_b64_tr_b16 v[202:203], v234 offset:6144\n"
    "ds_read_b64_tr_b16 v[204:205], v235\n"
    "ds_read_b64_tr_b16 v[206:207], v235 offset:6144\n"
    "ds_read_b64_tr_b16 v[208:209], v236\n"
    "ds_read_b64_tr_b16 v[210:211], v236 offset:6144\n"
    "ds_read_b64_tr_b16 v[212:213], v237\n"
    "ds_read_b64_tr_b16 v[214:215], v237 offset:6144\n"
    "ds_read_b64_tr_b16 v[184:185], v234 offset:128\n"
    "ds_read_b64_tr_b16 v[186:187], v234 offset:6272\n"
    "ds_read_b64_tr_b16 v[188:189], v235 offset:128\n"
    "ds_read_b64_tr_b16 v[190:191], v235 offset:6272\n"
    "ds_read_b64_tr_b16 v[192:193], v236 offset:128\n"
    "ds_read_b64_tr_b16 v[194:195], v236 offset:6272\n"
    "ds_read_b64_tr_b16 v[196:197], v237 offset:128\n"
    "ds_read_b64_tr_b16 v[198:199], v237 offset:6272\n"
    "v_pk_mul_f32 v[0:1], v[0:1], v[226:227] op_sel_hi:[1,0]\n"
    "v_pk_mul_f32 v[2:3], v[2:3], v[226:227] op_sel_hi:[1,0]\n"
    "v_pk_mul_f32 v[4:5], v[4:5], v[228:229] op_sel_hi:[1,0]\n"
    "v_pk_mul_f32 v[6:7], v[6:7], v[228:229] op_sel_hi:[1,0]\n"
    "v_pk_mul_f32 v[8:9], v[8:9], v[226:227] op_sel_hi:[1,0]\n"
    "v_pk_mul_f32 v[10:11], v[10:11], v[226:227] op_sel_hi:[1,0]\n"
    "v_pk_mul_f32 v[12:13], v[12:13], v[228:229] op_sel_hi:[1,0]\n"
    "v_pk_mul_f32 v[14:15], v[14:15], v[228:229] op_sel_hi:[1,0]\n"
    "v_pk_mul_f32 v[16:17], v[16:17], v[226:227] op_sel_hi:[1,0]\n"
    "v_pk_mul_f32 v[18:19], v[18:19], v[226:227] op_sel_hi:[1,0]\n"
    "v_pk_mul_f32 v[20:21], v[20:21], v[228:229] op_sel_hi:[1,0]\n"
    "v_pk_mul_f32 v[22:23], v[22:23], v[228:229] op_sel_hi:[1,0]\n"
    "v_pk_mul_f32 v[24:25], v[24:25], v[226:227] op_sel_hi:[1,0]\n"
    "v_pk_mul_f32 v[26:27], v[26:27], v[226:227] op_sel_hi:[1,0]\n"
    "v_pk_mul_f32 v[28:29], v[28:29], v[228:229] op_sel_hi:[1,0]\n"
    "v_pk_mul_f32 v[30:31], v[30:31], v[228:229] op_sel_hi:[1,0]\n"
    "s_waitcnt lgkmcnt(4)\n"
    "v_mfma_f32_16x16x32_bf16 v[0:3], v[200:203], v[176:179], v[0:3]\n"
    "v_mfma_f32_16x16x32_bf16 v[4:7], v[200:203], v[180:183], v[4:7]\n"
    "ds_read_b64_tr_b16 v[200:201], v234 offset:256\n"
    "ds_read_b64_tr_b16 v[202:203], v234 offset:6400\n"
    "v_mfma_f32_16x16x32_bf16 v[8:11], v[204:207], v[176:179], v[8:11]\n"
    "v_mfma_f32_16x16x32_bf16 v[12:15], v[204:207], v[180:183], v[12:15]\n"
    "ds_read_b64_tr_b16 v[204:205], v235 offset:256\n"
    "ds_read_b64_tr_b16 v[206:207], v235 offset:6400\n"
    "v_mfma_f32_16x16x32_bf16 v[16:19], v[208:211], v[176:179], v[16:19]\n"
    "v_mfma_f32_16x16x32_bf16 v[20:23], v[208:211], v[180:183], v[20:23]\n"
    "ds_read_b64_tr_b16 v[208:209], v236 offset:256\n"
    "ds_read_b64_tr_b16 v[210:211], v236 offset:6400\n"
    "v_mfma_f32_16x16x32_bf16 v[24:27], v[212:215], v[176:179], v[24:27]\n"
    "v_mfma_f32_16x16x32_bf16 v[28:31], v[212:215], v[180:183], v[28:31]\n"
    "ds_read_b64_tr_b16 v[212:213], v237 offset:256\n"
    "ds_read_b64_tr_b16 v[214:215], v237 offset:6400\n"
    "v_mfma_f32_16x16x32_bf16 v[32:35], v[184:187], v[176:179], v[32:35]\n"
    "v_cvt_pk_bf16_f32 v96, v0, v1\n"
    "v_cvt_pk_bf16_f32 v97, v2, v3\n"
    "v_mfma_f32_16x16x32_bf16 v[36:39], v[184:187], v[180:183], v[36:39]\n"
    "v_cvt_pk_bf16_f32 v98, v8, v9\n"
    "v_cvt_pk_bf16_f32 v99, v10, v11\n"
    "v_mfma_f32_16x16x32_bf16 v[40:43], v[188:191], v[176:179], v[40:43]\n"
    "v_cvt_pk_bf16_f32 v100, v4, v5\n"
    "v_cvt_pk_bf16_f32 v101, v6, v7\n"
    "v_mfma_f32_16x16x32_bf16 v[44:47], v[188:191], v[180:183], v[44:47]\n"
    "v_cvt_pk_bf16_f32 v102, v12, v13\n"
    "s_waitcnt lgkmcnt(0)\n"
    "v_mfma_f32_16x16x32_bf16 v[48:51], v[192:195], v[176:179], v[48:51]\n"
    "v_cvt_pk_bf16_f32 v103, v14, v15\n"
    "v_cvt_pk_bf16_f32 v104, v16, v17\n"
    "v_mfma_f32_16x16x32_bf16 v[52:55], v[192:195], v[180:183], v[52:55]\n"
    "v_cvt_pk_bf16_f32 v105, v18, v19\n"
    "v_cvt_pk_bf16_f32 v106, v24, v25\n"
    "v_mfma_f32_16x16x32_bf16 v[56:59], v[196:199], v[176:179], v[56:59]\n"
    "v_cvt_pk_bf16_f32 v107, v26, v27\n"
    "v_cvt_pk_bf16_f32 v108, v20, v21\n"
    "v_mfma_f32_16x16x32_bf16 v[60:63], v[196:199], v[180:183], v[60:63]\n"
    "v_cvt_pk_bf16_f32 v109, v22, v23\n"
    "v_cvt_pk_bf16_f32 v110, v28, v29\n"
    "v_mfma_f32_16x16x32_bf16 v[64:67], v[200:203], v[176:179], v[64:67]\n"
    "v_cvt_pk_bf16_f32 v111, v30, v31\n"
    "v_cvt_pk_bf16_f32 v112, v32, v33\n"
    "v_mfma_f32_16x16x32_bf16 v[68:71], v[200:203], v[180:183], v[68:71]\n"
    "v_cvt_pk_bf16_f32 v113, v34, v35\n"
    "v_cvt_pk_bf16_f32 v114, v40, v41\n"
    "v_mfma_f32_16x16x32_bf16 v[72:75], v[204:207], v[176:179], v[72:75]\n"
    "v_cvt_pk_bf16_f32 v115, v42, v43\n"
    "v_cvt_pk_bf16_f32 v116, v36, v37\n"
    "v_mfma_f32_16x16x32_bf16 v[76:79], v[204:207], v[180:183], v[76:79]\n"
    "v_cvt_pk_bf16_f32 v117, v38, v39\n"
    "v_cvt_pk_bf16_f32 v118, v44, v45\n"
    "v_mfma_f32_16x16x32_bf16 v[80:83], v[208:211], v[176:179], v[80:83]\n"
    "v_cvt_pk_bf16_f32 v119, v46, v47\n"
    "v_cvt_pk_bf16_f32 v120, v48, v49\n"
    "v_mfma_f32_16x16x32_bf16 v[84:87], v[208:211], v[180:183], v[84:87]\n"
    "v_cvt_pk_bf16_f32 v121, v50, v51\n"
    "v_cvt_pk_bf16_f32 v122, v56, v57\n"
    "v_mfma_f32_16x16x32_bf16 v[88:91], v[212:215], v[176:179], v[88:91]\n"
    "v_cvt_pk_bf16_f32 v123, v58, v59\n"
    "v_cvt_pk_bf16_f32 v124, v52, v53\n"
    "v_mfma_f32_16x16x32_bf16 v[92:95], v[212:215], v[180:183], v[92:95]\n"
    "v_cvt_pk_bf16_f32 v125, v54, v55\n"
    "v_cvt_pk_bf16_f32 v126, v60, v61\n"
    "v_cvt_pk_bf16_f32 v127, v62, v63\n"
    "v_cvt_pk_bf16_f32 v128, v64, v65\n"
    "v_cvt_pk_bf16_f32 v129, v66, v67\n"
    "v_cvt_pk_bf16_f32 v130, v72, v73\n"
    "v_cvt_pk_bf16_f32 v131, v74, v75\n"
    "v_cvt_pk_bf16_f32 v132, v68, v69\n"
    "v_cvt_pk_bf16_f32 v133, v70, v71\n"
    "v_cvt_pk_bf16_f32 v134, v76, v77\n"
    "v_cvt_pk_bf16_f32 v135, v78, v79\n"
    "v_cvt_pk_bf16_f32 v136, v80, v81\n"
    "v_cvt_pk_bf16_f32 v137, v82, v83\n"
    "v_cvt_pk_bf16_f32 v138, v88, v89\n"
    "v_cvt_pk_bf16_f32 v139, v90, v91\n"
    "v_cvt_pk_bf16_f32 v140, v84, v85\n"
    "v_cvt_pk_bf16_f32 v141, v86, v87\n"
    "v_cvt_pk_bf16_f32 v142, v92, v93\n"
    "v_cvt_pk_bf16_f32 v143, v94, v95\n"
    "v_add_u32_e32 v226, s34, v241\n"
    "s_cmp_eq_u32 s32, 0\n"
    "s_cbranch_scc1 .Lfz_OA\n"
    "s_waitcnt vmcnt(0)\n"
    "ds_write_b128 v226, v[96:99]\n"
    "ds_write_b128 v226, v[100:103] offset:1024\n"
    "ds_write_b128 v226, v[104:107] offset:2048\n"
    "ds_write_b128 v226, v[108:111] offset:12288\n"
    "ds_write_b128 v226, v[112:115] offset:13312\n"
    "ds_write_b128 v226, v[116:119] offset:14336\n"
    "ds_write_b128 v226, v[120:123] offset:24576\n"
    "ds_write_b128 v226, v[124:127] offset:25600\n"
    "ds_write_b128 v226, v[128:131] offset:26624\n"
    "ds_write_b128 v226, v[132:135] offset:36864\n"
    "ds_write_b128 v226, v[136:139] offset:37888\n"
    "ds_write_b128 v226, v[140:143] offset:38912\n"
    "s_waitcnt lgkmcnt(0)\n"
    "s_barrier\n"
    "buffer_load_dword v208, off, s[28:31], s93 sc1\n"
    "s_add_u32 m0, s34, 0x1e000\n"
    "s_add_u32 s42, s66, 0x3c000\n"
    "s_add_u32 s43, s66, 0x3e400\n"
    "s_add_u32 s44, s66, 0x40800\n"
    "buffer_load_dwordx4 v254, s[24:27], s42 offen lds\n"
    "buffer_load_dwordx4 v254, s[24:27], s43 offen offset:1024 lds\n"
    "buffer_load_dwordx4 v254, s[24:27], s44 offen offset:2048 lds\n"
    "s_add_u32 m0, s34, 0xc000\n"
    "s_add_u32 s42, s66, 0x5a000\n"
    "s_add_u32 s43, s66, 0x5c400\n"
    "s_add_u32 s44, s66, 0x5e800\n"
    "buffer_load_dwordx4 v254, s[24:27], s42 offen lds\n"
    "buffer_load_dwordx4 v254, s[24:27], s43 offen offset:1024 lds\n"
    "buffer_load_dwordx4 v254, s[24:27], s44 offen offset:2048 lds\n"
    "s_add_u32 m0, s34, 0xf000\n"
    "s_add_u32 s42, s67, 0x3c000\n"
    "s_add_u32 s43, s67, 0x3e400\n"
    "s_add_u32 s44, s67, 0x40800\n"
    "buffer_load_dwordx4 v254, s[24:27], s42 offen lds\n"
    "buffer_load_dwordx4 v254, s[24:27], s43 offen offset:1024 lds\n"
    "buffer_load_dwordx4 v254, s[24:27], s44 offen offset:2048 lds\n"
    "s_waitcnt lgkmcnt(0)\n"
    "s_barrier\n"
    "s_mov_b32 m0, s34\n"
    "s_add_u32 s42, s67, 0x5a000\n"
    "s_add_u32 s43, s67, 0x5c400\n"
    "s_add_u32 s44, s67, 0x5e800\n"
    "buffer_load_dwordx4 v254, s[24:27], s42 offen lds\n"
    "buffer_load_dwordx4 v254, s[24:27], s43 offen offset:1024 lds\n"
    "buffer_load_dwordx4 v254, s[24:27], s44 offen offset:2048 lds\n"
    "s_add_u32 m0, s34, 0x3000\n"
    "s_add_u32 s42, s68, 0x3c000\n"
    "s_add_u32 s43, s68, 0x3e400\n"
    "s_add_u32 s44, s68, 0x40800\n"
    "buffer_load_dwordx4 v254, s[24:27], s42 offen lds\n"
    "buffer_load_dwordx4 v254, s[24:27], s43 offen offset:1024 lds\n"
    "buffer_load_dwordx4 v254, s[24:27], s44 offen offset:2048 lds\n"
    "s_add_u32 m0, s34, 0x6000\n"
    "s_add_u32 s42, s68, 0x5a000\n"
    "s_add_u32 s43, s68, 0x5c400\n"
    "s_add_u32 s44, s68, 0x5e800\n"
    "buffer_load_dwordx4 v254, s[24:27], s42 offen lds\n"
    "buffer_load_dwordx4 v254, s[24:27], s43 offen offset:1024 lds\n"
    "buffer_load_dwordx4 v254, s[24:27], s44 offen offset:2048 lds\n"
    "s_waitcnt lgkmcnt(0)\n"
    "s_barrier\n"
    "s_add_u32 m0, s34, 0x12000\n"
    "s_add_u32 s42, s69, 0x3c000\n"
    "s_add_u32 s43, s69, 0x3e400\n"
    "s_add_u32 s44, s69, 0x40800\n"
    "buffer_load_dwordx4 v254, s[24:27], s42 offen lds\n"
    "buffer_load_dwordx4 v254, s[24:27], s43 offen offset:1024 lds\n"
    "buffer_load_dwordx4 v254, s[24:27], s44 offen offset:2048 lds\n"
    "s_add_u32 m0, s34, 0x15000\n"
    "s_add_u32 s42, s69, 0x5a000\n"
    "s_add_u32 s43, s69, 0x5c400\n"
    "s_add_u32 s44, s69, 0x5e800\n"
    "buffer_load_dwordx4 v254, s[24:27], s42 offen lds\n"
    "buffer_load_dwordx4 v254, s[24:27], s43 offen offset:1024 lds\n"
    "buffer_load_dwordx4 v254, s[24:27], s44 offen offset:2048 lds\n"
    "v_lshrrev_b32_e32 v218, 4, v241\n"
    "v_lshrrev_b32_e32 v217, 2, v241\n"
    "v_lshrrev_b32_e32 v219, 4, v218\n"
    "v_bfe_u32 v220, v218, 2, 1\n"
    "v_lshlrev_b32_e32 v220, 1, v220\n"
    "v_sub_u32_e32 v219, v219, v220\n"
    "v_lshlrev_b32_e64 v219, 2, v219\n"
    "v_bfe_u32 v220, v218, 3, 1\n"
    "v_lshl_add_u32 v219, v220, 1, v219\n"
    "v_bfe_u32 v220, v218, 1, 1\n"
    "v_add_u32_e32 v219, v219, v220\n"
    "v_and_b32_e32 v220, 1, v218\n"
    "v_lshlrev_b32_e32 v220, 4, v220\n"
    "s_movk_i32 s42, 0x3f80\n"
    "v_lshlrev_b32_e64 v220, v220, s42\n"
    "v_cmp_eq_u32_e32 vcc, 0, v219\n"
    "s_nop 1\n"
    "v_cndmask_b32_e32 v192, 0, v220, vcc\n"
    "v_cmp_eq_u32_e32 vcc, 1, v219\n"
    "s_nop 1\n"
    "v_cndmask_b32_e32 v193, 0, v220, vcc\n"
    "v_cmp_eq_u32_e32 vcc, 2, v219\n"
    "s_nop 1\n"
    "v_cndmask_b32_e32 v194, 0, v220, vcc\n"
    "v_cmp_eq_u32_e32 vcc, 3, v219\n"
    "s_nop 1\n"
    "v_cndmask_b32_e32 v195, 0, v220, vcc\n"
    "v_cmp_eq_u32_e32 vcc, 4, v219\n"
    "s_nop 1\n"
    "v_cndmask_b32_e32 v196, 0, v220, vcc\n"
    "v_cmp_eq_u32_e32 vcc, 5, v219\n"
    "s_nop 1\n"
    "v_cndmask_b32_e32 v197, 0, v220, vcc\n"
    "v_cmp_eq_u32_e32 vcc, 6, v219\n"
    "s_nop 1\n"
    "v_cndmask_b32_e32 v198, 0, v220, vcc\n"
    "v_cmp_eq_u32_e32 vcc, 7, v219\n"
    "s_nop 1\n"
    "v_cndmask_b32_e32 v199, 0, v220, vcc\n"
    "v_and_b32_e64 v219, 15, v218\n"
    "v_mul_u32_u24_e32 v219, 0xa00, v219\n"
    "v_and_b32_e32 v220, 48, v218\n"
    "v_add_u32_e32 v216, v219, v220\n"
    "s_mul_i32 s36, s86, 0x400\n"
    "s_lshr_b32 s37, s86, 1\n"
    "s_mul_i32 s37, s37, 0xa000\n"
    "s_and_b32 s42, s86, 1\n"
    "s_lshl_b32 s42, s42, 6\n"
    "s_add_u32 s37, s37, s42\n"
    "s_add_u32 s42, s59, 0x9000\n"
    "v_mov_b32_e32 v222, s42\n"
    "s_cmp_eq_u32 s33, 3\n"
    "s_cbranch_scc0 .Lfz_LD\n"
    "v_cmp_gt_u32_e32 vcc, 64, v217\n"
    "v_mov_b32_e64 v221, 2.0\n"
    "v_subrev_u32_e32 v254, 0x16800, v254\n"
    "v_cndmask_b32_e32 v217, v221, v217, vcc\n"
    "v_lshrrev_b32_e32 v221, 8, v241\n"
    "v_lshl_add_u32 v222, v221, 2, v222\n"
    "v_mov_b32_e64 v221, 0\n"
    "ds_write_b32 v222, v221\n"
    "s_waitcnt lgkmcnt(0)\n"
    "s_barrier\n"
    "s_add_u32 m0, s59, 0x18000\n"
    "s_add_u32 s42, s70, 0x3c000\n"
    "s_add_u32 s43, s70, 0x3e400\n"
    "s_add_u32 s44, s70, 0x40800\n"
    "s_add_u32 s45, s70, 0x42c00\n"
    "buffer_load_dwordx4 v254, s[24:27], s42 offen lds\n"
    "buffer_load_dwordx4 v254, s[24:27], s43 offen offset:1024 lds\n"
    "buffer_load_dwordx4 v254, s[24:27], s44 offen offset:2048 lds\n"
    "buffer_load_dwordx4 v254, s[24:27], s45 offen offset:3072 lds\n"
    "s_add_u32 m0, s59, 0x19000\n"
    "s_add_u32 s42, s70, 0x46000\n"
    "s_add_u32 s43, s70, 0x48400\n"
    "s_add_u32 s44, s70, 0x4a800\n"
    "s_add_u32 s45, s70, 0x4cc00\n"
    "buffer_load_dwordx4 v254, s[24:27], s42 offen lds\n"
    "buffer_load_dwordx4 v254, s[24:27], s43 offen offset:1024 lds\n"
    "buffer_load_dwordx4 v254, s[24:27], s44 offen offset:2048 lds\n"
    "buffer_load_dwordx4 v254, s[24:27], s45 offen offset:3072 lds\n"
    "s_add_u32 m0, s59, 0x1a000\n"
    "s_add_u32 s42, s70, 0x50000\n"
    "s_add_u32 s43, s70, 0x52400\n"
    "s_add_u32 s44, s70, 0x54800\n"
    "s_add_u32 s45, s70, 0x56c00\n"
    "buffer_load_dwordx4 v254, s[24:27], s42 offen lds\n"
    "buffer_load_dwordx4 v254, s[24:27], s43 offen offset:1024 lds\n"
    "buffer_load_dwordx4 v254, s[24:27], s44 offen offset:2048 lds\n"
    "buffer_load_dwordx4 v254, s[24:27], s45 offen offset:3072 lds\n"
    "s_waitcnt vmcnt(33) lgkmcnt(0)\n"
    "s_barrier\n"
    "s_waitcnt vmcnt(36)\n"
    "v_readfirstlane_b32 s95, v208\n"
    "v_mov_b32_e64 v224, 0\n"
    "buffer_atomic_or v224, v217, s[28:31], s92 offen sc0\n"
    "s_add_u32 m0, s59, 0x1b000\n"
    "s_add_u32 s42, s70, 0x5a000\n"
    "s_add_u32 s43, s70, 0x5c400\n"
    "s_add_u32 s44, s70, 0x5e800\n"
    "s_add_u32 s45, s70, 0x60c00\n"
    "buffer_load_dwordx4 v254, s[24:27], s42 offen lds\n"
    "buffer_load_dwordx4 v254, s[24:27], s43 offen offset:1024 lds\n"
    "buffer_load_dwordx4 v254, s[24:27], s44 offen offset:2048 lds\n"
    "buffer_load_dwordx4 v254, s[24:27], s45 offen offset:3072 lds\n"
    "s_add_u32 m0, s59, 0x1c000\n"
    "s_add_u32 s42, s70, 0x64000\n"
    "s_add_u32 s43, s70, 0x66400\n"
    "s_add_u32 s44, s70, 0x68800\n"
    "s_add_u32 s45, s70, 0x6ac00\n"
    "buffer_load_dwordx4 v254, s[24:27], s42 offen lds\n"
    "buffer_load_dwordx4 v254, s[24:27], s43 offen offset:1024 lds\n"
    "buffer_load_dwordx4 v254, s[24:27], s44 offen offset:2048 lds\n"
    "buffer_load_dwordx4 v254, s[24:27], s45 offen offset:3072 lds\n"
    "s_add_u32 m0, s59, 0x1d000\n"
    "s_add_u32 s42, s70, 0x6e000\n"
    "s_add_u32 s43, s70, 0x70400\n"
    "s_add_u32 s44, s70, 0x72800\n"
    "s_add_u32 s45, s70, 0x74c00\n"
    "buffer_load_dwordx4 v254, s[24:27], s42 offen lds\n"
    "buffer_load_dwordx4 v254, s[24:27], s43 offen offset:1024 lds\n"
    "buffer_load_dwordx4 v254, s[24:27], s44 offen offset:2048 lds\n"
    "buffer_load_dwordx4 v254, s[24:27], s45 offen offset:3072 lds\n"
    "s_waitcnt vmcnt(43) lgkmcnt(0)\n"
    "s_barrier\n"
    "v_mov_b32_e64 v225, 0\n"
    "buffer_atomic_or v225, v217, s[28:31], s92 offen sc0\n"
    "s_add_u32 m0, s59, 0x1e000\n"
    "s_add_u32 s42, s71, 0x3c000\n"
    "s_add_u32 s43, s71, 0x3e400\n"
    "s_add_u32 s44, s71, 0x40800\n"
    "s_add_u32 s45, s71, 0x42c00\n"
    "buffer_load_dwordx4 v254, s[24:27], s42 offen lds\n"
    "buffer_load_dwordx4 v254, s[24:27], s43 offen offset:1024 lds\n"
    "buffer_load_dwordx4 v254, s[24:27], s44 offen offset:2048 lds\n"
    "buffer_load_dwordx4 v254, s[24:27], s45 offen offset:3072 lds\n"
    "s_add_u32 m0, s59, 0x1f000\n"
    "s_add_u32 s42, s71, 0x46000\n"
    "s_add_u32 s43, s71, 0x48400\n"
    "s_add_u32 s44, s71, 0x4a800\n"
    "s_add_u32 s45, s71, 0x4cc00\n"
    "buffer_load_dwordx4 v254, s[24:27], s42 offen lds\n"
    "buffer_load_dwordx4 v254, s[24:27], s43 offen offset:1024 lds\n"
    "buffer_load_dwordx4 v254, s[24:27], s44 offen offset:2048 lds\n"
    "buffer_load_dwordx4 v254, s[24:27], s45 offen offset:3072 lds\n"
    "s_add_u32 m0, s59, 0x20000\n"
    "s_add_u32 s42, s71, 0x50000\n"
    "s_add_u32 s43, s71, 0x52400\n"
    "s_add_u32 s44, s71, 0x54800\n"
    "s_add_u32 s45, s71, 0x56c00\n"
    "buffer_load_dwordx4 v254, s[24:27], s42 offen lds\n"
    "buffer_load_dwordx4 v254, s[24:27], s43 offen offset:1024 lds\n"
    "buffer_load_dwordx4 v254, s[24:27], s44 offen offset:2048 lds\n"
    "buffer_load_dwordx4 v254, s[24:27], s45 offen offset:3072 lds\n"
    "s_waitcnt vmcnt(53) lgkmcnt(0)\n"
    "s_barrier\n"
    "s_waitcnt vmcnt(25)\n"
    "v_subrev_u32_e32 v221, s95, v224\n"
    "v_cmp_lt_u32_e32 vcc, 10, v221\n"
    "s_nop 1\n"
    "v_cndmask_b32_e64 v221, v221, 0, vcc\n"
    "s_nop 1\n"
    "v_min_u32_dpp v221, v221, v221 row_ror:8 row_mask:0xf bank_mask:0xf\n"
    "s_nop 1\n"
    "v_min_u32_dpp v221, v221, v221 row_ror:4 row_mask:0xf bank_mask:0xf\n"
    "s_nop 1\n"
    "v_min_u32_dpp v221, v221, v221 row_ror:2 row_mask:0xf bank_mask:0xf\n"
    "s_nop 1\n"
    "v_min_u32_dpp v221, v221, v221 row_ror:1 row_mask:0xf bank_mask:0xf\n"
    "ds_write_b32 v222, v221\n"
    "v_mov_b32_e64 v226, 0\n"
    "buffer_atomic_or v226, v217, s[28:31], s92 offen sc0\n"
    "s_add_u32 m0, s59, 0xc000\n"
    "s_add_u32 s42, s71, 0x5a000\n"
    "s_add_u32 s43, s71, 0x5c400\n"
    "s_add_u32 s44, s71, 0x5e800\n"
    "s_add_u32 s45, s71, 0x60c00\n"
    "buffer_load_dwordx4 v254, s[24:27], s42 offen lds\n"
    "buffer_load_dwordx4 v254, s[24:27], s43 offen offset:1024 lds\n"
    "buffer_load_dwordx4 v254, s[24:27], s44 offen offset:2048 lds\n"
    "buffer_load_dwordx4 v254, s[24:27], s45 offen offset:3072 lds\n"
    "s_add_u32 m0, s59, 0xd000\n"
    "s_add_u32 s42, s71, 0x64000\n"
    "s_add_u32 s43, s71, 0x66400\n"
    "s_add_u32 s44, s71, 0x68800\n"
    "s_add_u32 s45, s71, 0x6ac00\n"
    "buffer_load_dwordx4 v254, s[24:27], s42 offen lds\n"
    "buffer_load_dwordx4 v254, s[24:27], s43 offen offset:1024 lds\n"
    "buffer_load_dwordx4 v254, s[24:27], s44 offen offset:2048 lds\n"
    "buffer_load_dwordx4 v254, s[24:27], s45 offen offset:3072 lds\n"
    "s_add_u32 m0, s59, 0xe000\n"
    "s_add_u32 s42, s71, 0x6e000\n"
    "s_add_u32 s43, s71, 0x70400\n"
    "s_add_u32 s44, s71, 0x72800\n"
    "s_add_u32 s45, s71, 0x74c00\n"
    "buffer_load_dwordx4 v254, s[24:27], s42 offen lds\n"
    "buffer_load_dwordx4 v254, s[24:27], s43 offen offset:1024 lds\n"
    "buffer_load_dwordx4 v254, s[24:27], s44 offen offset:2048 lds\n"
    "buffer_load_dwordx4 v254, s[24:27], s45 offen offset:3072 lds\n"
    "s_waitcnt lgkmcnt(0)\n"
    "s_barrier\n"
    "s_waitcnt vmcnt(25)\n"
    "v_subrev_u32_e32 v221, s95, v225\n"
    "v_cmp_lt_u32_e32 vcc, 10, v221\n"
    "s_nop 1\n"
    "v_cndmask_b32_e64 v221, v221, 0, vcc\n"
    "s_nop 1\n"
    "v_min_u32_dpp v221, v221, v221 row_ror:8 row_mask:0xf bank_mask:0xf\n"
    "s_nop 1\n"
    "v_min_u32_dpp v221, v221, v221 row_ror:4 row_mask:0xf bank_mask:0xf\n"
    "s_nop 1\n"
    "v_min_u32_dpp v221, v221, v221 row_ror:2 row_mask:0xf bank_mask:0xf\n"
    "s_nop 1\n"
    "v_min_u32_dpp v221, v221, v221 row_ror:1 row_mask:0xf bank_mask:0xf\n"
    "ds_write_b32 v222, v221\n"
    "v_mov_b32_e64 v227, 0\n"
    "buffer_atomic_or v227, v217, s[28:31], s92 offen sc0\n"
    "s_add_u32 m0, s59, 0xf000\n"
    "s_add_u32 s42, s63, 0x3c000\n"
    "s_add_u32 s43, s63, 0x3e400\n"
    "s_add_u32 s44, s63, 0x40800\n"
    "s_add_u32 s45, s63, 0x42c00\n"
    "buffer_load_dwordx4 v254, s[24:27], s42 offen lds\n"
    "buffer_load_dwordx4 v254, s[24:27], s43 offen offset:1024 lds\n"
    "buffer_load_dwordx4 v254, s[24:27], s44 offen offset:2048 lds\n"
    "buffer_load_dwordx4 v254, s[24:27], s45 offen offset:3072 lds\n"
    "s_add_u32 m0, s59, 0x10000\n"
    "s_add_u32 s42, s63, 0x46000\n"
    "s_add_u32 s43, s63, 0x48400\n"
    "s_add_u32 s44, s63, 0x4a800\n"
    "s_add_u32 s45, s63, 0x4cc00\n"
    "buffer_load_dwordx4 v254, s[24:27], s42 offen lds\n"
    "buffer_load_dwordx4 v254, s[24:27], s43 offen offset:1024 lds\n"
    "buffer_load_dwordx4 v254, s[24:27], s44 offen offset:2048 lds\n"
    "buffer_load_dwordx4 v254, s[24:27], s45 offen offset:3072 lds\n"
    "s_add_u32 m0, s59, 0x11000\n"
    "s_add_u32 s42, s63, 0x50000\n"
    "s_add_u32 s43, s63, 0x52400\n"
    "s_add_u32 s44, s63, 0x54800\n"
    "s_add_u32 s45, s63, 0x56c00\n"
    "buffer_load_dwordx4 v254, s[24:27], s42 offen lds\n"
    "buffer_load_dwordx4 v254, s[24:27], s43 offen offset:1024 lds\n"
    "buffer_load_dwordx4 v254, s[24:27], s44 offen offset:2048 lds\n"
    "buffer_load_dwordx4 v254, s[24:27], s45 offen offset:3072 lds\n"
    "s_waitcnt lgkmcnt(0)\n"
    "s_barrier\n"
    "s_waitcnt vmcnt(25)\n"
    "v_subrev_u32_e32 v221, s95, v226\n"
    "v_cmp_lt_u32_e32 vcc, 10, v221\n"
    "s_nop 1\n"
    "v_cndmask_b32_e64 v221, v221, 0, vcc\n"
    "s_nop 1\n"
    "v_min_u32_dpp v221, v221, v221 row_ror:8 row_mask:0xf bank_mask:0xf\n"
    "s_nop 1\n"
    "v_min_u32_dpp v221, v221, v221 row_ror:4 row_mask:0xf bank_mask:0xf\n"
    "s_nop 1\n"
    "v_min_u32_dpp v221, v221, v221 row_ror:2 row_mask:0xf bank_mask:0xf\n"
    "s_nop 1\n"
    "v_min_u32_dpp v221, v221, v221 row_ror:1 row_mask:0xf bank_mask:0xf\n"
    "ds_write_b32 v222, v221\n"
    "v_mov_b32_e64 v224, 0\n"
    "buffer_atomic_or v224, v217, s[28:31], s92 offen sc0\n"
    "s_add_u32 m0, s59, 0x0\n"
    "s_add_u32 s42, s63, 0x5a000\n"
    "s_add_u32 s43, s63, 0x5c400\n"
    "s_add_u32 s44, s63, 0x5e800\n"
    "s_add_u32 s45, s63, 0x60c00\n"
    "buffer_load_dwordx4 v254, s[24:27], s42 offen lds\n"
    "buffer_load_dwordx4 v254, s[24:27], s43 offen offset:1024 lds\n"
    "buffer_load_dwordx4 v254, s[24:27], s44 offen offset:2048 lds\n"
    "buffer_load_dwordx4 v254, s[24:27], s45 offen offset:3072 lds\n"
    "s_add_u32 m0, s59, 0x1000\n"
    "s_add_u32 s42, s63, 0x64000\n"
    "s_add_u32 s43, s63, 0x66400\n"
    "s_add_u32 s44, s63, 0x68800\n"
    "s_add_u32 s45, s63, 0x6ac00\n"
    "buffer_load_dwordx4 v254, s[24:27], s42 offen lds\n"
    "buffer_load_dwordx4 v254, s[24:27], s43 offen offset:1024 lds\n"
    "buffer_load_dwordx4 v254, s[24:27], s44 offen offset:2048 lds\n"
    "buffer_load_dwordx4 v254, s[24:27], s45 offen offset:3072 lds\n"
    "s_add_u32 m0, s59, 0x2000\n"
    "s_add_u32 s42, s63, 0x6e000\n"
    "s_add_u32 s43, s63, 0x70400\n"
    "s_add_u32 s44, s63, 0x72800\n"
    "s_add_u32 s45, s63, 0x74c00\n"
    "buffer_load_dwordx4 v254, s[24:27], s42 offen lds\n"
    "buffer_load_dwordx4 v254, s[24:27], s43 offen offset:1024 lds\n"
    "buffer_load_dwordx4 v254, s[24:27], s44 offen offset:2048 lds\n"
    "buffer_load_dwordx4 v254, s[24:27], s45 offen offset:3072 lds\n"
    "s_waitcnt lgkmcnt(0)\n"
    "s_barrier\n"
    "s_waitcnt vmcnt(25)\n"
    "v_subrev_u32_e32 v221, s95, v227\n"
    "v_cmp_lt_u32_e32 vcc, 10, v221\n"
    "s_nop 1\n"
    "v_cndmask_b32_e64 v221, v221, 0, vcc\n"
    "s_nop 1\n"
    "v_min_u32_dpp v221, v221, v221 row_ror:8 row_mask:0xf bank_mask:0xf\n"
    "s_nop 1\n"
    "v_min_u32_dpp v221, v221, v221 row_ror:4 row_mask:0xf bank_mask:0xf\n"
    "s_nop 1\n"
    "v_min_u32_dpp v221, v221, v221 row_ror:2 row_mask:0xf bank_mask:0xf\n"
    "s_nop 1\n"
    "v_min_u32_dpp v221, v221, v221 row_ror:1 row_mask:0xf bank_mask:0xf\n"
    "ds_write_b32 v222, v221\n"
    "v_mov_b32_e32 v225, 0\n"
    "buffer_atomic_or v225, v217, s[28:31], s92 offen sc0\n"
    "s_add_u32 m0, s59, 0x3000\n"
    "s_add_u32 s42, s64, 0x3c000\n"
    "s_add_u32 s43, s64, 0x3e400\n"
    "s_add_u32 s44, s64, 0x40800\n"
    "s_add_u32 s45, s64, 0x42c00\n"
    "buffer_load_dwordx4 v254, s[24:27], s42 offen lds\n"
    "buffer_load_dwordx4 v254, s[24:27], s43 offen offset:1024 lds\n"
    "buffer_load_dwordx4 v254, s[24:27], s44 offen offset:2048 lds\n"
    "buffer_load_dwordx4 v254, s[24:27], s45 offen offset:3072 lds\n"
    "s_add_u32 m0, s59, 0x4000\n"
    "s_add_u32 s42, s64, 0x46000\n"
    "s_add_u32 s43, s64, 0x48400\n"
    "s_add_u32 s44, s64, 0x4a800\n"
    "s_add_u32 s45, s64, 0x4cc00\n"
    "buffer_load_dwordx4 v254, s[24:27], s42 offen lds\n"
    "buffer_load_dwordx4 v254, s[24:27], s43 offen offset:1024 lds\n"
    "buffer_load_dwordx4 v254, s[24:27], s44 offen offset:2048 lds\n"
    "buffer_load_dwordx4 v254, s[24:27], s45 offen offset:3072 lds\n"
    "s_add_u32 m0, s59, 0x5000\n"
    "s_add_u32 s42, s64, 0x50000\n"
    "s_add_u32 s43, s64, 0x52400\n"
    "s_add_u32 s44, s64, 0x54800\n"
    "s_add_u32 s45, s64, 0x56c00\n"
    "buffer_load_dwordx4 v254, s[24:27], s42 offen lds\n"
    "buffer_load_dwordx4 v254, s[24:27], s43 offen offset:1024 lds\n"
    "buffer_load_dwordx4 v254, s[24:27], s44 offen offset:2048 lds\n"
    "buffer_load_dwordx4 v254, s[24:27], s45 offen offset:3072 lds\n"
    "s_waitcnt lgkmcnt(0)\n"
    "s_barrier\n"
    "s_waitcnt vmcnt(25)\n"
    "v_subrev_u32_e32 v221, s95, v224\n"
    "v_cmp_lt_u32_e32 vcc, 10, v221\n"
    "s_nop 1\n"
    "v_cndmask_b32_e64 v221, v221, 0, vcc\n"
    "s_nop 1\n"
    "v_min_u32_dpp v221, v221, v221 row_ror:8 row_mask:0xf bank_mask:0xf\n"
    "s_nop 1\n"
    "v_min_u32_dpp v221, v221, v221 row_ror:4 row_mask:0xf bank_mask:0xf\n"
    "s_nop 1\n"
    "v_min_u32_dpp v221, v221, v221 row_ror:2 row_mask:0xf bank_mask:0xf\n"
    "s_nop 1\n"
    "v_min_u32_dpp v221, v221, v221 row_ror:1 row_mask:0xf bank_mask:0xf\n"
    "ds_write_b32 v222, v221\n"
    "v_mov_b32_e64 v226, 0\n"
    "buffer_atomic_or v226, v217, s[28:31], s92 offen sc0\n"
    "s_add_u32 m0, s59, 0x6000\n"
    "s_add_u32 s42, s64, 0x5a000\n"
    "s_add_u32 s43, s64, 0x5c400\n"
    "s_add_u32 s44, s64, 0x5e800\n"
    "s_add_u32 s45, s64, 0x60c00\n"
    "buffer_load_dwordx4 v254, s[24:27], s42 offen lds\n"
    "buffer_load_dwordx4 v254, s[24:27], s43 offen offset:1024 lds\n"
    "buffer_load_dwordx4 v254, s[24:27], s44 offen offset:2048 lds\n"
    "buffer_load_dwordx4 v254, s[24:27], s45 offen offset:3072 lds\n"
    "s_add_u32 m0, s59, 0x7000\n"
    "s_add_u32 s42, s64, 0x64000\n"
    "s_add_u32 s43, s64, 0x66400\n"
    "s_add_u32 s44, s64, 0x68800\n"
    "s_add_u32 s45, s64, 0x6ac00\n"
    "buffer_load_dwordx4 v254, s[24:27], s42 offen lds\n"
    "buffer_load_dwordx4 v254, s[24:27], s43 offen offset:1024 lds\n"
    "buffer_load_dwordx4 v254, s[24:27], s44 offen offset:2048 lds\n"
    "buffer_load_dwordx4 v254, s[24:27], s45 offen offset:3072 lds\n"
    "s_add_u32 m0, s59, 0x8000\n"
    "s_add_u32 s42, s64, 0x6e000\n"
    "s_add_u32 s43, s64, 0x70400\n"
    "s_add_u32 s44, s64, 0x72800\n"
    "s_add_u32 s45, s64, 0x74c00\n"
    "buffer_load_dwordx4 v254, s[24:27], s42 offen lds\n"
    "buffer_load_dwordx4 v254, s[24:27], s43 offen offset:1024 lds\n"
    "buffer_load_dwordx4 v254, s[24:27], s44 offen offset:2048 lds\n"
    "buffer_load_dwordx4 v254, s[24:27], s45 offen offset:3072 lds\n"
    "s_waitcnt lgkmcnt(0)\n"
    "s_barrier\n"
    "s_waitcnt vmcnt(25)\n"
    "v_subrev_u32_e32 v221, s95, v225\n"
    "v_cmp_lt_u32_e32 vcc, 10, v221\n"
    "s_nop 1\n"
    "v_cndmask_b32_e64 v221, v221, 0, vcc\n"
    "s_nop 1\n"
    "v_min_u32_dpp v221, v221, v221 row_ror:8 row_mask:0xf bank_mask:0xf\n"
    "s_nop 1\n"
    "v_min_u32_dpp v221, v221, v221 row_ror:4 row_mask:0xf bank_mask:0xf\n"
    "s_nop 1\n"
    "v_min_u32_dpp v221, v221, v221 row_ror:2 row_mask:0xf bank_mask:0xf\n"
    "s_nop 1\n"
    "v_min_u32_dpp v221, v221, v221 row_ror:1 row_mask:0xf bank_mask:0xf\n"
    "ds_write_b32 v222, v221\n"
    "v_mov_b32_e64 v227, 0\n"
    "buffer_atomic_or v227, v217, s[28:31], s92 offen sc0\n"
    "s_waitcnt vmcnt(1) lgkmcnt(0)\n"
    "s_barrier\n"
    "s_waitcnt vmcnt(13)\n"
    "v_subrev_u32_e32 v221, s95, v226\n"
    "v_cmp_lt_u32_e32 vcc, 10, v221\n"
    "s_nop 1\n"
    "v_cndmask_b32_e64 v221, v221, 0, vcc\n"
    "s_nop 1\n"
    "v_min_u32_dpp v221, v221, v221 row_ror:8 row_mask:0xf bank_mask:0xf\n"
    "s_nop 1\n"
    "v_min_u32_dpp v221, v221, v221 row_ror:4 row_mask:0xf bank_mask:0xf\n"
    "s_nop 1\n"
    "v_min_u32_dpp v221, v221, v221 row_ror:2 row_mask:0xf bank_mask:0xf\n"
    "s_nop 1\n"
    "v_min_u32_dpp v221, v221, v221 row_ror:1 row_mask:0xf bank_mask:0xf\n"
    "ds_write_b32 v222, v221\n"
    "v_mov_b32_e64 v224, 0\n"
    "buffer_atomic_or v224, v217, s[28:31], s92 offen sc0\n"
    "s_mov_b32 s97, 9\n"
    "s_add_u32 s43, s97, 1\n"
    "s_sub_u32 s44, s97, 8\n"
    "s_cmp_lt_u32 s97, 8\n"
    "s_cselect_b32 s43, s43, s44\n"
    "s_cmp_eq_u32 s97, 0\n"
    "s_cselect_b32 s43, 9, s43\n"
    "s_mul_i32 s44, s43, 0x3800\n"
    "s_add_u32 s44, s44, s36\n"
    "s_lshl_b32 s54, s43, 7\n"
    "s_add_u32 s54, s54, s37\n"
    "s_cmp_ge_u32 s86, 14\n"
    "s_cbranch_scc1 .Lfz_END\n"
    ".Lfz_PL:\n"
    "s_waitcnt vmcnt(1)\n"
    "v_subrev_u32_e32 v221, s95, v227\n"
    "v_cmp_lt_u32_e32 vcc, 10, v221\n"
    "s_nop 1\n"
    "v_cndmask_b32_e64 v221, v221, 0, vcc\n"
    "s_nop 1\n"
    "v_min_u32_dpp v221, v221, v221 row_ror:8 row_mask:0xf bank_mask:0xf\n"
    "s_nop 1\n"
    "v_min_u32_dpp v221, v221, v221 row_ror:4 row_mask:0xf bank_mask:0xf\n"
    "s_nop 1\n"
    "v_min_u32_dpp v221, v221, v221 row_ror:2 row_mask:0xf bank_mask:0xf\n"
    "s_nop 1\n"
    "v_min_u32_dpp v221, v221, v221 row_ror:1 row_mask:0xf bank_mask:0xf\n"
    "ds_write_b32 v222, v221\n"
    "v_mov_b32_e64 v225, 0\n"
    "buffer_atomic_or v225, v217, s[28:31], s92 offen sc0\n"
    "v_readfirstlane_b32 s42, v221\n"
    "s_sub_u32 s96, s96, 1\n"
    "s_cselect_b32 s43, 0, 10\n"
    "s_nop 1\n"
    "s_cmp_ge_u32 s42, s43\n"
    "s_cbranch_scc1 .Lfz_PG\n"
    "s_sleep 4\n"
    "s_waitcnt vmcnt(1)\n"
    "v_subrev_u32_e32 v221, s95, v224\n"
    "v_cmp_lt_u32_e32 vcc, 10, v221\n"
    "s_nop 1\n"
    "v_cndmask_b32_e64 v221, v221, 0, vcc\n"
    "s_nop 1\n"
    "v_min_u32_dpp v221, v221, v221 row_ror:8 row_mask:0xf bank_mask:0xf\n"
    "s_nop 1\n"
    "v_min_u32_dpp v221, v221, v221 row_ror:4 row_mask:0xf bank_mask:0xf\n"
    "s_nop 1\n"
    "v_min_u32_dpp v221, v221, v221 row_ror:2 row_mask:0xf bank_mask:0xf\n"
    "s_nop 1\n"
    "v_min_u32_dpp v221, v221, v221 row_ror:1 row_mask:0xf bank_mask:0xf\n"
    "ds_write_b32 v222, v221\n"
    "v_mov_b32_e32 v226, 0\n"
    "buffer_atomic_or v226, v217, s[28:31], s92 offen sc0\n"
    "v_readfirstlane_b32 s42, v221\n"
    "s_sub_u32 s96, s96, 1\n"
    "s_cselect_b32 s43, 0, 10\n"
    "s_nop 1\n"
    "s_cmp_ge_u32 s42, s43\n"
    "s_cbranch_scc1 .Lfz_PG\n"
    "s_sleep 4\n"
    "s_waitcnt vmcnt(1)\n"
    "v_subrev_u32_e32 v221, s95, v225\n"
    "v_cmp_lt_u32_e32 vcc, 10, v221\n"
    "s_nop 1\n"
    "v_cndmask_b32_e64 v221, v221, 0, vcc\n"
    "s_nop 1\n"
    "v_min_u32_dpp v221, v221, v221 row_ror:8 row_mask:0xf bank_mask:0xf\n"
    "s_nop 1\n"
    "v_min_u32_dpp v221, v221, v221 row_ror:4 row_mask:0xf bank_mask:0xf\n"
    "s_nop 1\n"
    "v_min_u32_dpp v221, v221, v221 row_ror:2 row_mask:0xf bank_mask:0xf\n"
    "s_nop 1\n"
    "v_min_u32_dpp v221, v221, v221 row_ror:1 row_mask:0xf bank_mask:0xf\n"
    "ds_write_b32 v222, v221\n"
    "v_mov_b32_e32 v227, 0\n"
    "buffer_atomic_or v227, v217, s[28:31], s92 offen sc0\n"
    "v_readfirstlane_b32 s42, v221\n"
    "s_sub_u32 s96, s96, 1\n"
    "s_cselect_b32 s43, 0, 10\n"
    "s_nop 1\n"
    "s_cmp_ge_u32 s42, s43\n"
    "s_cbranch_scc1 .Lfz_PG\n"
    "s_sleep 4\n"
    "s_waitcnt vmcnt(1)\n"
    "v_subrev_u32_e32 v221, s95, v226\n"
    "v_cmp_lt_u32_e32 vcc, 10, v221\n"
    "s_nop 1\n"
    "v_cndmask_b32_e64 v221, v221, 0, vcc\n"
    "s_nop 1\n"
    "v_min_u32_dpp v221, v221, v221 row_ror:8 row_mask:0xf bank_mask:0xf\n"
    "s_nop 1\n"
    "v_min_u32_dpp v221, v221, v221 row_ror:4 row_mask:0xf bank_mask:0xf\n"
    "s_nop 1\n"
    "v_min_u32_dpp v221, v221, v221 row_ror:2 row_mask:0xf bank_mask:0xf\n"
    "s_nop 1\n"
    "v_min_u32_dpp v221, v221, v221 row_ror:1 row_mask:0xf bank_mask:0xf\n"
    "ds_write_b32 v222, v221\n"
    "v_mov_b32_e32 v224, 0\n"
    "buffer_atomic_or v224, v217, s[28:31], s92 offen sc0\n"
    "v_readfirstlane_b32 s42, v221\n"
    "s_sub_u32 s96, s96, 1\n"
    "s_cselect_b32 s43, 0, 10\n"
    "s_nop 1\n"
    "s_cmp_ge_u32 s42, s43\n"
    "s_cbranch_scc1 .Lfz_PG\n"
    "s_sleep 4\n"
    "s_branch .Lfz_PL\n"
    ".Lfz_PG:\n"
    "s_mov_b32 s45, s87\n"
    "s_add_u32 s45, s45, 0x23000\n"
    "s_sub_u32 s43, s45, 0x230000\n"
    "s_cselect_b32 s45, s45, s43\n"
    "s_add_u32 s43, s44, s45\n"
    "buffer_load_dwordx4 v[4:7], v241, s[28:31], s43 offen sc1\n"
    "s_add_u32 s45, s45, 0x23000\n"
    "s_sub_u32 s43, s45, 0x230000\n"
    "s_cselect_b32 s45, s45, s43\n"
    "s_add_u32 s43, s44, s45\n"
    "buffer_load_dwordx4 v[8:11], v241, s[28:31], s43 offen sc1\n"
    "s_add_u32 s45, s45, 0x23000\n"
    "s_sub_u32 s43, s45, 0x230000\n"
    "s_cselect_b32 s45, s45, s43\n"
    "s_add_u32 s43, s44, s45\n"
    "buffer_load_dwordx4 v[12:15], v241, s[28:31], s43 offen sc1\n"
    "s_add_u32 s45, s45, 0x23000\n"
    "s_sub_u32 s43, s45, 0x230000\n"
    "s_cselect_b32 s45, s45, s43\n"
    "s_add_u32 s43, s44, s45\n"
    "buffer_load_dwordx4 v[16:19], v241, s[28:31], s43 offen sc1\n"
    "s_add_u32 s45, s45, 0x23000\n"
    "s_sub_u32 s43, s45, 0x230000\n"
    "s_cselect_b32 s45, s45, s43\n"
    "s_add_u32 s43, s44, s45\n"
    "buffer_load_dwordx4 v[20:23], v241, s[28:31], s43 offen sc1\n"
    "s_add_u32 s45, s45, 0x23000\n"
    "s_sub_u32 s43, s45, 0x230000\n"
    "s_cselect_b32 s45, s45, s43\n"
    "s_add_u32 s43, s44, s45\n"
    "buffer_load_dwordx4 v[24:27], v241, s[28:31], s43 offen sc1\n"
    "s_add_u32 s45, s45, 0x23000\n"
    "s_sub_u32 s43, s45, 0x230000\n"
    "s_cselect_b32 s45, s45, s43\n"
    "s_add_u32 s43, s44, s45\n"
    "buffer_load_dwordx4 v[28:31], v241, s[28:31], s43 offen sc1\n"
    "s_add_u32 s45, s45, 0x23000\n"
    "s_sub_u32 s43, s45, 0x230000\n"
    "s_cselect_b32 s45, s45, s43\n"
    "s_add_u32 s43, s44, s45\n"
    "buffer_load_dwordx4 v[32:35], v241, s[28:31], s43 offen sc1\n"
    "s_add_u32 s45, s45, 0x23000\n"
    "s_sub_u32 s43, s45, 0x230000\n"
    "s_cselect_b32 s45, s45, s43\n"
    "s_add_u32 s43, s44, s45\n"
    "buffer_load_dwordx4 v[36:39], v241, s[28:31], s43 offen sc1\n"
    "s_add_u32 s45, s45, 0x23000\n"
    "s_sub_u32 s43, s45, 0x230000\n"
    "s_cselect_b32 s45, s45, s43\n"
    "s_add_u32 s43, s44, s45\n"
    "buffer_load_dwordx4 v[40:43], v241, s[28:31], s43 offen sc1\n"
    "s_add_u32 s45, s45, 0x23000\n"
    "s_sub_u32 s43, s45, 0x230000\n"
    "s_cselect_b32 s45, s45, s43\n"
    "s_add_u32 s43, s44, s45\n"
    "buffer_load_dwordx4 v[44:47], v241, s[28:31], s43 offen sc1\n"
    "s_add_u32 s45, s45, 0x23000\n"
    "s_sub_u32 s43, s45, 0x230000\n"
    "s_cselect_b32 s45, s45, s43\n"
    "s_add_u32 s43, s44, s45\n"
    "buffer_load_dwordx4 v[48:51], v241, s[28:31], s43 offen sc1\n"
    "s_add_u32 s45, s45, 0x23000\n"
    "s_sub_u32 s43, s45, 0x230000\n"
    "s_cselect_b32 s45, s45, s43\n"
    "s_add_u32 s43, s44, s45\n"
    "buffer_load_dwordx4 v[52:55], v241, s[28:31], s43 offen sc1\n"
    "s_add_u32 s45, s45, 0x23000\n"
    "s_sub_u32 s43, s45, 0x230000\n"
    "s_cselect_b32 s45, s45, s43\n"
    "s_add_u32 s43, s44, s45\n"
    "buffer_load_dwordx4 v[56:59], v241, s[28:31], s43 offen sc1\n"
    "s_add_u32 s45, s45, 0x23000\n"
    "s_sub_u32 s43, s45, 0x230000\n"
    "s_cselect_b32 s45, s45, s43\n"
    "s_add_u32 s43, s44, s45\n"
    "buffer_load_dwordx4 v[60:63], v241, s[28:31], s43 offen sc1\n"
    "s_lshl_b32 s45, s97, 10\n"
    "s_add_u32 s45, s45, s59\n"
    "v_add_u32_e32 v218, s45, v241\n"
    "ds_read_b128 v[0:3], v218 offset:37888\n"
    "v_mov_b32_e64 v221, 10\n"
    "ds_write_b32 v222, v221\n"
    "s_waitcnt lgkmcnt(1)\n"
    "v_mfma_f32_16x16x32_bf16 v[200:203], v[192:195], v[0:3], 0\n"
    "v_mfma_f32_16x16x32_bf16 v[204:207], v[196:199], v[0:3], 0\n"
    "s_waitcnt vmcnt(14)\n"
    "v_mfma_f32_16x16x32_bf16 v[200:203], v[192:195], v[4:7], v[200:203]\n"
    "v_mfma_f32_16x16x32_bf16 v[204:207], v[196:199], v[4:7], v[204:207]\n"
    "s_waitcnt vmcnt(13)\n"
    "v_mfma_f32_16x16x32_bf16 v[200:203], v[192:195], v[8:11], v[200:203]\n"
    "v_mfma_f32_16x16x32_bf16 v[204:207], v[196:199], v[8:11], v[204:207]\n"
    "s_waitcnt vmcnt(12)\n"
    "v_mfma_f32_16x16x32_bf16 v[200:203], v[192:195], v[12:15], v[200:203]\n"
    "v_mfma_f32_16x16x32_bf16 v[204:207], v[196:199], v[12:15], v[204:207]\n"
    "s_waitcnt vmcnt(11)\n"
    "v_mfma_f32_16x16x32_bf16 v[200:203], v[192:195], v[16:19], v[200:203]\n"
    "v_mfma_f32_16x16x32_bf16 v[204:207], v[196:199], v[16:19], v[204:207]\n"
    "s_waitcnt vmcnt(10)\n"
    "v_mfma_f32_16x16x32_bf16 v[200:203], v[192:195], v[20:23], v[200:203]\n"
    "v_mfma_f32_16x16x32_bf16 v[204:207], v[196:199], v[20:23], v[204:207]\n"
    "s_waitcnt vmcnt(9)\n"
    "v_mfma_f32_16x16x32_bf16 v[200:203], v[192:195], v[24:27], v[200:203]\n"
    "v_mfma_f32_16x16x32_bf16 v[204:207], v[196:199], v[24:27], v[204:207]\n"
    "s_waitcnt vmcnt(8)\n"
    "v_mfma_f32_16x16x32_bf16 v[200:203], v[192:195], v[28:31], v[200:203]\n"
    "v_mfma_f32_16x16x32_bf16 v[204:207], v[196:199], v[28:31], v[204:207]\n"
    "s_waitcnt vmcnt(7)\n"
    "v_mfma_f32_16x16x32_bf16 v[200:203], v[192:195], v[32:35], v[200:203]\n"
    "v_mfma_f32_16x16x32_bf16 v[204:207], v[196:199], v[32:35], v[204:207]\n"
    "s_waitcnt vmcnt(6)\n"
    "v_mfma_f32_16x16x32_bf16 v[200:203], v[192:195], v[36:39], v[200:203]\n"
    "v_mfma_f32_16x16x32_bf16 v[204:207], v[196:199], v[36:39], v[204:207]\n"
    "s_waitcnt vmcnt(5)\n"
    "v_mfma_f32_16x16x32_bf16 v[200:203], v[192:195], v[40:43], v[200:203]\n"
    "v_mfma_f32_16x16x32_bf16 v[204:207], v[196:199], v[40:43], v[204:207]\n"
    "s_waitcnt vmcnt(4)\n"
    "v_mfma_f32_16x16x32_bf16 v[200:203], v[192:195], v[44:47], v[200:203]\n"
    "v_mfma_f32_16x16x32_bf16 v[204:207], v[196:199], v[44:47], v[204:207]\n"
    "s_waitcnt vmcnt(3)\n"
    "v_mfma_f32_16x16x32_bf16 v[200:203], v[192:195], v[48:51], v[200:203]\n"
    "v_mfma_f32_16x16x32_bf16 v[204:207], v[196:199], v[48:51], v[204:207]\n"
    "s_waitcnt vmcnt(2)\n"
    "v_mfma_f32_16x16x32_bf16 v[200:203], v[192:195], v[52:55], v[200:203]\n"
    "v_mfma_f32_16x16x32_bf16 v[204:207], v[196:199], v[52:55], v[204:207]\n"
    "s_waitcnt vmcnt(1)\n"
    "v_mfma_f32_16x16x32_bf16 v[200:203], v[192:195], v[56:59], v[200:203]\n"
    "v_mfma_f32_16x16x32_bf16 v[204:207], v[196:199], v[56:59], v[204:207]\n"
    "s_waitcnt vmcnt(0)\n"
    "v_mfma_f32_16x16x32_bf16 v[200:203], v[192:195], v[60:63], v[200:203]\n"
    "v_mfma_f32_16x16x32_bf16 v[204:207], v[196:199], v[60:63], v[204:207]\n"
    "s_nop 7\n"
    "s_nop 1\n"
    "v_cvt_pk_bf16_f32 v212, v200, v201\n"
    "v_cvt_pk_bf16_f32 v213, v202, v203\n"
    "v_cvt_pk_bf16_f32 v214, v204, v205\n"
    "v_cvt_pk_bf16_f32 v215, v206, v207\n"
    "buffer_store_dwordx4 v[212:215], v216, s[88:91], s54 offen sc1\n"
    "s_branch .Lfz_END\n"
    ".Lfz_LD:\n"
    "s_waitcnt lgkmcnt(0)\n"
    "s_barrier\n"
    "s_waitcnt vmcnt(21) lgkmcnt(0)\n"
    "s_barrier\n"
    "s_waitcnt vmcnt(0) lgkmcnt(0)\n"
    "s_barrier\n"
    "s_mov_b32 s41, 7\n"
    "s_cmp_ge_u32 s86, 14\n"
    "s_cselect_b32 s97, 64, s33\n"
    "s_cselect_b32 s98, -1, 0\n"
    "s_add_u32 s43, s97, 1\n"
    "s_sub_u32 s44, s97, 8\n"
    "s_cmp_lt_u32 s97, 8\n"
    "s_cselect_b32 s43, s43, s44\n"
    "s_cmp_eq_u32 s97, 0\n"
    "s_cselect_b32 s43, 9, s43\n"
    "s_mul_i32 s44, s43, 0x3800\n"
    "s_add_u32 s44, s44, s36\n"
    "s_lshl_b32 s54, s43, 7\n"
    "s_add_u32 s54, s54, s37\n"
    "s_branch .Lfz_LC0\n"
    ".Lfz_LW0:\n"
    "s_sub_u32 s41, s41, 1\n"
    "s_cbranch_scc1 .Lfz_LN0\n"
    "s_barrier\n"
    "s_branch .Lfz_LC0\n"
    ".Lfz_LN0:\n"
    "s_cmp_lg_u32 s98, 0\n"
    "s_cbranch_scc1 .Lfz_END\n"
    "s_mov_b32 s41, 0\n"
    "s_sleep 1\n"
    ".Lfz_LC0:\n"
    "ds_read_b32 v223, v222\n"
    "s_waitcnt lgkmcnt(0)\n"
    "v_readfirstlane_b32 s42, v223\n"
    "s_nop 3\n"
    "s_cmp_gt_u32 s42, s97\n"
    "s_cbranch_scc0 .Lfz_LW0\n"
    "s_mov_b32 s45, s87\n"
    "s_add_u32 s45, s45, 0x23000\n"
    "s_sub_u32 s43, s45, 0x230000\n"
    "s_cselect_b32 s45, s45, s43\n"
    "s_add_u32 s43, s44, s45\n"
    "buffer_load_dwordx4 v[4:7], v241, s[28:31], s43 offen sc1\n"
    "s_add_u32 s45, s45, 0x23000\n"
    "s_sub_u32 s43, s45, 0x230000\n"
    "s_cselect_b32 s45, s45, s43\n"
    "s_add_u32 s43, s44, s45\n"
    "buffer_load_dwordx4 v[8:11], v241, s[28:31], s43 offen sc1\n"
    "s_add_u32 s45, s45, 0x23000\n"
    "s_sub_u32 s43, s45, 0x230000\n"
    "s_cselect_b32 s45, s45, s43\n"
    "s_add_u32 s43, s44, s45\n"
    "buffer_load_dwordx4 v[12:15], v241, s[28:31], s43 offen sc1\n"
    "s_add_u32 s45, s45, 0x23000\n"
    "s_sub_u32 s43, s45, 0x230000\n"
    "s_cselect_b32 s45, s45, s43\n"
    "s_add_u32 s43, s44, s45\n"
    "buffer_load_dwordx4 v[16:19], v241, s[28:31], s43 offen sc1\n"
    "s_add_u32 s45, s45, 0x23000\n"
    "s_sub_u32 s43, s45, 0x230000\n"
    "s_cselect_b32 s45, s45, s43\n"
    "s_add_u32 s43, s44, s45\n"
    "buffer_load_dwordx4 v[20:23], v241, s[28:31], s43 offen sc1\n"
    "s_add_u32 s45, s45, 0x23000\n"
    "s_sub_u32 s43, s45, 0x230000\n"
    "s_cselect_b32 s45, s45, s43\n"
    "s_add_u32 s43, s44, s45\n"
    "buffer_load_dwordx4 v[24:27], v241, s[28:31], s43 offen sc1\n"
    "s_add_u32 s45, s45, 0x23000\n"
    "s_sub_u32 s43, s45, 0x230000\n"
    "s_cselect_b32 s45, s45, s43\n"
    "s_add_u32 s43, s44, s45\n"
    "buffer_load_dwordx4 v[28:31], v241, s[28:31], s43 offen sc1\n"
    "s_add_u32 s45, s45, 0x23000\n"
    "s_sub_u32 s43, s45, 0x230000\n"
    "s_cselect_b32 s45, s45, s43\n"
    "s_add_u32 s43, s44, s45\n"
    "buffer_load_dwordx4 v[32:35], v241, s[28:31], s43 offen sc1\n"
    "s_add_u32 s45, s45, 0x23000\n"
    "s_sub_u32 s43, s45, 0x230000\n"
    "s_cselect_b32 s45, s45, s43\n"
    "s_add_u32 s43, s44, s45\n"
    "buffer_load_dwordx4 v[36:39], v241, s[28:31], s43 offen sc1\n"
    "s_add_u32 s45, s45, 0x23000\n"
    "s_sub_u32 s43, s45, 0x230000\n"
    "s_cselect_b32 s45, s45, s43\n"
    "s_add_u32 s43, s44, s45\n"
    "buffer_load_dwordx4 v[40:43], v241, s[28:31], s43 offen sc1\n"
    "s_add_u32 s45, s45, 0x23000\n"
    "s_sub_u32 s43, s45, 0x230000\n"
    "s_cselect_b32 s45, s45, s43\n"
    "s_add_u32 s43, s44, s45\n"
    "buffer_load_dwordx4 v[44:47], v241, s[28:31], s43 offen sc1\n"
    "s_add_u32 s45, s45, 0x23000\n"
    "s_sub_u32 s43, s45, 0x230000\n"
    "s_cselect_b32 s45, s45, s43\n"
    "s_add_u32 s43, s44, s45\n"
    "buffer_load_dwordx4 v[48:51], v241, s[28:31], s43 offen sc1\n"
    "s_add_u32 s45, s45, 0x23000\n"
    "s_sub_u32 s43, s45, 0x230000\n"
    "s_cselect_b32 s45, s45, s43\n"
    "s_add_u32 s43, s44, s45\n"
    "buffer_load_dwordx4 v[52:55], v241, s[28:31], s43 offen sc1\n"
    "s_add_u32 s45, s45, 0x23000\n"
    "s_sub_u32 s43, s45, 0x230000\n"
    "s_cselect_b32 s45, s45, s43\n"
    "s_add_u32 s43, s44, s45\n"
    "buffer_load_dwordx4 v[56:59], v241, s[28:31], s43 offen sc1\n"
    "s_add_u32 s45, s45, 0x23000\n"
    "s_sub_u32 s43, s45, 0x230000\n"
    "s_cselect_b32 s45, s45, s43\n"
    "s_add_u32 s43, s44, s45\n"
    "buffer_load_dwordx4 v[60:63], v241, s[28:31], s43 offen sc1\n"
    "s_lshl_b32 s45, s97, 10\n"
    "s_add_u32 s45, s45, s59\n"
    "v_add_u32_e32 v218, s45, v241\n"
    "ds_read_b128 v[0:3], v218 offset:37888\n"
    "s_add_u32 s97, s97, 3\n"
    "s_add_u32 s43, s97, 1\n"
    "s_sub_u32 s44, s97, 8\n"
    "s_cmp_lt_u32 s97, 8\n"
    "s_cselect_b32 s43, s43, s44\n"
    "s_cmp_eq_u32 s97, 0\n"
    "s_cselect_b32 s43, 9, s43\n"
    "s_mul_i32 s44, s43, 0x3800\n"
    "s_add_u32 s44, s44, s36\n"
    "s_lshl_b32 s55, s43, 7\n"
    "s_add_u32 s55, s55, s37\n"
    "s_branch .Lfz_LC1\n"
    ".Lfz_LW1:\n"
    "s_sub_u32 s41, s41, 1\n"
    "s_cbranch_scc1 .Lfz_LN1\n"
    "s_barrier\n"
    "s_branch .Lfz_LC1\n"
    ".Lfz_LN1:\n"
    "s_mov_b32 s41, 0\n"
    "s_sleep 1\n"
    ".Lfz_LC1:\n"
    "ds_read_b32 v223, v222\n"
    "s_waitcnt lgkmcnt(0)\n"
    "v_readfirstlane_b32 s42, v223\n"
    "s_nop 3\n"
    "s_cmp_gt_u32 s42, s97\n"
    "s_cbranch_scc0 .Lfz_LW1\n"
    "s_mov_b32 s45, s87\n"
    "s_add_u32 s45, s45, 0x23000\n"
    "s_sub_u32 s43, s45, 0x230000\n"
    "s_cselect_b32 s45, s45, s43\n"
    "s_add_u32 s43, s44, s45\n"
    "buffer_load_dwordx4 v[68:71], v241, s[28:31], s43 offen sc1\n"
    "s_add_u32 s45, s45, 0x23000\n"
    "s_sub_u32 s43, s45, 0x230000\n"
    "s_cselect_b32 s45, s45, s43\n"
    "s_add_u32 s43, s44, s45\n"
    "buffer_load_dwordx4 v[72:75], v241, s[28:31], s43 offen sc1\n"
    "s_add_u32 s45, s45, 0x23000\n"
    "s_sub_u32 s43, s45, 0x230000\n"
    "s_cselect_b32 s45, s45, s43\n"
    "s_add_u32 s43, s44, s45\n"
    "buffer_load_dwordx4 v[76:79], v241, s[28:31], s43 offen sc1\n"
    "s_add_u32 s45, s45, 0x23000\n"
    "s_sub_u32 s43, s45, 0x230000\n"
    "s_cselect_b32 s45, s45, s43\n"
    "s_add_u32 s43, s44, s45\n"
    "buffer_load_dwordx4 v[80:83], v241, s[28:31], s43 offen sc1\n"
    "s_add_u32 s45, s45, 0x23000\n"
    "s_sub_u32 s43, s45, 0x230000\n"
    "s_cselect_b32 s45, s45, s43\n"
    "s_add_u32 s43, s44, s45\n"
    "buffer_load_dwordx4 v[84:87], v241, s[28:31], s43 offen sc1\n"
    "s_add_u32 s45, s45, 0x23000\n"
    "s_sub_u32 s43, s45, 0x230000\n"
    "s_cselect_b32 s45, s45, s43\n"
    "s_add_u32 s43, s44, s45\n"
    "buffer_load_dwordx4 v[88:91], v241, s[28:31], s43 offen sc1\n"
    "s_add_u32 s45, s45, 0x23000\n"
    "s_sub_u32 s43, s45, 0x230000\n"
    "s_cselect_b32 s45, s45, s43\n"
    "s_add_u32 s43, s44, s45\n"
    "buffer_load_dwordx4 v[92:95], v241, s[28:31], s43 offen sc1\n"
    "s_add_u32 s45, s45, 0x23000\n"
    "s_sub_u32 s43, s45, 0x230000\n"
    "s_cselect_b32 s45, s45, s43\n"
    "s_add_u32 s43, s44, s45\n"
    "buffer_load_dwordx4 v[96:99], v241, s[28:31], s43 offen sc1\n"
    "s_add_u32 s45, s45, 0x23000\n"
    "s_sub_u32 s43, s45, 0x230000\n"
    "s_cselect_b32 s45, s45, s43\n"
    "s_add_u32 s43, s44, s45\n"
    "buffer_load_dwordx4 v[100:103], v241, s[28:31], s43 offen sc1\n"
    "s_add_u32 s45, s45, 0x23000\n"
    "s_sub_u32 s43, s45, 0x230000\n"
    "s_cselect_b32 s45, s45, s43\n"
    "s_add_u32 s43, s44, s45\n"
    "buffer_load_dwordx4 v[104:107], v241, s[28:31], s43 offen sc1\n"
    "s_add_u32 s45, s45, 0x23000\n"
    "s_sub_u32 s43, s45, 0x230000\n"
    "s_cselect_b32 s45, s45, s43\n"
    "s_add_u32 s43, s44, s45\n"
    "buffer_load_dwordx4 v[108:111], v241, s[28:31], s43 offen sc1\n"
    "s_add_u32 s45, s45, 0x23000\n"
    "s_sub_u32 s43, s45, 0x230000\n"
    "s_cselect_b32 s45, s45, s43\n"
    "s_add_u32 s43, s44, s45\n"
    "buffer_load_dwordx4 v[112:115], v241, s[28:31], s43 offen sc1\n"
    "s_add_u32 s45, s45, 0x23000\n"
    "s_sub_u32 s43, s45, 0x230000\n"
    "s_cselect_b32 s45, s45, s43\n"
    "s_add_u32 s43, s44, s45\n"
    "buffer_load_dwordx4 v[116:119], v241, s[28:31], s43 offen sc1\n"
    "s_add_u32 s45, s45, 0x23000\n"
    "s_sub_u32 s43, s45, 0x230000\n"
    "s_cselect_b32 s45, s45, s43\n"
    "s_add_u32 s43, s44, s45\n"
    "buffer_load_dwordx4 v[120:123], v241, s[28:31], s43 offen sc1\n"
    "s_add_u32 s45, s45, 0x23000\n"
    "s_sub_u32 s43, s45, 0x230000\n"
    "s_cselect_b32 s45, s45, s43\n"
    "s_add_u32 s43, s44, s45\n"
    "buffer_load_dwordx4 v[124:127], v241, s[28:31], s43 offen sc1\n"
    "s_lshl_b32 s45, s97, 10\n"
    "s_add_u32 s45, s45, s59\n"
    "v_add_u32_e32 v218, s45, v241\n"
    "ds_read_b128 v[64:67], v218 offset:37888\n"
    "s_add_u32 s97, s97, 3\n"
    "s_add_u32 s43, s97, 1\n"
    "s_sub_u32 s44, s97, 8\n"
    "s_cmp_lt_u32 s97, 8\n"
    "s_cselect_b32 s43, s43, s44\n"
    "s_cmp_eq_u32 s97, 0\n"
    "s_cselect_b32 s43, 9, s43\n"
    "s_mul_i32 s44, s43, 0x3800\n"
    "s_add_u32 s44, s44, s36\n"
    "s_lshl_b32 s56, s43, 7\n"
    "s_add_u32 s56, s56, s37\n"
    "s_branch .Lfz_LC2\n"
    ".Lfz_LW2:\n"
    "s_sub_u32 s41, s41, 1\n"
    "s_cbranch_scc1 .Lfz_LN2\n"
    "s_barrier\n"
    "s_branch .Lfz_LC2\n"
    ".Lfz_LN2:\n"
    "s_mov_b32 s41, 0\n"
    "s_sleep 1\n"
    ".Lfz_LC2:\n"
    "ds_read_b32 v223, v222\n"
    "s_waitcnt lgkmcnt(0)\n"
    "v_readfirstlane_b32 s42, v223\n"
    "s_nop 3\n"
    "s_cmp_gt_u32 s42, s97\n"
    "s_cbranch_scc0 .Lfz_LW2\n"
    "s_mov_b32 s45, s87\n"
    "s_add_u32 s45, s45, 0x23000\n"
    "s_sub_u32 s43, s45, 0x230000\n"
    "s_cselect_b32 s45, s45, s43\n"
    "s_add_u32 s43, s44, s45\n"
    "buffer_load_dwordx4 v[132:135], v241, s[28:31], s43 offen sc1\n"
    "s_add_u32 s45, s45, 0x23000\n"
    "s_sub_u32 s43, s45, 0x230000\n"
    "s_cselect_b32 s45, s45, s43\n"
    "s_add_u32 s43, s44, s45\n"
    "buffer_load_dwordx4 v[136:139], v241, s[28:31], s43 offen sc1\n"
    "s_add_u32 s45, s45, 0x23000\n"
    "s_sub_u32 s43, s45, 0x230000\n"
    "s_cselect_b32 s45, s45, s43\n"
    "s_add_u32 s43, s44, s45\n"
    "buffer_load_dwordx4 v[140:143], v241, s[28:31], s43 offen sc1\n"
    "s_add_u32 s45, s45, 0x23000\n"
    "s_sub_u32 s43, s45, 0x230000\n"
    "s_cselect_b32 s45, s45, s43\n"
    "s_add_u32 s43, s44, s45\n"
    "buffer_load_dwordx4 v[144:147], v241, s[28:31], s43 offen sc1\n"
    "s_add_u32 s45, s45, 0x23000\n"
    "s_sub_u32 s43, s45, 0x230000\n"
    "s_cselect_b32 s45, s45, s43\n"
    "s_add_u32 s43, s44, s45\n"
    "buffer_load_dwordx4 v[148:151], v241, s[28:31], s43 offen sc1\n"
    "s_add_u32 s45, s45, 0x23000\n"
    "s_sub_u32 s43, s45, 0x230000\n"
    "s_cselect_b32 s45, s45, s43\n"
    "s_add_u32 s43, s44, s45\n"
    "buffer_load_dwordx4 v[152:155], v241, s[28:31], s43 offen sc1\n"
    "s_add_u32 s45, s45, 0x23000\n"
    "s_sub_u32 s43, s45, 0x230000\n"
    "s_cselect_b32 s45, s45, s43\n"
    "s_add_u32 s43, s44, s45\n"
    "buffer_load_dwordx4 v[156:159], v241, s[28:31], s43 offen sc1\n"
    "s_add_u32 s45, s45, 0x23000\n"
    "s_sub_u32 s43, s45, 0x230000\n"
    "s_cselect_b32 s45, s45, s43\n"
    "s_add_u32 s43, s44, s45\n"
    "buffer_load_dwordx4 v[160:163], v241, s[28:31], s43 offen sc1\n"
    "s_add_u32 s45, s45, 0x23000\n"
    "s_sub_u32 s43, s45, 0x230000\n"
    "s_cselect_b32 s45, s45, s43\n"
    "s_add_u32 s43, s44, s45\n"
    "buffer_load_dwordx4 v[164:167], v241, s[28:31], s43 offen sc1\n"
    "s_add_u32 s45, s45, 0x23000\n"
    "s_sub_u32 s43, s45, 0x230000\n"
    "s_cselect_b32 s45, s45, s43\n"
    "s_add_u32 s43, s44, s45\n"
    "buffer_load_dwordx4 v[168:171], v241, s[28:31], s43 offen sc1\n"
    "s_add_u32 s45, s45, 0x23000\n"
    "s_sub_u32 s43, s45, 0x230000\n"
    "s_cselect_b32 s45, s45, s43\n"
    "s_add_u32 s43, s44, s45\n"
    "buffer_load_dwordx4 v[172:175], v241, s[28:31], s43 offen sc1\n"
    "s_add_u32 s45, s45, 0x23000\n"
    "s_sub_u32 s43, s45, 0x230000\n"
    "s_cselect_b32 s45, s45, s43\n"
    "s_add_u32 s43, s44, s45\n"
    "buffer_load_dwordx4 v[176:179], v241, s[28:31], s43 offen sc1\n"
    "s_add_u32 s45, s45, 0x23000\n"
    "s_sub_u32 s43, s45, 0x230000\n"
    "s_cselect_b32 s45, s45, s43\n"
    "s_add_u32 s43, s44, s45\n"
    "buffer_load_dwordx4 v[180:183], v241, s[28:31], s43 offen sc1\n"
    "s_add_u32 s45, s45, 0x23000\n"
    "s_sub_u32 s43, s45, 0x230000\n"
    "s_cselect_b32 s45, s45, s43\n"
    "s_add_u32 s43, s44, s45\n"
    "buffer_load_dwordx4 v[184:187], v241, s[28:31], s43 offen sc1\n"
    "s_add_u32 s45, s45, 0x23000\n"
    "s_sub_u32 s43, s45, 0x230000\n"
    "s_cselect_b32 s45, s45, s43\n"
    "s_add_u32 s43, s44, s45\n"
    "buffer_load_dwordx4 v[188:191], v241, s[28:31], s43 offen sc1\n"
    "s_lshl_b32 s45, s97, 10\n"
    "s_add_u32 s45, s45, s59\n"
    "v_add_u32_e32 v218, s45, v241\n"
    "ds_read_b128 v[128:131], v218 offset:37888\n"
    "s_add_u32 s97, s97, 3\n"
    "v_mfma_f32_16x16x32_bf16 v[200:203], v[192:195], v[0:3], 0\n"
    "v_mfma_f32_16x16x32_bf16 v[204:207], v[196:199], v[0:3], 0\n"
    "s_waitcnt vmcnt(44)\n"
    "v_mfma_f32_16x16x32_bf16 v[200:203], v[192:195], v[4:7], v[200:203]\n"
    "v_mfma_f32_16x16x32_bf16 v[204:207], v[196:199], v[4:7], v[204:207]\n"
    "s_waitcnt vmcnt(43)\n"
    "v_mfma_f32_16x16x32_bf16 v[200:203], v[192:195], v[8:11], v[200:203]\n"
    "v_mfma_f32_16x16x32_bf16 v[204:207], v[196:199], v[8:11], v[204:207]\n"
    "s_waitcnt vmcnt(42)\n"
    "v_mfma_f32_16x16x32_bf16 v[200:203], v[192:195], v[12:15], v[200:203]\n"
    "v_mfma_f32_16x16x32_bf16 v[204:207], v[196:199], v[12:15], v[204:207]\n"
    "s_waitcnt vmcnt(41)\n"
    "v_mfma_f32_16x16x32_bf16 v[200:203], v[192:195], v[16:19], v[200:203]\n"
    "v_mfma_f32_16x16x32_bf16 v[204:207], v[196:199], v[16:19], v[204:207]\n"
    "s_waitcnt vmcnt(40)\n"
    "v_mfma_f32_16x16x32_bf16 v[200:203], v[192:195], v[20:23], v[200:203]\n"
    "v_mfma_f32_16x16x32_bf16 v[204:207], v[196:199], v[20:23], v[204:207]\n"
    "s_waitcnt vmcnt(39)\n"
    "v_mfma_f32_16x16x32_bf16 v[200:203], v[192:195], v[24:27], v[200:203]\n"
    "v_mfma_f32_16x16x32_bf16 v[204:207], v[196:199], v[24:27], v[204:207]\n"
    "s_waitcnt vmcnt(38)\n"
    "v_mfma_f32_16x16x32_bf16 v[200:203], v[192:195], v[28:31], v[200:203]\n"
    "v_mfma_f32_16x16x32_bf16 v[204:207], v[196:199], v[28:31], v[204:207]\n"
    "s_waitcnt vmcnt(37)\n"
    "v_mfma_f32_16x16x32_bf16 v[200:203], v[192:195], v[32:35], v[200:203]\n"
    "v_mfma_f32_16x16x32_bf16 v[204:207], v[196:199], v[32:35], v[204:207]\n"
    "s_waitcnt vmcnt(36)\n"
    "v_mfma_f32_16x16x32_bf16 v[200:203], v[192:195], v[36:39], v[200:203]\n"
    "v_mfma_f32_16x16x32_bf16 v[204:207], v[196:199], v[36:39], v[204:207]\n"
    "s_waitcnt vmcnt(35)\n"
    "v_mfma_f32_16x16x32_bf16 v[200:203], v[192:195], v[40:43], v[200:203]\n"
    "v_mfma_f32_16x16x32_bf16 v[204:207], v[196:199], v[40:43], v[204:207]\n"
    "s_waitcnt vmcnt(34)\n"
    "v_mfma_f32_16x16x32_bf16 v[200:203], v[192:195], v[44:47], v[200:203]\n"
    "v_mfma_f32_16x16x32_bf16 v[204:207], v[196:199], v[44:47], v[204:207]\n"
    "s_waitcnt vmcnt(33)\n"
    "v_mfma_f32_16x16x32_bf16 v[200:203], v[192:195], v[48:51], v[200:203]\n"
    "v_mfma_f32_16x16x32_bf16 v[204:207], v[196:199], v[48:51], v[204:207]\n"
    "s_waitcnt vmcnt(32)\n"
    "v_mfma_f32_16x16x32_bf16 v[200:203], v[192:195], v[52:55], v[200:203]\n"
    "v_mfma_f32_16x16x32_bf16 v[204:207], v[196:199], v[52:55], v[204:207]\n"
    "s_waitcnt vmcnt(31)\n"
    "v_mfma_f32_16x16x32_bf16 v[200:203], v[192:195], v[56:59], v[200:203]\n"
    "v_mfma_f32_16x16x32_bf16 v[204:207], v[196:199], v[56:59], v[204:207]\n"
    "s_waitcnt vmcnt(30)\n"
    "v_mfma_f32_16x16x32_bf16 v[200:203], v[192:195], v[60:63], v[200:203]\n"
    "v_mfma_f32_16x16x32_bf16 v[204:207], v[196:199], v[60:63], v[204:207]\n"
    "s_nop 7\n"
    "s_nop 1\n"
    "v_cvt_pk_bf16_f32 v212, v200, v201\n"
    "v_cvt_pk_bf16_f32 v213, v202, v203\n"
    "v_cvt_pk_bf16_f32 v214, v204, v205\n"
    "v_cvt_pk_bf16_f32 v215, v206, v207\n"
    "buffer_store_dwordx4 v[212:215], v216, s[88:91], s54 offen sc1\n"
    "v_mfma_f32_16x16x32_bf16 v[200:203], v[192:195], v[64:67], 0\n"
    "v_mfma_f32_16x16x32_bf16 v[204:207], v[196:199], v[64:67], 0\n"
    "s_waitcnt vmcnt(30)\n"
    "v_mfma_f32_16x16x32_bf16 v[200:203], v[192:195], v[68:71], v[200:203]\n"
    "v_mfma_f32_16x16x32_bf16 v[204:207], v[196:199], v[68:71], v[204:207]\n"
    "s_waitcnt vmcnt(29)\n"
    "v_mfma_f32_16x16x32_bf16 v[200:203], v[192:195], v[72:75], v[200:203]\n"
    "v_mfma_f32_16x16x32_bf16 v[204:207], v[196:199], v[72:75], v[204:207]\n"
    "s_waitcnt vmcnt(28)\n"
    "v_mfma_f32_16x16x32_bf16 v[200:203], v[192:195], v[76:79], v[200:203]\n"
    "v_mfma_f32_16x16x32_bf16 v[204:207], v[196:199], v[76:79], v[204:207]\n"
    "s_waitcnt vmcnt(27)\n"
    "v_mfma_f32_16x16x32_bf16 v[200:203], v[192:195], v[80:83], v[200:203]\n"
    "v_mfma_f32_16x16x32_bf16 v[204:207], v[196:199], v[80:83], v[204:207]\n"
    "s_waitcnt vmcnt(26)\n"
    "v_mfma_f32_16x16x32_bf16 v[200:203], v[192:195], v[84:87], v[200:203]\n"
    "v_mfma_f32_16x16x32_bf16 v[204:207], v[196:199], v[84:87], v[204:207]\n"
    "s_waitcnt vmcnt(25)\n"
    "v_mfma_f32_16x16x32_bf16 v[200:203], v[192:195], v[88:91], v[200:203]\n"
    "v_mfma_f32_16x16x32_bf16 v[204:207], v[196:199], v[88:91], v[204:207]\n"
    "s_waitcnt vmcnt(24)\n"
    "v_mfma_f32_16x16x32_bf16 v[200:203], v[192:195], v[92:95], v[200:203]\n"
    "v_mfma_f32_16x16x32_bf16 v[204:207], v[196:199], v[92:95], v[204:207]\n"
    "s_waitcnt vmcnt(23)\n"
    "v_mfma_f32_16x16x32_bf16 v[200:203], v[192:195], v[96:99], v[200:203]\n"
    "v_mfma_f32_16x16x32_bf16 v[204:207], v[196:199], v[96:99], v[204:207]\n"
    "s_waitcnt vmcnt(22)\n"
    "v_mfma_f32_16x16x32_bf16 v[200:203], v[192:195], v[100:103], v[200:203]\n"
    "v_mfma_f32_16x16x32_bf16 v[204:207], v[196:199], v[100:103], v[204:207]\n"
    "s_waitcnt vmcnt(21)\n"
    "v_mfma_f32_16x16x32_bf16 v[200:203], v[192:195], v[104:107], v[200:203]\n"
    "v_mfma_f32_16x16x32_bf16 v[204:207], v[196:199], v[104:107], v[204:207]\n"
    "s_waitcnt vmcnt(20)\n"
    "v_mfma_f32_16x16x32_bf16 v[200:203], v[192:195], v[108:111], v[200:203]\n"
    "v_mfma_f32_16x16x32_bf16 v[204:207], v[196:199], v[108:111], v[204:207]\n"
    "s_waitcnt vmcnt(19)\n"
    "v_mfma_f32_16x16x32_bf16 v[200:203], v[192:195], v[112:115], v[200:203]\n"
    "v_mfma_f32_16x16x32_bf16 v[204:207], v[196:199], v[112:115], v[204:207]\n"
    "s_waitcnt vmcnt(18)\n"
    "v_mfma_f32_16x16x32_bf16 v[200:203], v[192:195], v[116:119], v[200:203]\n"
    "v_mfma_f32_16x16x32_bf16 v[204:207], v[196:199], v[116:119], v[204:207]\n"
    "s_waitcnt vmcnt(17)\n"
    "v_mfma_f32_16x16x32_bf16 v[200:203], v[192:195], v[120:123], v[200:203]\n"
    "v_mfma_f32_16x16x32_bf16 v[204:207], v[196:199], v[120:123], v[204:207]\n"
    "s_waitcnt vmcnt(16)\n"
    "v_mfma_f32_16x16x32_bf16 v[200:203], v[192:195], v[124:127], v[200:203]\n"
    "v_mfma_f32_16x16x32_bf16 v[204:207], v[196:199], v[124:127], v[204:207]\n"
    "s_nop 7\n"
    "s_nop 1\n"
    "v_cvt_pk_bf16_f32 v212, v200, v201\n"
    "v_cvt_pk_bf16_f32 v213, v202, v203\n"
    "v_cvt_pk_bf16_f32 v214, v204, v205\n"
    "v_cvt_pk_bf16_f32 v215, v206, v207\n"
    "buffer_store_dwordx4 v[212:215], v216, s[88:91], s55 offen sc1\n"
    "s_waitcnt lgkmcnt(0)\n"
    "v_mfma_f32_16x16x32_bf16 v[200:203], v[192:195], v[128:131], 0\n"
    "v_mfma_f32_16x16x32_bf16 v[204:207], v[196:199], v[128:131], 0\n"
    "s_waitcnt vmcnt(16)\n"
    "v_mfma_f32_16x16x32_bf16 v[200:203], v[192:195], v[132:135], v[200:203]\n"
    "v_mfma_f32_16x16x32_bf16 v[204:207], v[196:199], v[132:135], v[204:207]\n"
    "s_waitcnt vmcnt(15)\n"
    "v_mfma_f32_16x16x32_bf16 v[200:203], v[192:195], v[136:139], v[200:203]\n"
    "v_mfma_f32_16x16x32_bf16 v[204:207], v[196:199], v[136:139], v[204:207]\n"
    "s_waitcnt vmcnt(14)\n"
    "v_mfma_f32_16x16x32_bf16 v[200:203], v[192:195], v[140:143], v[200:203]\n"
    "v_mfma_f32_16x16x32_bf16 v[204:207], v[196:199], v[140:143], v[204:207]\n"
    "s_waitcnt vmcnt(13)\n"
    "v_mfma_f32_16x16x32_bf16 v[200:203], v[192:195], v[144:147], v[200:203]\n"
    "v_mfma_f32_16x16x32_bf16 v[204:207], v[196:199], v[144:147], v[204:207]\n"
    "s_waitcnt vmcnt(12)\n"
    "v_mfma_f32_16x16x32_bf16 v[200:203], v[192:195], v[148:151], v[200:203]\n"
    "v_mfma_f32_16x16x32_bf16 v[204:207], v[196:199], v[148:151], v[204:207]\n"
    "s_waitcnt vmcnt(11)\n"
    "v_mfma_f32_16x16x32_bf16 v[200:203], v[192:195], v[152:155], v[200:203]\n"
    "v_mfma_f32_16x16x32_bf16 v[204:207], v[196:199], v[152:155], v[204:207]\n"
    "s_waitcnt vmcnt(10)\n"
    "v_mfma_f32_16x16x32_bf16 v[200:203], v[192:195], v[156:159], v[200:203]\n"
    "v_mfma_f32_16x16x32_bf16 v[204:207], v[196:199], v[156:159], v[204:207]\n"
    "s_waitcnt vmcnt(9)\n"
    "v_mfma_f32_16x16x32_bf16 v[200:203], v[192:195], v[160:163], v[200:203]\n"
    "v_mfma_f32_16x16x32_bf16 v[204:207], v[196:199], v[160:163], v[204:207]\n"
    "s_waitcnt vmcnt(8)\n"
    "v_mfma_f32_16x16x32_bf16 v[200:203], v[192:195], v[164:167], v[200:203]\n"
    "v_mfma_f32_16x16x32_bf16 v[204:207], v[196:199], v[164:167], v[204:207]\n"
    "s_waitcnt vmcnt(7)\n"
    "v_mfma_f32_16x16x32_bf16 v[200:203], v[192:195], v[168:171], v[200:203]\n"
    "v_mfma_f32_16x16x32_bf16 v[204:207], v[196:199], v[168:171], v[204:207]\n"
    "s_waitcnt vmcnt(6)\n"
    "v_mfma_f32_16x16x32_bf16 v[200:203], v[192:195], v[172:175], v[200:203]\n"
    "v_mfma_f32_16x16x32_bf16 v[204:207], v[196:199], v[172:175], v[204:207]\n"
    "s_waitcnt vmcnt(5)\n"
    "v_mfma_f32_16x16x32_bf16 v[200:203], v[192:195], v[176:179], v[200:203]\n"
    "v_mfma_f32_16x16x32_bf16 v[204:207], v[196:199], v[176:179], v[204:207]\n"
    "s_waitcnt vmcnt(4)\n"
    "v_mfma_f32_16x16x32_bf16 v[200:203], v[192:195], v[180:183], v[200:203]\n"
    "v_mfma_f32_16x16x32_bf16 v[204:207], v[196:199], v[180:183], v[204:207]\n"
    "s_waitcnt vmcnt(3)\n"
    "v_mfma_f32_16x16x32_bf16 v[200:203], v[192:195], v[184:187], v[200:203]\n"
    "v_mfma_f32_16x16x32_bf16 v[204:207], v[196:199], v[184:187], v[204:207]\n"
    "s_waitcnt vmcnt(2)\n"
    "v_mfma_f32_16x16x32_bf16 v[200:203], v[192:195], v[188:191], v[200:203]\n"
    "v_mfma_f32_16x16x32_bf16 v[204:207], v[196:199], v[188:191], v[204:207]\n"
    "s_nop 7\n"
    "s_nop 1\n"
    "v_cvt_pk_bf16_f32 v212, v200, v201\n"
    "v_cvt_pk_bf16_f32 v213, v202, v203\n"
    "v_cvt_pk_bf16_f32 v214, v204, v205\n"
    "v_cvt_pk_bf16_f32 v215, v206, v207\n"
    "buffer_store_dwordx4 v[212:215], v216, s[88:91], s56 offen sc1\n"
    "s_branch .Lfz_END\n"
    ".Lfz_OA:\n"
    "v_mov_b32_e32 v252, v226\n"
    "v_mov_b32_e32 v244, v240\n"
    "v_xor_b32_e32 v245, 32, v240\n"
    "v_xor_b32_e32 v246, 64, v240\n"
    "v_xor_b32_e32 v247, 0x60, v240\n"
    "s_lshl_b32 s42, s33, 12\n"
    "s_cmp_lt_u32 s33, 3\n"
    "s_cselect_b32 s43, 0x800, 2.0\n"
    "s_cselect_b32 s44, 0x800, 0\n"
    "s_add_u32 s45, s59, 0x21000\n"
    "v_add_u32_e32 v248, s42, v241\n"
    "v_add_u32_e32 v249, s43, v248\n"
    "v_add_u32_e32 v250, s45, v248\n"
    "v_add_u32_e32 v251, s44, v250\n"
    "s_waitcnt vmcnt(6) lgkmcnt(0)\n"
    "s_barrier\n"
    "ds_read_b64_tr_b16 v[192:193], v244\n"
    "ds_read_b64_tr_b16 v[194:195], v244 offset:2048\n"
    "s_waitcnt lgkmcnt(0)\n"
    "v_mfma_f32_16x16x32_bf16 v[160:163], v[192:195], v[96:99], 0\n"
    "ds_read_b64_tr_b16 v[196:197], v244 offset:4096\n"
    "ds_read_b64_tr_b16 v[198:199], v244 offset:6144\n"
    "v_mfma_f32_16x16x32_bf16 v[164:167], v[192:195], v[100:103], 0\n"
    "s_add_u32 m0, s34, 0x1e000\n"
    "s_waitcnt lgkmcnt(0)\n"
    "v_mfma_f32_16x16x32_bf16 v[160:163], v[196:199], v[104:107], v[160:163]\n"
    "s_add_u32 s42, s65, 0x0\n"
    "s_add_u32 s43, s65, 0x2400\n"
    "v_mfma_f32_16x16x32_bf16 v[164:167], v[196:199], v[108:111], v[164:167]\n"
    "s_add_u32 s44, s65, 0x4800\n"
    "ds_read_b64_tr_b16 v[200:201], v244 offset:8192\n"
    "buffer_load_dwordx4 v254, s[24:27], s42 offen lds\n"
    "buffer_load_dwordx4 v254, s[24:27], s43 offen offset:1024 lds\n"
    "buffer_load_dwordx4 v254, s[24:27], s44 offen offset:2048 lds\n"
    "ds_read_b64_tr_b16 v[202:203], v244 offset:10240\n"
    "ds_read_b64_tr_b16 v[204:205], v245\n"
    "ds_read_b64_tr_b16 v[206:207], v245 offset:2048\n"
    "ds_read_b64_tr_b16 v[208:209], v245 offset:4096\n"
    "ds_read_b64_tr_b16 v[210:211], v245 offset:6144\n"
    "ds_read_b64_tr_b16 v[212:213], v245 offset:8192\n"
    "ds_read_b64_tr_b16 v[214:215], v245 offset:10240\n"
    "s_waitcnt vmcnt(6) lgkmcnt(0)\n"
    "s_barrier\n"
    "ds_read_b64_tr_b16 v[216:217], v246\n"
    "ds_read_b64_tr_b16 v[218:219], v246 offset:2048\n"
    "v_mfma_f32_16x16x32_bf16 v[160:163], v[200:203], v[112:115], v[160:163]\n"
    "ds_read_b64_tr_b16 v[220:221], v246 offset:4096\n"
    "ds_read_b64_tr_b16 v[222:223], v246 offset:6144\n"
    "v_mfma_f32_16x16x32_bf16 v[164:167], v[200:203], v[116:119], v[164:167]\n"
    "ds_read_b64_tr_b16 v[192:193], v246 offset:8192\n"
    "ds_read_b64_tr_b16 v[194:195], v246 offset:10240\n"
    "v_mfma_f32_16x16x32_bf16 v[168:171], v[204:207], v[96:99], 0\n"
    "ds_read_b64_tr_b16 v[196:197], v247\n"
    "ds_read_b64_tr_b16 v[198:199], v247 offset:2048\n"
    "v_mfma_f32_16x16x32_bf16 v[172:175], v[204:207], v[100:103], 0\n"
    "ds_read_b64_tr_b16 v[200:201], v247 offset:4096\n"
    "ds_read_b64_tr_b16 v[202:203], v247 offset:6144\n"
    "v_mfma_f32_16x16x32_bf16 v[168:171], v[208:211], v[104:107], v[168:171]\n"
    "ds_read_b64_tr_b16 v[204:205], v247 offset:8192\n"
    "ds_read_b64_tr_b16 v[206:207], v247 offset:10240\n"
    "v_mfma_f32_16x16x32_bf16 v[172:175], v[208:211], v[108:111], v[172:175]\n"
    "ds_read_b64_tr_b16 v[208:209], v244 offset:12288\n"
    "ds_read_b64_tr_b16 v[210:211], v244 offset:14336\n"
    "v_mfma_f32_16x16x32_bf16 v[168:171], v[212:215], v[112:115], v[168:171]\n"
    "v_mfma_f32_16x16x32_bf16 v[172:175], v[212:215], v[116:119], v[172:175]\n"
    "ds_read_b64_tr_b16 v[212:213], v244 offset:16384\n"
    "s_waitcnt lgkmcnt(5)\n"
    "v_mfma_f32_16x16x32_bf16 v[176:179], v[216:219], v[96:99], 0\n"
    "ds_read_b64_tr_b16 v[214:215], v244 offset:18432\n"
    "v_mfma_f32_16x16x32_bf16 v[180:183], v[216:219], v[100:103], 0\n"
    "ds_read_b64_tr_b16 v[216:217], v244 offset:20480\n"
    "ds_read_b64_tr_b16 v[218:219], v244 offset:22528\n"
    "v_mfma_f32_16x16x32_bf16 v[176:179], v[220:223], v[104:107], v[176:179]\n"
    "v_mfma_f32_16x16x32_bf16 v[180:183], v[220:223], v[108:111], v[180:183]\n"
    "ds_read_b64_tr_b16 v[220:221], v245 offset:12288\n"
    "ds_read_b64_tr_b16 v[222:223], v245 offset:14336\n"
    "v_mfma_f32_16x16x32_bf16 v[176:179], v[192:195], v[112:115], v[176:179]\n"
    "v_mfma_f32_16x16x32_bf16 v[180:183], v[192:195], v[116:119], v[180:183]\n"
    "v_mfma_f32_16x16x32_bf16 v[184:187], v[196:199], v[96:99], 0\n"
    "v_mfma_f32_16x16x32_bf16 v[188:191], v[196:199], v[100:103], 0\n"
    "v_mfma_f32_16x16x32_bf16 v[184:187], v[200:203], v[104:107], v[184:187]\n"
    "v_mfma_f32_16x16x32_bf16 v[188:191], v[200:203], v[108:111], v[188:191]\n"
    "s_waitcnt lgkmcnt(0)\n"
    "v_mfma_f32_16x16x32_bf16 v[184:187], v[204:207], v[112:115], v[184:187]\n"
    "v_mfma_f32_16x16x32_bf16 v[188:191], v[204:207], v[116:119], v[188:191]\n"
    "s_waitcnt vmcnt(3) lgkmcnt(0)\n"
    "s_barrier\n"
    "ds_read_b64_tr_b16 v[192:193], v245 offset:16384\n"
    "ds_read_b64_tr_b16 v[194:195], v245 offset:18432\n"
    "v_mfma_f32_16x16x32_bf16 v[160:163], v[208:211], v[120:123], v[160:163]\n"
    "ds_read_b64_tr_b16 v[196:197], v245 offset:20480\n"
    "ds_read_b64_tr_b16 v[198:199], v245 offset:22528\n"
    "v_mfma_f32_16x16x32_bf16 v[164:167], v[208:211], v[124:127], v[164:167]\n"
    "s_add_u32 m0, s34, 0x12000\n"
    "s_add_u32 s42, s65, 0x1e000\n"
    "v_mfma_f32_16x16x32_bf16 v[160:163], v[212:215], v[128:131], v[160:163]\n"
    "s_add_u32 s43, s65, 0x20400\n"
    "s_add_u32 s44, s65, 0x22800\n"
    "v_mfma_f32_16x16x32_bf16 v[164:167], v[212:215], v[132:135], v[164:167]\n"
    "buffer_load_dwordx4 v254, s[24:27], s42 offen lds\n"
    "v_mfma_f32_16x16x32_bf16 v[160:163], v[216:219], v[136:139], v[160:163]\n"
    "buffer_load_dwordx4 v254, s[24:27], s43 offen offset:1024 lds\n"
    "v_mfma_f32_16x16x32_bf16 v[164:167], v[216:219], v[140:143], v[164:167]\n"
    "buffer_load_dwordx4 v254, s[24:27], s44 offen offset:2048 lds\n"
    "v_mfma_f32_16x16x32_bf16 v[168:171], v[220:223], v[120:123], v[168:171]\n"
    "ds_read_b64_tr_b16 v[200:201], v246 offset:12288\n"
    "ds_read_b64_tr_b16 v[202:203], v246 offset:14336\n"
    "v_mfma_f32_16x16x32_bf16 v[172:175], v[220:223], v[124:127], v[172:175]\n"
    "ds_read_b64_tr_b16 v[204:205], v246 offset:16384\n"
    "s_waitcnt lgkmcnt(3)\n"
    "v_mfma_f32_16x16x32_bf16 v[168:171], v[192:195], v[128:131], v[168:171]\n"
    "ds_read_b64_tr_b16 v[206:207], v246 offset:18432\n"
    "ds_read_b64_tr_b16 v[208:209], v246 offset:20480\n"
    "v_mfma_f32_16x16x32_bf16 v[172:175], v[192:195], v[132:135], v[172:175]\n"
    "ds_read_b64_tr_b16 v[210:211], v246 offset:22528\n"
    "ds_read_b64_tr_b16 v[212:213], v247 offset:12288\n"
    "v_mfma_f32_16x16x32_bf16 v[168:171], v[196:199], v[136:139], v[168:171]\n"
    "ds_read_b64_tr_b16 v[214:215], v247 offset:14336\n"
    "v_cvt_pk_bf16_f32 v224, v160, v161\n"
    "v_mfma_f32_16x16x32_bf16 v[172:175], v[196:199], v[140:143], v[172:175]\n"
    "v_cvt_pk_bf16_f32 v225, v162, v163\n"
    "v_cvt_pk_bf16_f32 v228, v164, v165\n"
    "s_waitcnt lgkmcnt(0)\n"
    "s_barrier\n"
    "ds_read_b64_tr_b16 v[216:217], v247 offset:16384\n"
    "ds_read_b64_tr_b16 v[218:219], v247 offset:18432\n"
    "v_mfma_f32_16x16x32_bf16 v[176:179], v[200:203], v[120:123], v[176:179]\n"
    "ds_read_b64_tr_b16 v[220:221], v247 offset:20480\n"
    "ds_read_b64_tr_b16 v[222:223], v247 offset:22528\n"
    "v_mfma_f32_16x16x32_bf16 v[180:183], v[200:203], v[124:127], v[180:183]\n"
    "ds_read_b64_tr_b16 v[192:193], v244 offset:24576\n"
    "ds_read_b64_tr_b16 v[194:195], v244 offset:26624\n"
    "v_mfma_f32_16x16x32_bf16 v[176:179], v[204:207], v[128:131], v[176:179]\n"
    "ds_read_b64_tr_b16 v[196:197], v244 offset:28672\n"
    "ds_read_b64_tr_b16 v[198:199], v244 offset:30720\n"
    "v_mfma_f32_16x16x32_bf16 v[180:183], v[204:207], v[132:135], v[180:183]\n"
    "ds_read_b64_tr_b16 v[200:201], v244 offset:32768\n"
    "ds_read_b64_tr_b16 v[202:203], v244 offset:34816\n"
    "v_mfma_f32_16x16x32_bf16 v[176:179], v[208:211], v[136:139], v[176:179]\n"
    "ds_read_b64_tr_b16 v[204:205], v245 offset:24576\n"
    "ds_read_b64_tr_b16 v[206:207], v245 offset:26624\n"
    "v_mfma_f32_16x16x32_bf16 v[180:183], v[208:211], v[140:143], v[180:183]\n"
    "ds_read_b64_tr_b16 v[208:209], v245 offset:28672\n"
    "ds_read_b64_tr_b16 v[210:211], v245 offset:30720\n"
    "v_mfma_f32_16x16x32_bf16 v[184:187], v[212:215], v[120:123], v[184:187]\n"
    "v_cvt_pk_bf16_f32 v229, v166, v167\n"
    "v_cvt_pk_bf16_f32 v226, v168, v169\n"
    "v_mfma_f32_16x16x32_bf16 v[188:191], v[212:215], v[124:127], v[188:191]\n"
    "ds_read_b64_tr_b16 v[212:213], v245 offset:32768\n"
    "s_waitcnt lgkmcnt(5)\n"
    "v_mfma_f32_16x16x32_bf16 v[184:187], v[216:219], v[128:131], v[184:187]\n"
    "ds_read_b64_tr_b16 v[214:215], v245 offset:34816\n"
    "v_cvt_pk_bf16_f32 v227, v170, v171\n"
    "v_mfma_f32_16x16x32_bf16 v[188:191], v[216:219], v[132:135], v[188:191]\n"
    "ds_read_b64_tr_b16 v[216:217], v246 offset:24576\n"
    "ds_read_b64_tr_b16 v[218:219], v246 offset:26624\n"
    "v_mfma_f32_16x16x32_bf16 v[184:187], v[220:223], v[136:139], v[184:187]\n"
    "v_cvt_pk_bf16_f32 v230, v172, v173\n"
    "v_cvt_pk_bf16_f32 v231, v174, v175\n"
    "v_mfma_f32_16x16x32_bf16 v[188:191], v[220:223], v[140:143], v[188:191]\n"
    "ds_read_b64_tr_b16 v[220:221], v246 offset:28672\n"
    "ds_read_b64_tr_b16 v[222:223], v246 offset:30720\n"
    "v_mfma_f32_16x16x32_bf16 v[160:163], v[192:195], v[96:99], 0\n"
    "ds_write_b128 v251, v[228:231]\n"
    "v_mfma_f32_16x16x32_bf16 v[164:167], v[192:195], v[100:103], 0\n"
    "ds_write_b128 v250, v[224:227]\n"
    "v_mfma_f32_16x16x32_bf16 v[160:163], v[196:199], v[104:107], v[160:163]\n"
    "v_cvt_pk_bf16_f32 v232, v176, v177\n"
    "v_cvt_pk_bf16_f32 v233, v178, v179\n"
    "v_mfma_f32_16x16x32_bf16 v[164:167], v[196:199], v[108:111], v[164:167]\n"
    "v_cvt_pk_bf16_f32 v236, v180, v181\n"
    "v_cvt_pk_bf16_f32 v237, v182, v183\n"
    "v_mfma_f32_16x16x32_bf16 v[160:163], v[200:203], v[112:115], v[160:163]\n"
    "v_cvt_pk_bf16_f32 v234, v184, v185\n"
    "v_cvt_pk_bf16_f32 v235, v186, v187\n"
    "v_mfma_f32_16x16x32_bf16 v[164:167], v[200:203], v[116:119], v[164:167]\n"
    "v_cvt_pk_bf16_f32 v238, v188, v189\n"
    "s_waitcnt lgkmcnt(0)\n"
    "v_mfma_f32_16x16x32_bf16 v[168:171], v[204:207], v[96:99], 0\n"
    "v_cvt_pk_bf16_f32 v239, v190, v191\n"
    "v_mfma_f32_16x16x32_bf16 v[172:175], v[204:207], v[100:103], 0\n"
    "ds_write_b128 v251, v[236:239] offset:1024\n"
    "s_waitcnt lgkmcnt(0)\n"
    "s_barrier\n"
    "ds_read_b64_tr_b16 v[192:193], v246 offset:32768\n"
    "ds_read_b64_tr_b16 v[194:195], v246 offset:34816\n"
    "v_mfma_f32_16x16x32_bf16 v[168:171], v[208:211], v[104:107], v[168:171]\n"
    "ds_read_b64_tr_b16 v[196:197], v247 offset:24576\n"
    "ds_read_b64_tr_b16 v[198:199], v247 offset:26624\n"
    "v_mfma_f32_16x16x32_bf16 v[172:175], v[208:211], v[108:111], v[172:175]\n"
    "s_add_u32 m0, s34, 0x15000\n"
    "s_add_u32 s42, s66, 0x0\n"
    "v_mfma_f32_16x16x32_bf16 v[168:171], v[212:215], v[112:115], v[168:171]\n"
    "s_add_u32 s43, s66, 0x2400\n"
    "s_add_u32 s44, s66, 0x4800\n"
    "v_mfma_f32_16x16x32_bf16 v[172:175], v[212:215], v[116:119], v[172:175]\n"
    "buffer_load_dwordx4 v254, s[24:27], s42 offen lds\n"
    "v_mfma_f32_16x16x32_bf16 v[176:179], v[216:219], v[96:99], 0\n"
    "buffer_load_dwordx4 v254, s[24:27], s43 offen offset:1024 lds\n"
    "v_mfma_f32_16x16x32_bf16 v[180:183], v[216:219], v[100:103], 0\n"
    "buffer_load_dwordx4 v254, s[24:27], s44 offen offset:2048 lds\n"
    "v_mfma_f32_16x16x32_bf16 v[176:179], v[220:223], v[104:107], v[176:179]\n"
    "ds_read_b64_tr_b16 v[200:201], v247 offset:28672\n"
    "ds_read_b64_tr_b16 v[202:203], v247 offset:30720\n"
    "v_mfma_f32_16x16x32_bf16 v[180:183], v[220:223], v[108:111], v[180:183]\n"
    "ds_read_b64_tr_b16 v[204:205], v247 offset:32768\n"
    "s_waitcnt lgkmcnt(3)\n"
    "v_mfma_f32_16x16x32_bf16 v[176:179], v[192:195], v[112:115], v[176:179]\n"
    "ds_read_b64_tr_b16 v[206:207], v247 offset:34816\n"
    "ds_read_b64_tr_b16 v[208:209], v244 offset:36864\n"
    "v_mfma_f32_16x16x32_bf16 v[180:183], v[192:195], v[116:119], v[180:183]\n"
    "ds_read_b64_tr_b16 v[210:211], v244 offset:38912\n"
    "ds_read_b64_tr_b16 v[212:213], v244 offset:40960\n"
    "v_mfma_f32_16x16x32_bf16 v[184:187], v[196:199], v[96:99], 0\n"
    "ds_read_b64_tr_b16 v[214:215], v244 offset:43008\n"
    "v_mfma_f32_16x16x32_bf16 v[188:191], v[196:199], v[100:103], 0\n"
    "ds_write_b128 v250, v[232:235] offset:1024\n"
    "s_waitcnt lgkmcnt(0)\n"
    "s_barrier\n"
    "ds_read_b64_tr_b16 v[216:217], v244 offset:45056\n"
    "ds_read_b64_tr_b16 v[218:219], v244 offset:47104\n"
    "v_mfma_f32_16x16x32_bf16 v[184:187], v[200:203], v[104:107], v[184:187]\n"
    "ds_read_b64_tr_b16 v[220:221], v245 offset:36864\n"
    "ds_read_b64_tr_b16 v[222:223], v245 offset:38912\n"
    "v_mfma_f32_16x16x32_bf16 v[188:191], v[200:203], v[108:111], v[188:191]\n"
    "s_add_u32 m0, s34, 0x18000\n"
    "s_add_u32 s42, s66, 0x1e000\n"
    "v_mfma_f32_16x16x32_bf16 v[184:187], v[204:207], v[112:115], v[184:187]\n"
    "s_add_u32 s43, s66, 0x20400\n"
    "s_add_u32 s44, s66, 0x22800\n"
    "v_mfma_f32_16x16x32_bf16 v[188:191], v[204:207], v[116:119], v[188:191]\n"
    "buffer_load_dwordx4 v254, s[24:27], s42 offen lds\n"
    "v_mfma_f32_16x16x32_bf16 v[160:163], v[208:211], v[120:123], v[160:163]\n"
    "buffer_load_dwordx4 v254, s[24:27], s43 offen offset:1024 lds\n"
    "v_mfma_f32_16x16x32_bf16 v[164:167], v[208:211], v[124:127], v[164:167]\n"
    "buffer_load_dwordx4 v254, s[24:27], s44 offen offset:2048 lds\n"
    "v_mfma_f32_16x16x32_bf16 v[160:163], v[212:215], v[128:131], v[160:163]\n"
    "ds_read_b64_tr_b16 v[192:193], v245 offset:40960\n"
    "ds_read_b64_tr_b16 v[194:195], v245 offset:43008\n"
    "v_mfma_f32_16x16x32_bf16 v[164:167], v[212:215], v[132:135], v[164:167]\n"
    "ds_read_b64_tr_b16 v[196:197], v245 offset:45056\n"
    "s_waitcnt lgkmcnt(3)\n"
    "v_mfma_f32_16x16x32_bf16 v[160:163], v[216:219], v[136:139], v[160:163]\n"
    "ds_read_b64_tr_b16 v[198:199], v245 offset:47104\n"
    "ds_read_b64_tr_b16 v[200:201], v246 offset:36864\n"
    "v_mfma_f32_16x16x32_bf16 v[164:167], v[216:219], v[140:143], v[164:167]\n"
    "ds_read_b64_tr_b16 v[202:203], v246 offset:38912\n"
    "ds_read_b64_tr_b16 v[204:205], v246 offset:40960\n"
    "v_mfma_f32_16x16x32_bf16 v[168:171], v[220:223], v[120:123], v[168:171]\n"
    "ds_read_b64_tr_b16 v[206:207], v246 offset:43008\n"
    "ds_read_b64_tr_b16 v[208:209], v246 offset:45056\n"
    "v_mfma_f32_16x16x32_bf16 v[172:175], v[220:223], v[124:127], v[172:175]\n"
    "ds_read_b64_tr_b16 v[210:211], v246 offset:47104\n"
    "s_waitcnt lgkmcnt(7)\n"
    "v_mfma_f32_16x16x32_bf16 v[168:171], v[192:195], v[128:131], v[168:171]\n"
    "ds_read_b64_tr_b16 v[212:213], v247 offset:36864\n"
    "ds_read_b64_tr_b16 v[214:215], v247 offset:38912\n"
    "v_mfma_f32_16x16x32_bf16 v[172:175], v[192:195], v[132:135], v[172:175]\n"
    "ds_read_b64_tr_b16 v[216:217], v247 offset:40960\n"
    "s_waitcnt lgkmcnt(6)\n"
    "v_mfma_f32_16x16x32_bf16 v[168:171], v[196:199], v[136:139], v[168:171]\n"
    "ds_read_b64_tr_b16 v[218:219], v247 offset:43008\n"
    "ds_read_b64_tr_b16 v[220:221], v247 offset:45056\n"
    "v_mfma_f32_16x16x32_bf16 v[172:175], v[196:199], v[140:143], v[172:175]\n"
    "ds_read_b64_tr_b16 v[222:223], v247 offset:47104\n"
    "v_cvt_pk_bf16_f32 v224, v160, v161\n"
    "v_mfma_f32_16x16x32_bf16 v[176:179], v[200:203], v[120:123], v[176:179]\n"
    "v_cvt_pk_bf16_f32 v225, v162, v163\n"
    "v_cvt_pk_bf16_f32 v228, v164, v165\n"
    "v_mfma_f32_16x16x32_bf16 v[180:183], v[200:203], v[124:127], v[180:183]\n"
    "v_cvt_pk_bf16_f32 v229, v166, v167\n"
    "s_waitcnt lgkmcnt(3)\n"
    "v_mfma_f32_16x16x32_bf16 v[176:179], v[204:207], v[128:131], v[176:179]\n"
    "v_mfma_f32_16x16x32_bf16 v[180:183], v[204:207], v[132:135], v[180:183]\n"
    "v_cvt_pk_bf16_f32 v226, v168, v169\n"
    "v_cvt_pk_bf16_f32 v227, v170, v171\n"
    "s_waitcnt vmcnt(9) lgkmcnt(0)\n"
    "s_barrier\n"
    "ds_read_b64_tr_b16 v[192:193], v244 offset:49152\n"
    "ds_read_b64_tr_b16 v[194:195], v244 offset:51200\n"
    "v_mfma_f32_16x16x32_bf16 v[176:179], v[208:211], v[136:139], v[176:179]\n"
    "ds_read_b64_tr_b16 v[196:197], v244 offset:53248\n"
    "ds_read_b64_tr_b16 v[198:199], v244 offset:55296\n"
    "v_mfma_f32_16x16x32_bf16 v[180:183], v[208:211], v[140:143], v[180:183]\n"
    "s_add_u32 m0, s34, 0x1b000\n"
    "s_add_u32 s42, s67, 0x0\n"
    "v_mfma_f32_16x16x32_bf16 v[184:187], v[212:215], v[120:123], v[184:187]\n"
    "s_add_u32 s43, s67, 0x2400\n"
    "s_add_u32 s44, s67, 0x4800\n"
    "v_mfma_f32_16x16x32_bf16 v[188:191], v[212:215], v[124:127], v[188:191]\n"
    "buffer_load_dwordx4 v254, s[24:27], s42 offen lds\n"
    "v_mfma_f32_16x16x32_bf16 v[184:187], v[216:219], v[128:131], v[184:187]\n"
    "buffer_load_dwordx4 v254, s[24:27], s43 offen offset:1024 lds\n"
    "v_mfma_f32_16x16x32_bf16 v[188:191], v[216:219], v[132:135], v[188:191]\n"
    "buffer_load_dwordx4 v254, s[24:27], s44 offen offset:2048 lds\n"
    "v_mfma_f32_16x16x32_bf16 v[184:187], v[220:223], v[136:139], v[184:187]\n"
    "ds_read_b64_tr_b16 v[200:201], v244 offset:57344\n"
    "ds_read_b64_tr_b16 v[202:203], v244 offset:59392\n"
    "v_mfma_f32_16x16x32_bf16 v[188:191], v[220:223], v[140:143], v[188:191]\n"
    "ds_read_b64_tr_b16 v[204:205], v245 offset:49152\n"
    "s_waitcnt lgkmcnt(3)\n"
    "v_mfma_f32_16x16x32_bf16 v[160:163], v[192:195], v[96:99], 0\n"
    "ds_read_b64_tr_b16 v[206:207], v245 offset:51200\n"
    "ds_read_b64_tr_b16 v[208:209], v245 offset:53248\n"
    "v_mfma_f32_16x16x32_bf16 v[164:167], v[192:195], v[100:103], 0\n"
    "ds_read_b64_tr_b16 v[210:211], v245 offset:55296\n"
    "ds_read_b64_tr_b16 v[212:213], v245 offset:57344\n"
    "v_mfma_f32_16x16x32_bf16 v[160:163], v[196:199], v[104:107], v[160:163]\n"
    "ds_read_b64_tr_b16 v[214:215], v245 offset:59392\n"
    "v_cvt_pk_bf16_f32 v230, v172, v173\n"
    "v_mfma_f32_16x16x32_bf16 v[164:167], v[196:199], v[108:111], v[164:167]\n"
    "v_cvt_pk_bf16_f32 v231, v174, v175\n"
    "s_waitcnt vmcnt(9) lgkmcnt(0)\n"
    "s_barrier\n"
    "ds_read_b64_tr_b16 v[216:217], v246 offset:49152\n"
    "ds_read_b64_tr_b16 v[218:219], v246 offset:51200\n"
    "v_mfma_f32_16x16x32_bf16 v[160:163], v[200:203], v[112:115], v[160:163]\n"
    "ds_read_b64_tr_b16 v[220:221], v246 offset:53248\n"
    "ds_read_b64_tr_b16 v[222:223], v246 offset:55296\n"
    "v_mfma_f32_16x16x32_bf16 v[164:167], v[200:203], v[116:119], v[164:167]\n"
    "ds_read_b64_tr_b16 v[192:193], v246 offset:57344\n"
    "ds_read_b64_tr_b16 v[194:195], v246 offset:59392\n"
    "v_mfma_f32_16x16x32_bf16 v[168:171], v[204:207], v[96:99], 0\n"
    "ds_read_b64_tr_b16 v[196:197], v247 offset:49152\n"
    "ds_read_b64_tr_b16 v[198:199], v247 offset:51200\n"
    "v_mfma_f32_16x16x32_bf16 v[172:175], v[204:207], v[100:103], 0\n"
    "ds_read_b64_tr_b16 v[200:201], v247 offset:53248\n"
    "ds_read_b64_tr_b16 v[202:203], v247 offset:55296\n"
    "v_mfma_f32_16x16x32_bf16 v[168:171], v[208:211], v[104:107], v[168:171]\n"
    "ds_read_b64_tr_b16 v[204:205], v247 offset:57344\n"
    "ds_read_b64_tr_b16 v[206:207], v247 offset:59392\n"
    "v_mfma_f32_16x16x32_bf16 v[172:175], v[208:211], v[108:111], v[172:175]\n"
    "ds_read_b64_tr_b16 v[208:209], v244\n"
    "ds_read_b64_tr_b16 v[210:211], v244 offset:2048\n"
    "v_mfma_f32_16x16x32_bf16 v[168:171], v[212:215], v[112:115], v[168:171]\n"
    "ds_write_b128 v251, v[228:231] offset:14336\n"
    "v_mfma_f32_16x16x32_bf16 v[172:175], v[212:215], v[116:119], v[172:175]\n"
    "ds_read_b64_tr_b16 v[212:213], v244 offset:4096\n"
    "ds_write_b128 v250, v[224:227] offset:14336\n"
    "v_cvt_pk_bf16_f32 v232, v176, v177\n"
    "v_cvt_pk_bf16_f32 v233, v178, v179\n"
    "v_cvt_pk_bf16_f32 v236, v180, v181\n"
    "v_cvt_pk_bf16_f32 v237, v182, v183\n"
    "s_waitcnt lgkmcnt(5)\n"
    "v_mfma_f32_16x16x32_bf16 v[176:179], v[216:219], v[96:99], 0\n"
    "ds_read_b64_tr_b16 v[214:215], v244 offset:6144\n"
    "v_cvt_pk_bf16_f32 v234, v184, v185\n"
    "v_mfma_f32_16x16x32_bf16 v[180:183], v[216:219], v[100:103], 0\n"
    "ds_read_b64_tr_b16 v[216:217], v244 offset:8192\n"
    "ds_read_b64_tr_b16 v[218:219], v244 offset:10240\n"
    "v_mfma_f32_16x16x32_bf16 v[176:179], v[220:223], v[104:107], v[176:179]\n"
    "v_cvt_pk_bf16_f32 v235, v186, v187\n"
    "v_cvt_pk_bf16_f32 v238, v188, v189\n"
    "v_mfma_f32_16x16x32_bf16 v[180:183], v[220:223], v[108:111], v[180:183]\n"
    "ds_read_b64_tr_b16 v[220:221], v245\n"
    "ds_read_b64_tr_b16 v[222:223], v245 offset:2048\n"
    "v_mfma_f32_16x16x32_bf16 v[176:179], v[192:195], v[112:115], v[176:179]\n"
    "v_cvt_pk_bf16_f32 v239, v190, v191\n"
    "v_mfma_f32_16x16x32_bf16 v[180:183], v[192:195], v[116:119], v[180:183]\n"
    "ds_write_b128 v251, v[236:239] offset:15360\n"
    "v_mfma_f32_16x16x32_bf16 v[184:187], v[196:199], v[96:99], 0\n"
    "ds_write_b128 v250, v[232:235] offset:15360\n"
    "v_mfma_f32_16x16x32_bf16 v[188:191], v[196:199], v[100:103], 0\n"
    "v_mfma_f32_16x16x32_bf16 v[184:187], v[200:203], v[104:107], v[184:187]\n"
    "v_mfma_f32_16x16x32_bf16 v[188:191], v[200:203], v[108:111], v[188:191]\n"
    "v_mfma_f32_16x16x32_bf16 v[184:187], v[204:207], v[112:115], v[184:187]\n"
    "v_mfma_f32_16x16x32_bf16 v[188:191], v[204:207], v[116:119], v[188:191]\n"
    "s_waitcnt lgkmcnt(0)\n"
    "s_barrier\n"
    "ds_read_b64_tr_b16 v[192:193], v245 offset:4096\n"
    "ds_read_b64_tr_b16 v[194:195], v245 offset:6144\n"
    "v_mfma_f32_16x16x32_bf16 v[160:163], v[208:211], v[120:123], v[160:163]\n"
    "ds_read_b64_tr_b16 v[196:197], v245 offset:8192\n"
    "ds_read_b64_tr_b16 v[198:199], v245 offset:10240\n"
    "v_mfma_f32_16x16x32_bf16 v[164:167], v[208:211], v[124:127], v[164:167]\n"
    "s_add_u32 m0, s34, 0x1e000\n"
    "s_add_u32 s42, s67, 0x1e000\n"
    "v_mfma_f32_16x16x32_bf16 v[160:163], v[212:215], v[128:131], v[160:163]\n"
    "s_add_u32 s43, s67, 0x20400\n"
    "s_add_u32 s44, s67, 0x22800\n"
    "v_mfma_f32_16x16x32_bf16 v[164:167], v[212:215], v[132:135], v[164:167]\n"
    "buffer_load_dwordx4 v254, s[24:27], s42 offen lds\n"
    "v_mfma_f32_16x16x32_bf16 v[160:163], v[216:219], v[136:139], v[160:163]\n"
    "buffer_load_dwordx4 v254, s[24:27], s43 offen offset:1024 lds\n"
    "v_mfma_f32_16x16x32_bf16 v[164:167], v[216:219], v[140:143], v[164:167]\n"
    "buffer_load_dwordx4 v254, s[24:27], s44 offen offset:2048 lds\n"
    "v_mfma_f32_16x16x32_bf16 v[168:171], v[220:223], v[120:123], v[168:171]\n"
    "ds_read_b64_tr_b16 v[200:201], v246\n"
    "ds_read_b64_tr_b16 v[202:203], v246 offset:2048\n"
    "v_mfma_f32_16x16x32_bf16 v[172:175], v[220:223], v[124:127], v[172:175]\n"
    "ds_read_b64_tr_b16 v[204:205], v246 offset:4096\n"
    "s_waitcnt lgkmcnt(3)\n"
    "v_mfma_f32_16x16x32_bf16 v[168:171], v[192:195], v[128:131], v[168:171]\n"
    "ds_read_b64_tr_b16 v[206:207], v246 offset:6144\n"
    "ds_read_b64_tr_b16 v[208:209], v246 offset:8192\n"
    "v_mfma_f32_16x16x32_bf16 v[172:175], v[192:195], v[132:135], v[172:175]\n"
    "ds_read_b64_tr_b16 v[210:211], v246 offset:10240\n"
    "ds_read_b64_tr_b16 v[212:213], v247\n"
    "v_mfma_f32_16x16x32_bf16 v[168:171], v[196:199], v[136:139], v[168:171]\n"
    "ds_read_b64_tr_b16 v[214:215], v247 offset:2048\n"
    "v_cvt_pk_bf16_f32 v0, v160, v161\n"
    "v_mfma_f32_16x16x32_bf16 v[172:175], v[196:199], v[140:143], v[172:175]\n"
    "v_cvt_pk_bf16_f32 v1, v162, v163\n"
    "v_cvt_pk_bf16_f32 v4, v164, v165\n"
    "s_waitcnt vmcnt(9) lgkmcnt(0)\n"
    "s_barrier\n"
    "ds_read_b64_tr_b16 v[216:217], v247 offset:4096\n"
    "ds_read_b64_tr_b16 v[218:219], v247 offset:6144\n"
    "v_mfma_f32_16x16x32_bf16 v[176:179], v[200:203], v[120:123], v[176:179]\n"
    "ds_read_b64_tr_b16 v[220:221], v247 offset:8192\n"
    "ds_read_b64_tr_b16 v[222:223], v247 offset:10240\n"
    "v_mfma_f32_16x16x32_bf16 v[180:183], v[200:203], v[124:127], v[180:183]\n"
    "ds_read_b64_tr_b16 v[192:193], v244 offset:12288\n"
    "ds_read_b64_tr_b16 v[194:195], v244 offset:14336\n"
    "v_mfma_f32_16x16x32_bf16 v[176:179], v[204:207], v[128:131], v[176:179]\n"
    "ds_read_b64_tr_b16 v[196:197], v244 offset:16384\n"
    "ds_read_b64_tr_b16 v[198:199], v244 offset:18432\n"
    "v_mfma_f32_16x16x32_bf16 v[180:183], v[204:207], v[132:135], v[180:183]\n"
    "ds_read_b64_tr_b16 v[200:201], v244 offset:20480\n"
    "ds_read_b64_tr_b16 v[202:203], v244 offset:22528\n"
    "v_mfma_f32_16x16x32_bf16 v[176:179], v[208:211], v[136:139], v[176:179]\n"
    "ds_read_b64_tr_b16 v[204:205], v245 offset:12288\n"
    "ds_read_b64_tr_b16 v[206:207], v245 offset:14336\n"
    "v_mfma_f32_16x16x32_bf16 v[180:183], v[208:211], v[140:143], v[180:183]\n"
    "ds_read_b64_tr_b16 v[208:209], v245 offset:16384\n"
    "ds_read_b64_tr_b16 v[210:211], v245 offset:18432\n"
    "v_mfma_f32_16x16x32_bf16 v[184:187], v[212:215], v[120:123], v[184:187]\n"
    "v_cvt_pk_bf16_f32 v5, v166, v167\n"
    "v_cvt_pk_bf16_f32 v2, v168, v169\n"
    "v_mfma_f32_16x16x32_bf16 v[188:191], v[212:215], v[124:127], v[188:191]\n"
    "ds_read_b64_tr_b16 v[212:213], v245 offset:20480\n"
    "s_waitcnt lgkmcnt(5)\n"
    "v_mfma_f32_16x16x32_bf16 v[184:187], v[216:219], v[128:131], v[184:187]\n"
    "ds_read_b64_tr_b16 v[214:215], v245 offset:22528\n"
    "v_cvt_pk_bf16_f32 v3, v170, v171\n"
    "v_mfma_f32_16x16x32_bf16 v[188:191], v[216:219], v[132:135], v[188:191]\n"
    "ds_read_b64_tr_b16 v[216:217], v246 offset:12288\n"
    "ds_read_b64_tr_b16 v[218:219], v246 offset:14336\n"
    "v_mfma_f32_16x16x32_bf16 v[184:187], v[220:223], v[136:139], v[184:187]\n"
    "v_cvt_pk_bf16_f32 v6, v172, v173\n"
    "v_cvt_pk_bf16_f32 v7, v174, v175\n"
    "v_mfma_f32_16x16x32_bf16 v[188:191], v[220:223], v[140:143], v[188:191]\n"
    "ds_read_b64_tr_b16 v[220:221], v246 offset:16384\n"
    "ds_read_b64_tr_b16 v[222:223], v246 offset:18432\n"
    "v_mfma_f32_16x16x32_bf16 v[160:163], v[192:195], v[96:99], 0\n"
    "v_cvt_pk_bf16_f32 v8, v176, v177\n"
    "v_cvt_pk_bf16_f32 v9, v178, v179\n"
    "v_mfma_f32_16x16x32_bf16 v[164:167], v[192:195], v[100:103], 0\n"
    "v_cvt_pk_bf16_f32 v12, v180, v181\n"
    "v_cvt_pk_bf16_f32 v13, v182, v183\n"
    "v_mfma_f32_16x16x32_bf16 v[160:163], v[196:199], v[104:107], v[160:163]\n"
    "v_mfma_f32_16x16x32_bf16 v[164:167], v[196:199], v[108:111], v[164:167]\n"
    "v_cvt_pk_bf16_f32 v10, v184, v185\n"
    "v_cvt_pk_bf16_f32 v11, v186, v187\n"
    "v_mfma_f32_16x16x32_bf16 v[160:163], v[200:203], v[112:115], v[160:163]\n"
    "v_cvt_pk_bf16_f32 v14, v188, v189\n"
    "v_cvt_pk_bf16_f32 v15, v190, v191\n"
    "v_mfma_f32_16x16x32_bf16 v[164:167], v[200:203], v[116:119], v[164:167]\n"
    "s_waitcnt lgkmcnt(0)\n"
    "v_mfma_f32_16x16x32_bf16 v[168:171], v[204:207], v[96:99], 0\n"
    "v_mfma_f32_16x16x32_bf16 v[172:175], v[204:207], v[100:103], 0\n"
    "s_waitcnt vmcnt(6) lgkmcnt(0)\n"
    "s_barrier\n"
    "ds_read_b64_tr_b16 v[192:193], v246 offset:20480\n"
    "ds_read_b64_tr_b16 v[194:195], v246 offset:22528\n"
    "v_mfma_f32_16x16x32_bf16 v[168:171], v[208:211], v[104:107], v[168:171]\n"
    "ds_read_b64_tr_b16 v[196:197], v247 offset:12288\n"
    "ds_read_b64_tr_b16 v[198:199], v247 offset:14336\n"
    "v_mfma_f32_16x16x32_bf16 v[172:175], v[208:211], v[108:111], v[172:175]\n"
    "s_add_u32 m0, s34, 0x12000\n"
    "s_add_u32 s42, s68, 0x0\n"
    "v_mfma_f32_16x16x32_bf16 v[168:171], v[212:215], v[112:115], v[168:171]\n"
    "s_add_u32 s43, s68, 0x2400\n"
    "s_add_u32 s44, s68, 0x4800\n"
    "v_mfma_f32_16x16x32_bf16 v[172:175], v[212:215], v[116:119], v[172:175]\n"
    "buffer_load_dwordx4 v254, s[24:27], s42 offen lds\n"
    "v_mfma_f32_16x16x32_bf16 v[176:179], v[216:219], v[96:99], 0\n"
    "buffer_load_dwordx4 v254, s[24:27], s43 offen offset:1024 lds\n"
    "v_mfma_f32_16x16x32_bf16 v[180:183], v[216:219], v[100:103], 0\n"
    "buffer_load_dwordx4 v254, s[24:27], s44 offen offset:2048 lds\n"
    "v_mfma_f32_16x16x32_bf16 v[176:179], v[220:223], v[104:107], v[176:179]\n"
    "ds_read_b64_tr_b16 v[200:201], v247 offset:16384\n"
    "ds_read_b64_tr_b16 v[202:203], v247 offset:18432\n"
    "v_mfma_f32_16x16x32_bf16 v[180:183], v[220:223], v[108:111], v[180:183]\n"
    "ds_read_b64_tr_b16 v[204:205], v247 offset:20480\n"
    "s_waitcnt lgkmcnt(3)\n"
    "v_mfma_f32_16x16x32_bf16 v[176:179], v[192:195], v[112:115], v[176:179]\n"
    "ds_read_b64_tr_b16 v[206:207], v247 offset:22528\n"
    "ds_read_b64_tr_b16 v[208:209], v244 offset:24576\n"
    "v_mfma_f32_16x16x32_bf16 v[180:183], v[192:195], v[116:119], v[180:183]\n"
    "ds_read_b64_tr_b16 v[210:211], v244 offset:26624\n"
    "ds_read_b64_tr_b16 v[212:213], v244 offset:28672\n"
    "v_mfma_f32_16x16x32_bf16 v[184:187], v[196:199], v[96:99], 0\n"
    "ds_read_b64_tr_b16 v[214:215], v244 offset:30720\n"
    "v_mfma_f32_16x16x32_bf16 v[188:191], v[196:199], v[100:103], 0\n"
    "s_waitcnt lgkmcnt(0)\n"
    "s_barrier\n"
    "ds_read_b64_tr_b16 v[216:217], v244 offset:32768\n"
    "ds_read_b64_tr_b16 v[218:219], v244 offset:34816\n"
    "v_mfma_f32_16x16x32_bf16 v[184:187], v[200:203], v[104:107], v[184:187]\n"
    "ds_read_b64_tr_b16 v[220:221], v245 offset:24576\n"
    "ds_read_b64_tr_b16 v[222:223], v245 offset:26624\n"
    "v_mfma_f32_16x16x32_bf16 v[188:191], v[200:203], v[108:111], v[188:191]\n"
    "s_add_u32 m0, s34, 0x15000\n"
    "s_add_u32 s42, s68, 0x1e000\n"
    "v_mfma_f32_16x16x32_bf16 v[184:187], v[204:207], v[112:115], v[184:187]\n"
    "s_add_u32 s43, s68, 0x20400\n"
    "s_add_u32 s44, s68, 0x22800\n"
    "v_mfma_f32_16x16x32_bf16 v[188:191], v[204:207], v[116:119], v[188:191]\n"
    "buffer_load_dwordx4 v254, s[24:27], s42 offen lds\n"
    "v_mfma_f32_16x16x32_bf16 v[160:163], v[208:211], v[120:123], v[160:163]\n"
    "buffer_load_dwordx4 v254, s[24:27], s43 offen offset:1024 lds\n"
    "v_mfma_f32_16x16x32_bf16 v[164:167], v[208:211], v[124:127], v[164:167]\n"
    "buffer_load_dwordx4 v254, s[24:27], s44 offen offset:2048 lds\n"
    "v_mfma_f32_16x16x32_bf16 v[160:163], v[212:215], v[128:131], v[160:163]\n"
    "ds_read_b64_tr_b16 v[192:193], v245 offset:28672\n"
    "ds_read_b64_tr_b16 v[194:195], v245 offset:30720\n"
    "v_mfma_f32_16x16x32_bf16 v[164:167], v[212:215], v[132:135], v[164:167]\n"
    "ds_read_b64_tr_b16 v[196:197], v245 offset:32768\n"
    "s_waitcnt lgkmcnt(3)\n"
    "v_mfma_f32_16x16x32_bf16 v[160:163], v[216:219], v[136:139], v[160:163]\n"
    "ds_read_b64_tr_b16 v[198:199], v245 offset:34816\n"
    "ds_read_b64_tr_b16 v[200:201], v246 offset:24576\n"
    "v_mfma_f32_16x16x32_bf16 v[164:167], v[216:219], v[140:143], v[164:167]\n"
    "ds_read_b64_tr_b16 v[202:203], v246 offset:26624\n"
    "ds_read_b64_tr_b16 v[204:205], v246 offset:28672\n"
    "v_mfma_f32_16x16x32_bf16 v[168:171], v[220:223], v[120:123], v[168:171]\n"
    "ds_read_b64_tr_b16 v[206:207], v246 offset:30720\n"
    "ds_read_b64_tr_b16 v[208:209], v246 offset:32768\n"
    "v_mfma_f32_16x16x32_bf16 v[172:175], v[220:223], v[124:127], v[172:175]\n"
    "ds_read_b64_tr_b16 v[210:211], v246 offset:34816\n"
    "s_waitcnt lgkmcnt(7)\n"
    "v_mfma_f32_16x16x32_bf16 v[168:171], v[192:195], v[128:131], v[168:171]\n"
    "ds_read_b64_tr_b16 v[212:213], v247 offset:24576\n"
    "ds_read_b64_tr_b16 v[214:215], v247 offset:26624\n"
    "v_mfma_f32_16x16x32_bf16 v[172:175], v[192:195], v[132:135], v[172:175]\n"
    "ds_read_b64_tr_b16 v[216:217], v247 offset:28672\n"
    "s_waitcnt lgkmcnt(6)\n"
    "v_mfma_f32_16x16x32_bf16 v[168:171], v[196:199], v[136:139], v[168:171]\n"
    "ds_read_b64_tr_b16 v[218:219], v247 offset:30720\n"
    "ds_read_b64_tr_b16 v[220:221], v247 offset:32768\n"
    "v_mfma_f32_16x16x32_bf16 v[172:175], v[196:199], v[140:143], v[172:175]\n"
    "ds_read_b64_tr_b16 v[222:223], v247 offset:34816\n"
    "v_cvt_pk_bf16_f32 v16, v160, v161\n"
    "v_mfma_f32_16x16x32_bf16 v[176:179], v[200:203], v[120:123], v[176:179]\n"
    "v_cvt_pk_bf16_f32 v17, v162, v163\n"
    "v_cvt_pk_bf16_f32 v20, v164, v165\n"
    "v_mfma_f32_16x16x32_bf16 v[180:183], v[200:203], v[124:127], v[180:183]\n"
    "v_cvt_pk_bf16_f32 v21, v166, v167\n"
    "s_waitcnt lgkmcnt(3)\n"
    "v_mfma_f32_16x16x32_bf16 v[176:179], v[204:207], v[128:131], v[176:179]\n"
    "v_mfma_f32_16x16x32_bf16 v[180:183], v[204:207], v[132:135], v[180:183]\n"
    "v_cvt_pk_bf16_f32 v18, v168, v169\n"
    "v_cvt_pk_bf16_f32 v19, v170, v171\n"
    "s_waitcnt vmcnt(9) lgkmcnt(0)\n"
    "s_barrier\n"
    "ds_read_b64_tr_b16 v[192:193], v244 offset:36864\n"
    "ds_read_b64_tr_b16 v[194:195], v244 offset:38912\n"
    "v_mfma_f32_16x16x32_bf16 v[176:179], v[208:211], v[136:139], v[176:179]\n"
    "ds_read_b64_tr_b16 v[196:197], v244 offset:40960\n"
    "ds_read_b64_tr_b16 v[198:199], v244 offset:43008\n"
    "v_mfma_f32_16x16x32_bf16 v[180:183], v[208:211], v[140:143], v[180:183]\n"
    "s_add_u32 m0, s34, 0x18000\n"
    "s_add_u32 s42, s69, 0x0\n"
    "v_mfma_f32_16x16x32_bf16 v[184:187], v[212:215], v[120:123], v[184:187]\n"
    "s_add_u32 s43, s69, 0x2400\n"
    "s_add_u32 s44, s69, 0x4800\n"
    "v_mfma_f32_16x16x32_bf16 v[188:191], v[212:215], v[124:127], v[188:191]\n"
    "buffer_load_dwordx4 v254, s[24:27], s42 offen lds\n"
    "v_mfma_f32_16x16x32_bf16 v[184:187], v[216:219], v[128:131], v[184:187]\n"
    "buffer_load_dwordx4 v254, s[24:27], s43 offen offset:1024 lds\n"
    "v_mfma_f32_16x16x32_bf16 v[188:191], v[216:219], v[132:135], v[188:191]\n"
    "buffer_load_dwordx4 v254, s[24:27], s44 offen offset:2048 lds\n"
    "v_mfma_f32_16x16x32_bf16 v[184:187], v[220:223], v[136:139], v[184:187]\n"
    "ds_read_b64_tr_b16 v[200:201], v244 offset:45056\n"
    "ds_read_b64_tr_b16 v[202:203], v244 offset:47104\n"
    "v_mfma_f32_16x16x32_bf16 v[188:191], v[220:223], v[140:143], v[188:191]\n"
    "ds_read_b64_tr_b16 v[204:205], v245 offset:36864\n"
    "s_waitcnt lgkmcnt(3)\n"
    "v_mfma_f32_16x16x32_bf16 v[160:163], v[192:195], v[96:99], 0\n"
    "ds_read_b64_tr_b16 v[206:207], v245 offset:38912\n"
    "ds_read_b64_tr_b16 v[208:209], v245 offset:40960\n"
    "v_mfma_f32_16x16x32_bf16 v[164:167], v[192:195], v[100:103], 0\n"
    "ds_read_b64_tr_b16 v[210:211], v245 offset:43008\n"
    "ds_read_b64_tr_b16 v[212:213], v245 offset:45056\n"
    "v_mfma_f32_16x16x32_bf16 v[160:163], v[196:199], v[104:107], v[160:163]\n"
    "ds_read_b64_tr_b16 v[214:215], v245 offset:47104\n"
    "v_cvt_pk_bf16_f32 v22, v172, v173\n"
    "v_mfma_f32_16x16x32_bf16 v[164:167], v[196:199], v[108:111], v[164:167]\n"
    "v_cvt_pk_bf16_f32 v23, v174, v175\n"
    "v_cvt_pk_bf16_f32 v24, v176, v177\n"
    "s_waitcnt vmcnt(9) lgkmcnt(0)\n"
    "s_barrier\n"
    "ds_read_b64_tr_b16 v[216:217], v246 offset:36864\n"
    "ds_read_b64_tr_b16 v[218:219], v246 offset:38912\n"
    "v_mfma_f32_16x16x32_bf16 v[160:163], v[200:203], v[112:115], v[160:163]\n"
    "ds_read_b64_tr_b16 v[220:221], v246 offset:40960\n"
    "ds_read_b64_tr_b16 v[222:223], v246 offset:43008\n"
    "v_mfma_f32_16x16x32_bf16 v[164:167], v[200:203], v[116:119], v[164:167]\n"
    "ds_read_b64_tr_b16 v[192:193], v246 offset:45056\n"
    "ds_read_b64_tr_b16 v[194:195], v246 offset:47104\n"
    "v_mfma_f32_16x16x32_bf16 v[168:171], v[204:207], v[96:99], 0\n"
    "ds_read_b64_tr_b16 v[196:197], v247 offset:36864\n"
    "ds_read_b64_tr_b16 v[198:199], v247 offset:38912\n"
    "v_mfma_f32_16x16x32_bf16 v[172:175], v[204:207], v[100:103], 0\n"
    "ds_read_b64_tr_b16 v[200:201], v247 offset:40960\n"
    "ds_read_b64_tr_b16 v[202:203], v247 offset:43008\n"
    "v_mfma_f32_16x16x32_bf16 v[168:171], v[208:211], v[104:107], v[168:171]\n"
    "ds_read_b64_tr_b16 v[204:205], v247 offset:45056\n"
    "ds_read_b64_tr_b16 v[206:207], v247 offset:47104\n"
    "v_mfma_f32_16x16x32_bf16 v[172:175], v[208:211], v[108:111], v[172:175]\n"
    "ds_read_b64_tr_b16 v[208:209], v244 offset:49152\n"
    "ds_read_b64_tr_b16 v[210:211], v244 offset:51200\n"
    "v_mfma_f32_16x16x32_bf16 v[168:171], v[212:215], v[112:115], v[168:171]\n"
    "v_cvt_pk_bf16_f32 v25, v178, v179\n"
    "v_cvt_pk_bf16_f32 v28, v180, v181\n"
    "v_mfma_f32_16x16x32_bf16 v[172:175], v[212:215], v[116:119], v[172:175]\n"
    "ds_read_b64_tr_b16 v[212:213], v244 offset:53248\n"
    "v_cvt_pk_bf16_f32 v29, v182, v183\n"
    "s_waitcnt lgkmcnt(5)\n"
    "v_mfma_f32_16x16x32_bf16 v[176:179], v[216:219], v[96:99], 0\n"
    "ds_read_b64_tr_b16 v[214:215], v244 offset:55296\n"
    "v_cvt_pk_bf16_f32 v26, v184, v185\n"
    "v_mfma_f32_16x16x32_bf16 v[180:183], v[216:219], v[100:103], 0\n"
    "ds_read_b64_tr_b16 v[216:217], v244 offset:57344\n"
    "ds_read_b64_tr_b16 v[218:219], v244 offset:59392\n"
    "v_mfma_f32_16x16x32_bf16 v[176:179], v[220:223], v[104:107], v[176:179]\n"
    "v_cvt_pk_bf16_f32 v27, v186, v187\n"
    "v_cvt_pk_bf16_f32 v30, v188, v189\n"
    "v_mfma_f32_16x16x32_bf16 v[180:183], v[220:223], v[108:111], v[180:183]\n"
    "ds_read_b64_tr_b16 v[220:221], v245 offset:49152\n"
    "ds_read_b64_tr_b16 v[222:223], v245 offset:51200\n"
    "v_mfma_f32_16x16x32_bf16 v[176:179], v[192:195], v[112:115], v[176:179]\n"
    "v_cvt_pk_bf16_f32 v31, v190, v191\n"
    "v_mfma_f32_16x16x32_bf16 v[180:183], v[192:195], v[116:119], v[180:183]\n"
    "v_mfma_f32_16x16x32_bf16 v[184:187], v[196:199], v[96:99], 0\n"
    "v_mfma_f32_16x16x32_bf16 v[188:191], v[196:199], v[100:103], 0\n"
    "v_mfma_f32_16x16x32_bf16 v[184:187], v[200:203], v[104:107], v[184:187]\n"
    "v_mfma_f32_16x16x32_bf16 v[188:191], v[200:203], v[108:111], v[188:191]\n"
    "s_waitcnt lgkmcnt(0)\n"
    "v_mfma_f32_16x16x32_bf16 v[184:187], v[204:207], v[112:115], v[184:187]\n"
    "v_mfma_f32_16x16x32_bf16 v[188:191], v[204:207], v[116:119], v[188:191]\n"
    "s_waitcnt lgkmcnt(0)\n"
    "s_barrier\n"
    "ds_read_b64_tr_b16 v[192:193], v245 offset:53248\n"
    "ds_read_b64_tr_b16 v[194:195], v245 offset:55296\n"
    "v_mfma_f32_16x16x32_bf16 v[160:163], v[208:211], v[120:123], v[160:163]\n"
    "ds_read_b64_tr_b16 v[196:197], v245 offset:57344\n"
    "ds_read_b64_tr_b16 v[198:199], v245 offset:59392\n"
    "v_mfma_f32_16x16x32_bf16 v[164:167], v[208:211], v[124:127], v[164:167]\n"
    "s_add_u32 m0, s34, 0x1b000\n"
    "s_add_u32 s42, s69, 0x1e000\n"
    "v_mfma_f32_16x16x32_bf16 v[160:163], v[212:215], v[128:131], v[160:163]\n"
    "s_add_u32 s43, s69, 0x20400\n"
    "s_add_u32 s44, s69, 0x22800\n"
    "v_mfma_f32_16x16x32_bf16 v[164:167], v[212:215], v[132:135], v[164:167]\n"
    "buffer_load_dwordx4 v254, s[24:27], s42 offen lds\n"
    "v_mfma_f32_16x16x32_bf16 v[160:163], v[216:219], v[136:139], v[160:163]\n"
    "buffer_load_dwordx4 v254, s[24:27], s43 offen offset:1024 lds\n"
    "v_mfma_f32_16x16x32_bf16 v[164:167], v[216:219], v[140:143], v[164:167]\n"
    "buffer_load_dwordx4 v254, s[24:27], s44 offen offset:2048 lds\n"
    "v_mfma_f32_16x16x32_bf16 v[168:171], v[220:223], v[120:123], v[168:171]\n"
    "ds_read_b64_tr_b16 v[200:201], v246 offset:49152\n"
    "ds_read_b64_tr_b16 v[202:203], v246 offset:51200\n"
    "v_mfma_f32_16x16x32_bf16 v[172:175], v[220:223], v[124:127], v[172:175]\n"
    "ds_read_b64_tr_b16 v[204:205], v246 offset:53248\n"
    "s_waitcnt lgkmcnt(3)\n"
    "v_mfma_f32_16x16x32_bf16 v[168:171], v[192:195], v[128:131], v[168:171]\n"
    "ds_read_b64_tr_b16 v[206:207], v246 offset:55296\n"
    "ds_read_b64_tr_b16 v[208:209], v246 offset:57344\n"
    "v_mfma_f32_16x16x32_bf16 v[172:175], v[192:195], v[132:135], v[172:175]\n"
    "ds_read_b64_tr_b16 v[210:211], v246 offset:59392\n"
    "ds_read_b64_tr_b16 v[212:213], v247 offset:49152\n"
    "v_mfma_f32_16x16x32_bf16 v[168:171], v[196:199], v[136:139], v[168:171]\n"
    "ds_read_b64_tr_b16 v[214:215], v247 offset:51200\n"
    "v_cvt_pk_bf16_f32 v32, v160, v161\n"
    "v_mfma_f32_16x16x32_bf16 v[172:175], v[196:199], v[140:143], v[172:175]\n"
    "v_cvt_pk_bf16_f32 v33, v162, v163\n"
    "v_cvt_pk_bf16_f32 v36, v164, v165\n"
    "s_waitcnt vmcnt(9) lgkmcnt(0)\n"
    "s_barrier\n"
    "ds_read_b64_tr_b16 v[216:217], v247 offset:53248\n"
    "ds_read_b64_tr_b16 v[218:219], v247 offset:55296\n"
    "v_mfma_f32_16x16x32_bf16 v[176:179], v[200:203], v[120:123], v[176:179]\n"
    "ds_read_b64_tr_b16 v[220:221], v247 offset:57344\n"
    "ds_read_b64_tr_b16 v[222:223], v247 offset:59392\n"
    "v_mfma_f32_16x16x32_bf16 v[180:183], v[200:203], v[124:127], v[180:183]\n"
    "ds_read_b64_tr_b16 v[192:193], v244\n"
    "ds_read_b64_tr_b16 v[194:195], v244 offset:2048\n"
    "v_mfma_f32_16x16x32_bf16 v[176:179], v[204:207], v[128:131], v[176:179]\n"
    "ds_read_b64_tr_b16 v[196:197], v244 offset:4096\n"
    "ds_read_b64_tr_b16 v[198:199], v244 offset:6144\n"
    "v_mfma_f32_16x16x32_bf16 v[180:183], v[204:207], v[132:135], v[180:183]\n"
    "ds_read_b64_tr_b16 v[200:201], v244 offset:8192\n"
    "ds_read_b64_tr_b16 v[202:203], v244 offset:10240\n"
    "v_mfma_f32_16x16x32_bf16 v[176:179], v[208:211], v[136:139], v[176:179]\n"
    "ds_read_b64_tr_b16 v[204:205], v245\n"
    "ds_read_b64_tr_b16 v[206:207], v245 offset:2048\n"
    "v_mfma_f32_16x16x32_bf16 v[180:183], v[208:211], v[140:143], v[180:183]\n"
    "ds_read_b64_tr_b16 v[208:209], v245 offset:4096\n"
    "ds_read_b64_tr_b16 v[210:211], v245 offset:6144\n"
    "v_mfma_f32_16x16x32_bf16 v[184:187], v[212:215], v[120:123], v[184:187]\n"
    "v_cvt_pk_bf16_f32 v37, v166, v167\n"
    "v_cvt_pk_bf16_f32 v34, v168, v169\n"
    "v_mfma_f32_16x16x32_bf16 v[188:191], v[212:215], v[124:127], v[188:191]\n"
    "ds_read_b64_tr_b16 v[212:213], v245 offset:8192\n"
    "s_waitcnt lgkmcnt(5)\n"
    "v_mfma_f32_16x16x32_bf16 v[184:187], v[216:219], v[128:131], v[184:187]\n"
    "ds_read_b64_tr_b16 v[214:215], v245 offset:10240\n"
    "v_cvt_pk_bf16_f32 v35, v170, v171\n"
    "v_mfma_f32_16x16x32_bf16 v[188:191], v[216:219], v[132:135], v[188:191]\n"
    "ds_read_b64_tr_b16 v[216:217], v246\n"
    "ds_read_b64_tr_b16 v[218:219], v246 offset:2048\n"
    "v_mfma_f32_16x16x32_bf16 v[184:187], v[220:223], v[136:139], v[184:187]\n"
    "v_cvt_pk_bf16_f32 v38, v172, v173\n"
    "v_cvt_pk_bf16_f32 v39, v174, v175\n"
    "v_mfma_f32_16x16x32_bf16 v[188:191], v[220:223], v[140:143], v[188:191]\n"
    "ds_read_b64_tr_b16 v[220:221], v246 offset:4096\n"
    "ds_read_b64_tr_b16 v[222:223], v246 offset:6144\n"
    "v_mfma_f32_16x16x32_bf16 v[160:163], v[192:195], v[96:99], 0\n"
    "v_cvt_pk_bf16_f32 v40, v176, v177\n"
    "v_cvt_pk_bf16_f32 v41, v178, v179\n"
    "v_mfma_f32_16x16x32_bf16 v[164:167], v[192:195], v[100:103], 0\n"
    "v_cvt_pk_bf16_f32 v44, v180, v181\n"
    "v_cvt_pk_bf16_f32 v45, v182, v183\n"
    "v_mfma_f32_16x16x32_bf16 v[160:163], v[196:199], v[104:107], v[160:163]\n"
    "v_mfma_f32_16x16x32_bf16 v[164:167], v[196:199], v[108:111], v[164:167]\n"
    "v_cvt_pk_bf16_f32 v42, v184, v185\n"
    "v_cvt_pk_bf16_f32 v43, v186, v187\n"
    "v_mfma_f32_16x16x32_bf16 v[160:163], v[200:203], v[112:115], v[160:163]\n"
    "v_cvt_pk_bf16_f32 v46, v188, v189\n"
    "v_cvt_pk_bf16_f32 v47, v190, v191\n"
    "v_mfma_f32_16x16x32_bf16 v[164:167], v[200:203], v[116:119], v[164:167]\n"
    "s_waitcnt lgkmcnt(0)\n"
    "v_mfma_f32_16x16x32_bf16 v[168:171], v[204:207], v[96:99], 0\n"
    "v_mfma_f32_16x16x32_bf16 v[172:175], v[204:207], v[100:103], 0\n"
    "s_waitcnt vmcnt(6) lgkmcnt(0)\n"
    "s_barrier\n"
    "ds_read_b64_tr_b16 v[192:193], v246 offset:8192\n"
    "ds_read_b64_tr_b16 v[194:195], v246 offset:10240\n"
    "v_mfma_f32_16x16x32_bf16 v[168:171], v[208:211], v[104:107], v[168:171]\n"
    "ds_read_b64_tr_b16 v[196:197], v247\n"
    "ds_read_b64_tr_b16 v[198:199], v247 offset:2048\n"
    "v_mfma_f32_16x16x32_bf16 v[172:175], v[208:211], v[108:111], v[172:175]\n"
    "s_add_u32 m0, s34, 0x1e000\n"
    "s_add_u32 s42, s70, 0x0\n"
    "v_mfma_f32_16x16x32_bf16 v[168:171], v[212:215], v[112:115], v[168:171]\n"
    "s_add_u32 s43, s70, 0x2400\n"
    "s_add_u32 s44, s70, 0x4800\n"
    "v_mfma_f32_16x16x32_bf16 v[172:175], v[212:215], v[116:119], v[172:175]\n"
    "buffer_load_dwordx4 v254, s[24:27], s42 offen lds\n"
    "v_mfma_f32_16x16x32_bf16 v[176:179], v[216:219], v[96:99], 0\n"
    "buffer_load_dwordx4 v254, s[24:27], s43 offen offset:1024 lds\n"
    "v_mfma_f32_16x16x32_bf16 v[180:183], v[216:219], v[100:103], 0\n"
    "buffer_load_dwordx4 v254, s[24:27], s44 offen offset:2048 lds\n"
    "v_mfma_f32_16x16x32_bf16 v[176:179], v[220:223], v[104:107], v[176:179]\n"
    "ds_read_b64_tr_b16 v[200:201], v247 offset:4096\n"
    "ds_read_b64_tr_b16 v[202:203], v247 offset:6144\n"
    "v_mfma_f32_16x16x32_bf16 v[180:183], v[220:223], v[108:111], v[180:183]\n"
    "ds_read_b64_tr_b16 v[204:205], v247 offset:8192\n"
    "s_waitcnt lgkmcnt(3)\n"
    "v_mfma_f32_16x16x32_bf16 v[176:179], v[192:195], v[112:115], v[176:179]\n"
    "ds_read_b64_tr_b16 v[206:207], v247 offset:10240\n"
    "ds_read_b64_tr_b16 v[208:209], v244 offset:12288\n"
    "v_mfma_f32_16x16x32_bf16 v[180:183], v[192:195], v[116:119], v[180:183]\n"
    "ds_read_b64_tr_b16 v[210:211], v244 offset:14336\n"
    "ds_read_b64_tr_b16 v[212:213], v244 offset:16384\n"
    "v_mfma_f32_16x16x32_bf16 v[184:187], v[196:199], v[96:99], 0\n"
    "ds_read_b64_tr_b16 v[214:215], v244 offset:18432\n"
    "v_mfma_f32_16x16x32_bf16 v[188:191], v[196:199], v[100:103], 0\n"
    "s_waitcnt lgkmcnt(0)\n"
    "s_barrier\n"
    "ds_read_b64_tr_b16 v[216:217], v244 offset:20480\n"
    "ds_read_b64_tr_b16 v[218:219], v244 offset:22528\n"
    "v_mfma_f32_16x16x32_bf16 v[184:187], v[200:203], v[104:107], v[184:187]\n"
    "ds_read_b64_tr_b16 v[220:221], v245 offset:12288\n"
    "ds_read_b64_tr_b16 v[222:223], v245 offset:14336\n"
    "v_mfma_f32_16x16x32_bf16 v[188:191], v[200:203], v[108:111], v[188:191]\n"
    "s_add_u32 m0, s34, 0x12000\n"
    "s_add_u32 s42, s70, 0x1e000\n"
    "v_mfma_f32_16x16x32_bf16 v[184:187], v[204:207], v[112:115], v[184:187]\n"
    "s_add_u32 s43, s70, 0x20400\n"
    "s_add_u32 s44, s70, 0x22800\n"
    "v_mfma_f32_16x16x32_bf16 v[188:191], v[204:207], v[116:119], v[188:191]\n"
    "buffer_load_dwordx4 v254, s[24:27], s42 offen lds\n"
    "v_mfma_f32_16x16x32_bf16 v[160:163], v[208:211], v[120:123], v[160:163]\n"
    "buffer_load_dwordx4 v254, s[24:27], s43 offen offset:1024 lds\n"
    "v_mfma_f32_16x16x32_bf16 v[164:167], v[208:211], v[124:127], v[164:167]\n"
    "buffer_load_dwordx4 v254, s[24:27], s44 offen offset:2048 lds\n"
    "v_mfma_f32_16x16x32_bf16 v[160:163], v[212:215], v[128:131], v[160:163]\n"
    "ds_read_b64_tr_b16 v[192:193], v245 offset:16384\n"
    "ds_read_b64_tr_b16 v[194:195], v245 offset:18432\n"
    "v_mfma_f32_16x16x32_bf16 v[164:167], v[212:215], v[132:135], v[164:167]\n"
    "ds_read_b64_tr_b16 v[196:197], v245 offset:20480\n"
    "s_waitcnt lgkmcnt(3)\n"
    "v_mfma_f32_16x16x32_bf16 v[160:163], v[216:219], v[136:139], v[160:163]\n"
    "ds_read_b64_tr_b16 v[198:199], v245 offset:22528\n"
    "ds_read_b64_tr_b16 v[200:201], v246 offset:12288\n"
    "v_mfma_f32_16x16x32_bf16 v[164:167], v[216:219], v[140:143], v[164:167]\n"
    "ds_read_b64_tr_b16 v[202:203], v246 offset:14336\n"
    "ds_read_b64_tr_b16 v[204:205], v246 offset:16384\n"
    "v_mfma_f32_16x16x32_bf16 v[168:171], v[220:223], v[120:123], v[168:171]\n"
    "ds_read_b64_tr_b16 v[206:207], v246 offset:18432\n"
    "ds_read_b64_tr_b16 v[208:209], v246 offset:20480\n"
    "v_mfma_f32_16x16x32_bf16 v[172:175], v[220:223], v[124:127], v[172:175]\n"
    "ds_read_b64_tr_b16 v[210:211], v246 offset:22528\n"
    "s_waitcnt lgkmcnt(7)\n"
    "v_mfma_f32_16x16x32_bf16 v[168:171], v[192:195], v[128:131], v[168:171]\n"
    "ds_read_b64_tr_b16 v[212:213], v247 offset:12288\n"
    "ds_read_b64_tr_b16 v[214:215], v247 offset:14336\n"
    "v_mfma_f32_16x16x32_bf16 v[172:175], v[192:195], v[132:135], v[172:175]\n"
    "ds_read_b64_tr_b16 v[216:217], v247 offset:16384\n"
    "s_waitcnt lgkmcnt(6)\n"
    "v_mfma_f32_16x16x32_bf16 v[168:171], v[196:199], v[136:139], v[168:171]\n"
    "ds_read_b64_tr_b16 v[218:219], v247 offset:18432\n"
    "ds_read_b64_tr_b16 v[220:221], v247 offset:20480\n"
    "v_mfma_f32_16x16x32_bf16 v[172:175], v[196:199], v[140:143], v[172:175]\n"
    "ds_read_b64_tr_b16 v[222:223], v247 offset:22528\n"
    "v_cvt_pk_bf16_f32 v48, v160, v161\n"
    "v_mfma_f32_16x16x32_bf16 v[176:179], v[200:203], v[120:123], v[176:179]\n"
    "v_cvt_pk_bf16_f32 v49, v162, v163\n"
    "v_cvt_pk_bf16_f32 v52, v164, v165\n"
    "v_mfma_f32_16x16x32_bf16 v[180:183], v[200:203], v[124:127], v[180:183]\n"
    "v_cvt_pk_bf16_f32 v53, v166, v167\n"
    "s_waitcnt lgkmcnt(3)\n"
    "v_mfma_f32_16x16x32_bf16 v[176:179], v[204:207], v[128:131], v[176:179]\n"
    "v_mfma_f32_16x16x32_bf16 v[180:183], v[204:207], v[132:135], v[180:183]\n"
    "v_cvt_pk_bf16_f32 v50, v168, v169\n"
    "v_cvt_pk_bf16_f32 v51, v170, v171\n"
    "s_waitcnt vmcnt(9) lgkmcnt(0)\n"
    "s_barrier\n"
    "ds_read_b64_tr_b16 v[192:193], v244 offset:24576\n"
    "ds_read_b64_tr_b16 v[194:195], v244 offset:26624\n"
    "v_mfma_f32_16x16x32_bf16 v[176:179], v[208:211], v[136:139], v[176:179]\n"
    "ds_read_b64_tr_b16 v[196:197], v244 offset:28672\n"
    "ds_read_b64_tr_b16 v[198:199], v244 offset:30720\n"
    "v_mfma_f32_16x16x32_bf16 v[180:183], v[208:211], v[140:143], v[180:183]\n"
    "s_add_u32 m0, s34, 0x15000\n"
    "s_add_u32 s42, s71, 0x0\n"
    "v_mfma_f32_16x16x32_bf16 v[184:187], v[212:215], v[120:123], v[184:187]\n"
    "s_add_u32 s43, s71, 0x2400\n"
    "s_add_u32 s44, s71, 0x4800\n"
    "v_mfma_f32_16x16x32_bf16 v[188:191], v[212:215], v[124:127], v[188:191]\n"
    "buffer_load_dwordx4 v254, s[24:27], s42 offen lds\n"
    "v_mfma_f32_16x16x32_bf16 v[184:187], v[216:219], v[128:131], v[184:187]\n"
    "buffer_load_dwordx4 v254, s[24:27], s43 offen offset:1024 lds\n"
    "v_mfma_f32_16x16x32_bf16 v[188:191], v[216:219], v[132:135], v[188:191]\n"
    "buffer_load_dwordx4 v254, s[24:27], s44 offen offset:2048 lds\n"
    "v_mfma_f32_16x16x32_bf16 v[184:187], v[220:223], v[136:139], v[184:187]\n"
    "ds_read_b64_tr_b16 v[200:201], v244 offset:32768\n"
    "ds_read_b64_tr_b16 v[202:203], v244 offset:34816\n"
    "v_mfma_f32_16x16x32_bf16 v[188:191], v[220:223], v[140:143], v[188:191]\n"
    "ds_read_b64_tr_b16 v[204:205], v245 offset:24576\n"
    "s_waitcnt lgkmcnt(3)\n"
    "v_mfma_f32_16x16x32_bf16 v[160:163], v[192:195], v[96:99], 0\n"
    "ds_read_b64_tr_b16 v[206:207], v245 offset:26624\n"
    "ds_read_b64_tr_b16 v[208:209], v245 offset:28672\n"
    "v_mfma_f32_16x16x32_bf16 v[164:167], v[192:195], v[100:103], 0\n"
    "ds_read_b64_tr_b16 v[210:211], v245 offset:30720\n"
    "ds_read_b64_tr_b16 v[212:213], v245 offset:32768\n"
    "v_mfma_f32_16x16x32_bf16 v[160:163], v[196:199], v[104:107], v[160:163]\n"
    "ds_read_b64_tr_b16 v[214:215], v245 offset:34816\n"
    "v_cvt_pk_bf16_f32 v54, v172, v173\n"
    "v_mfma_f32_16x16x32_bf16 v[164:167], v[196:199], v[108:111], v[164:167]\n"
    "v_cvt_pk_bf16_f32 v55, v174, v175\n"
    "v_cvt_pk_bf16_f32 v56, v176, v177\n"
    "s_waitcnt vmcnt(9) lgkmcnt(0)\n"
    "s_barrier\n"
    "ds_read_b64_tr_b16 v[216:217], v246 offset:24576\n"
    "ds_read_b64_tr_b16 v[218:219], v246 offset:26624\n"
    "v_mfma_f32_16x16x32_bf16 v[160:163], v[200:203], v[112:115], v[160:163]\n"
    "ds_read_b64_tr_b16 v[220:221], v246 offset:28672\n"
    "ds_read_b64_tr_b16 v[222:223], v246 offset:30720\n"
    "v_mfma_f32_16x16x32_bf16 v[164:167], v[200:203], v[116:119], v[164:167]\n"
    "ds_read_b64_tr_b16 v[192:193], v246 offset:32768\n"
    "ds_read_b64_tr_b16 v[194:195], v246 offset:34816\n"
    "v_mfma_f32_16x16x32_bf16 v[168:171], v[204:207], v[96:99], 0\n"
    "ds_read_b64_tr_b16 v[196:197], v247 offset:24576\n"
    "ds_read_b64_tr_b16 v[198:199], v247 offset:26624\n"
    "v_mfma_f32_16x16x32_bf16 v[172:175], v[204:207], v[100:103], 0\n"
    "ds_read_b64_tr_b16 v[200:201], v247 offset:28672\n"
    "ds_read_b64_tr_b16 v[202:203], v247 offset:30720\n"
    "v_mfma_f32_16x16x32_bf16 v[168:171], v[208:211], v[104:107], v[168:171]\n"
    "ds_read_b64_tr_b16 v[204:205], v247 offset:32768\n"
    "ds_read_b64_tr_b16 v[206:207], v247 offset:34816\n"
    "v_mfma_f32_16x16x32_bf16 v[172:175], v[208:211], v[108:111], v[172:175]\n"
    "ds_read_b64_tr_b16 v[208:209], v244 offset:36864\n"
    "ds_read_b64_tr_b16 v[210:211], v244 offset:38912\n"
    "v_mfma_f32_16x16x32_bf16 v[168:171], v[212:215], v[112:115], v[168:171]\n"
    "v_cvt_pk_bf16_f32 v57, v178, v179\n"
    "v_cvt_pk_bf16_f32 v60, v180, v181\n"
    "v_mfma_f32_16x16x32_bf16 v[172:175], v[212:215], v[116:119], v[172:175]\n"
    "ds_read_b64_tr_b16 v[212:213], v244 offset:40960\n"
    "v_cvt_pk_bf16_f32 v61, v182, v183\n"
    "s_waitcnt lgkmcnt(5)\n"
    "v_mfma_f32_16x16x32_bf16 v[176:179], v[216:219], v[96:99], 0\n"
    "ds_read_b64_tr_b16 v[214:215], v244 offset:43008\n"
    "v_cvt_pk_bf16_f32 v58, v184, v185\n"
    "v_mfma_f32_16x16x32_bf16 v[180:183], v[216:219], v[100:103], 0\n"
    "ds_read_b64_tr_b16 v[216:217], v244 offset:45056\n"
    "ds_read_b64_tr_b16 v[218:219], v244 offset:47104\n"
    "v_mfma_f32_16x16x32_bf16 v[176:179], v[220:223], v[104:107], v[176:179]\n"
    "v_cvt_pk_bf16_f32 v59, v186, v187\n"
    "v_cvt_pk_bf16_f32 v62, v188, v189\n"
    "v_mfma_f32_16x16x32_bf16 v[180:183], v[220:223], v[108:111], v[180:183]\n"
    "ds_read_b64_tr_b16 v[220:221], v245 offset:36864\n"
    "ds_read_b64_tr_b16 v[222:223], v245 offset:38912\n"
    "v_mfma_f32_16x16x32_bf16 v[176:179], v[192:195], v[112:115], v[176:179]\n"
    "v_cvt_pk_bf16_f32 v63, v190, v191\n"
    "v_mfma_f32_16x16x32_bf16 v[180:183], v[192:195], v[116:119], v[180:183]\n"
    "v_mfma_f32_16x16x32_bf16 v[184:187], v[196:199], v[96:99], 0\n"
    "v_mfma_f32_16x16x32_bf16 v[188:191], v[196:199], v[100:103], 0\n"
    "v_mfma_f32_16x16x32_bf16 v[184:187], v[200:203], v[104:107], v[184:187]\n"
    "v_mfma_f32_16x16x32_bf16 v[188:191], v[200:203], v[108:111], v[188:191]\n"
    "s_waitcnt lgkmcnt(0)\n"
    "v_mfma_f32_16x16x32_bf16 v[184:187], v[204:207], v[112:115], v[184:187]\n"
    "v_mfma_f32_16x16x32_bf16 v[188:191], v[204:207], v[116:119], v[188:191]\n"
    "s_waitcnt lgkmcnt(0)\n"
    "s_barrier\n"
    "ds_read_b64_tr_b16 v[192:193], v245 offset:40960\n"
    "ds_read_b64_tr_b16 v[194:195], v245 offset:43008\n"
    "v_mfma_f32_16x16x32_bf16 v[160:163], v[208:211], v[120:123], v[160:163]\n"
    "ds_read_b64_tr_b16 v[196:197], v245 offset:45056\n"
    "ds_read_b64_tr_b16 v[198:199], v245 offset:47104\n"
    "v_mfma_f32_16x16x32_bf16 v[164:167], v[208:211], v[124:127], v[164:167]\n"
    "s_add_u32 m0, s34, 0x18000\n"
    "s_add_u32 s42, s71, 0x1e000\n"
    "v_mfma_f32_16x16x32_bf16 v[160:163], v[212:215], v[128:131], v[160:163]\n"
    "s_add_u32 s43, s71, 0x20400\n"
    "s_add_u32 s44, s71, 0x22800\n"
    "v_mfma_f32_16x16x32_bf16 v[164:167], v[212:215], v[132:135], v[164:167]\n"
    "buffer_load_dwordx4 v254, s[24:27], s42 offen lds\n"
    "v_mfma_f32_16x16x32_bf16 v[160:163], v[216:219], v[136:139], v[160:163]\n"
    "buffer_load_dwordx4 v254, s[24:27], s43 offen offset:1024 lds\n"
    "v_mfma_f32_16x16x32_bf16 v[164:167], v[216:219], v[140:143], v[164:167]\n"
    "buffer_load_dwordx4 v254, s[24:27], s44 offen offset:2048 lds\n"
    "v_mfma_f32_16x16x32_bf16 v[168:171], v[220:223], v[120:123], v[168:171]\n"
    "buffer_load_dword v253, v243, s[76:79], 0 offen\n"
    "v_mfma_f32_16x16x32_bf16 v[172:175], v[220:223], v[124:127], v[172:175]\n"
    "ds_read_b64_tr_b16 v[200:201], v246 offset:36864\n"
    "s_waitcnt lgkmcnt(1)\n"
    "v_mfma_f32_16x16x32_bf16 v[168:171], v[192:195], v[128:131], v[168:171]\n"
    "ds_read_b64_tr_b16 v[202:203], v246 offset:38912\n"
    "ds_read_b64_tr_b16 v[204:205], v246 offset:40960\n"
    "v_mfma_f32_16x16x32_bf16 v[172:175], v[192:195], v[132:135], v[172:175]\n"
    "ds_read_b64_tr_b16 v[206:207], v246 offset:43008\n"
    "ds_read_b64_tr_b16 v[208:209], v246 offset:45056\n"
    "v_mfma_f32_16x16x32_bf16 v[168:171], v[196:199], v[136:139], v[168:171]\n"
    "ds_read_b64_tr_b16 v[210:211], v246 offset:47104\n"
    "ds_read_b64_tr_b16 v[212:213], v247 offset:36864\n"
    "v_mfma_f32_16x16x32_bf16 v[172:175], v[196:199], v[140:143], v[172:175]\n"
    "ds_read_b64_tr_b16 v[214:215], v247 offset:38912\n"
    "v_cvt_pk_bf16_f32 v64, v160, v161\n"
    "s_waitcnt vmcnt(10) lgkmcnt(0)\n"
    "s_barrier\n"
    "ds_read_b64_tr_b16 v[216:217], v247 offset:40960\n"
    "ds_read_b64_tr_b16 v[218:219], v247 offset:43008\n"
    "v_mfma_f32_16x16x32_bf16 v[176:179], v[200:203], v[120:123], v[176:179]\n"
    "ds_read_b64_tr_b16 v[220:221], v247 offset:45056\n"
    "ds_read_b64_tr_b16 v[222:223], v247 offset:47104\n"
    "v_mfma_f32_16x16x32_bf16 v[180:183], v[200:203], v[124:127], v[180:183]\n"
    "ds_read_b64_tr_b16 v[192:193], v244 offset:49152\n"
    "ds_read_b64_tr_b16 v[194:195], v244 offset:51200\n"
    "v_mfma_f32_16x16x32_bf16 v[176:179], v[204:207], v[128:131], v[176:179]\n"
    "ds_read_b64_tr_b16 v[196:197], v244 offset:53248\n"
    "ds_read_b64_tr_b16 v[198:199], v244 offset:55296\n"
    "v_mfma_f32_16x16x32_bf16 v[180:183], v[204:207], v[132:135], v[180:183]\n"
    "ds_read_b64_tr_b16 v[200:201], v244 offset:57344\n"
    "ds_read_b64_tr_b16 v[202:203], v244 offset:59392\n"
    "v_mfma_f32_16x16x32_bf16 v[176:179], v[208:211], v[136:139], v[176:179]\n"
    "ds_read_b64_tr_b16 v[204:205], v245 offset:49152\n"
    "ds_read_b64_tr_b16 v[206:207], v245 offset:51200\n"
    "v_mfma_f32_16x16x32_bf16 v[180:183], v[208:211], v[140:143], v[180:183]\n"
    "ds_read_b64_tr_b16 v[208:209], v245 offset:53248\n"
    "ds_read_b64_tr_b16 v[210:211], v245 offset:55296\n"
    "v_mfma_f32_16x16x32_bf16 v[184:187], v[212:215], v[120:123], v[184:187]\n"
    "v_cvt_pk_bf16_f32 v65, v162, v163\n"
    "v_cvt_pk_bf16_f32 v68, v164, v165\n"
    "v_mfma_f32_16x16x32_bf16 v[188:191], v[212:215], v[124:127], v[188:191]\n"
    "ds_read_b64_tr_b16 v[212:213], v245 offset:57344\n"
    "s_waitcnt lgkmcnt(5)\n"
    "v_mfma_f32_16x16x32_bf16 v[184:187], v[216:219], v[128:131], v[184:187]\n"
    "ds_read_b64_tr_b16 v[214:215], v245 offset:59392\n"
    "v_cvt_pk_bf16_f32 v69, v166, v167\n"
    "v_mfma_f32_16x16x32_bf16 v[188:191], v[216:219], v[132:135], v[188:191]\n"
    "ds_read_b64_tr_b16 v[216:217], v246 offset:49152\n"
    "ds_read_b64_tr_b16 v[218:219], v246 offset:51200\n"
    "v_mfma_f32_16x16x32_bf16 v[184:187], v[220:223], v[136:139], v[184:187]\n"
    "v_cvt_pk_bf16_f32 v66, v168, v169\n"
    "v_cvt_pk_bf16_f32 v67, v170, v171\n"
    "v_mfma_f32_16x16x32_bf16 v[188:191], v[220:223], v[140:143], v[188:191]\n"
    "ds_read_b64_tr_b16 v[220:221], v246 offset:53248\n"
    "ds_read_b64_tr_b16 v[222:223], v246 offset:55296\n"
    "v_mfma_f32_16x16x32_bf16 v[160:163], v[192:195], v[96:99], 0\n"
    "v_cvt_pk_bf16_f32 v70, v172, v173\n"
    "v_cvt_pk_bf16_f32 v71, v174, v175\n"
    "v_mfma_f32_16x16x32_bf16 v[164:167], v[192:195], v[100:103], 0\n"
    "v_cvt_pk_bf16_f32 v72, v176, v177\n"
    "v_cvt_pk_bf16_f32 v73, v178, v179\n"
    "v_mfma_f32_16x16x32_bf16 v[160:163], v[196:199], v[104:107], v[160:163]\n"
    "v_cvt_pk_bf16_f32 v76, v180, v181\n"
    "v_cvt_pk_bf16_f32 v77, v182, v183\n"
    "v_mfma_f32_16x16x32_bf16 v[164:167], v[196:199], v[108:111], v[164:167]\n"
    "v_cvt_pk_bf16_f32 v74, v184, v185\n"
    "v_cvt_pk_bf16_f32 v75, v186, v187\n"
    "v_mfma_f32_16x16x32_bf16 v[160:163], v[200:203], v[112:115], v[160:163]\n"
    "v_cvt_pk_bf16_f32 v78, v188, v189\n"
    "v_cvt_pk_bf16_f32 v79, v190, v191\n"
    "v_mfma_f32_16x16x32_bf16 v[164:167], v[200:203], v[116:119], v[164:167]\n"
    "s_waitcnt lgkmcnt(0)\n"
    "v_mfma_f32_16x16x32_bf16 v[168:171], v[204:207], v[96:99], 0\n"
    "v_mfma_f32_16x16x32_bf16 v[172:175], v[204:207], v[100:103], 0\n"
    "s_waitcnt vmcnt(7) lgkmcnt(0)\n"
    "s_barrier\n"
    "ds_read_b64_tr_b16 v[192:193], v246 offset:57344\n"
    "ds_read_b64_tr_b16 v[194:195], v246 offset:59392\n"
    "v_mfma_f32_16x16x32_bf16 v[168:171], v[208:211], v[104:107], v[168:171]\n"
    "ds_read_b64_tr_b16 v[196:197], v247 offset:49152\n"
    "ds_read_b64_tr_b16 v[198:199], v247 offset:51200\n"
    "v_mfma_f32_16x16x32_bf16 v[172:175], v[208:211], v[108:111], v[172:175]\n"
    "s_add_u32 m0, s34, 0x1b000\n"
    "s_add_u32 s42, s72, 0x0\n"
    "v_mfma_f32_16x16x32_bf16 v[168:171], v[212:215], v[112:115], v[168:171]\n"
    "s_add_u32 s43, s72, 0x2400\n"
    "s_add_u32 s44, s72, 0x4800\n"
    "v_mfma_f32_16x16x32_bf16 v[172:175], v[212:215], v[116:119], v[172:175]\n"
    "buffer_load_dwordx4 v254, s[24:27], s42 offen lds\n"
    "v_mfma_f32_16x16x32_bf16 v[176:179], v[216:219], v[96:99], 0\n"
    "buffer_load_dwordx4 v254, s[24:27], s43 offen offset:1024 lds\n"
    "v_mfma_f32_16x16x32_bf16 v[180:183], v[216:219], v[100:103], 0\n"
    "buffer_load_dwordx4 v254, s[24:27], s44 offen offset:2048 lds\n"
    "v_mfma_f32_16x16x32_bf16 v[176:179], v[220:223], v[104:107], v[176:179]\n"
    "ds_read_b64_tr_b16 v[200:201], v247 offset:53248\n"
    "ds_read_b64_tr_b16 v[202:203], v247 offset:55296\n"
    "v_mfma_f32_16x16x32_bf16 v[180:183], v[220:223], v[108:111], v[180:183]\n"
    "ds_read_b64_tr_b16 v[204:205], v247 offset:57344\n"
    "s_waitcnt lgkmcnt(3)\n"
    "v_mfma_f32_16x16x32_bf16 v[176:179], v[192:195], v[112:115], v[176:179]\n"
    "ds_read_b64_tr_b16 v[206:207], v247 offset:59392\n"
    "ds_read_b64_tr_b16 v[208:209], v244\n"
    "v_mfma_f32_16x16x32_bf16 v[180:183], v[192:195], v[116:119], v[180:183]\n"
    "ds_read_b64_tr_b16 v[210:211], v244 offset:2048\n"
    "ds_read_b64_tr_b16 v[212:213], v244 offset:4096\n"
    "v_mfma_f32_16x16x32_bf16 v[184:187], v[196:199], v[96:99], 0\n"
    "ds_read_b64_tr_b16 v[214:215], v244 offset:6144\n"
    "v_mfma_f32_16x16x32_bf16 v[188:191], v[196:199], v[100:103], 0\n"
    "s_waitcnt lgkmcnt(0)\n"
    "s_barrier\n"
    "ds_read_b64_tr_b16 v[216:217], v244 offset:8192\n"
    "ds_read_b64_tr_b16 v[218:219], v244 offset:10240\n"
    "v_mfma_f32_16x16x32_bf16 v[184:187], v[200:203], v[104:107], v[184:187]\n"
    "ds_read_b64_tr_b16 v[220:221], v245\n"
    "ds_read_b64_tr_b16 v[222:223], v245 offset:2048\n"
    "v_mfma_f32_16x16x32_bf16 v[188:191], v[200:203], v[108:111], v[188:191]\n"
    "s_add_u32 m0, s34, 0x1e000\n"
    "s_add_u32 s42, s72, 0x1e000\n"
    "v_mfma_f32_16x16x32_bf16 v[184:187], v[204:207], v[112:115], v[184:187]\n"
    "s_add_u32 s43, s72, 0x20400\n"
    "s_add_u32 s44, s72, 0x22800\n"
    "v_mfma_f32_16x16x32_bf16 v[188:191], v[204:207], v[116:119], v[188:191]\n"
    "buffer_load_dwordx4 v254, s[24:27], s42 offen lds\n"
    "v_mfma_f32_16x16x32_bf16 v[160:163], v[208:211], v[120:123], v[160:163]\n"
    "buffer_load_dwordx4 v254, s[24:27], s43 offen offset:1024 lds\n"
    "v_mfma_f32_16x16x32_bf16 v[164:167], v[208:211], v[124:127], v[164:167]\n"
    "buffer_load_dwordx4 v254, s[24:27], s44 offen offset:2048 lds\n"
    "v_mfma_f32_16x16x32_bf16 v[160:163], v[212:215], v[128:131], v[160:163]\n"
    "ds_read_b64_tr_b16 v[192:193], v245 offset:4096\n"
    "ds_read_b64_tr_b16 v[194:195], v245 offset:6144\n"
    "v_mfma_f32_16x16x32_bf16 v[164:167], v[212:215], v[132:135], v[164:167]\n"
    "ds_read_b64_tr_b16 v[196:197], v245 offset:8192\n"
    "s_waitcnt lgkmcnt(3)\n"
    "v_mfma_f32_16x16x32_bf16 v[160:163], v[216:219], v[136:139], v[160:163]\n"
    "ds_read_b64_tr_b16 v[198:199], v245 offset:10240\n"
    "ds_read_b64_tr_b16 v[200:201], v246\n"
    "v_mfma_f32_16x16x32_bf16 v[164:167], v[216:219], v[140:143], v[164:167]\n"
    "ds_read_b64_tr_b16 v[202:203], v246 offset:2048\n"
    "ds_read_b64_tr_b16 v[204:205], v246 offset:4096\n"
    "v_mfma_f32_16x16x32_bf16 v[168:171], v[220:223], v[120:123], v[168:171]\n"
    "ds_read_b64_tr_b16 v[206:207], v246 offset:6144\n"
    "ds_read_b64_tr_b16 v[208:209], v246 offset:8192\n"
    "v_mfma_f32_16x16x32_bf16 v[172:175], v[220:223], v[124:127], v[172:175]\n"
    "ds_read_b64_tr_b16 v[210:211], v246 offset:10240\n"
    "s_waitcnt lgkmcnt(7)\n"
    "v_mfma_f32_16x16x32_bf16 v[168:171], v[192:195], v[128:131], v[168:171]\n"
    "ds_read_b64_tr_b16 v[212:213], v247\n"
    "ds_read_b64_tr_b16 v[214:215], v247 offset:2048\n"
    "v_mfma_f32_16x16x32_bf16 v[172:175], v[192:195], v[132:135], v[172:175]\n"
    "ds_read_b64_tr_b16 v[216:217], v247 offset:4096\n"
    "s_waitcnt lgkmcnt(6)\n"
    "v_mfma_f32_16x16x32_bf16 v[168:171], v[196:199], v[136:139], v[168:171]\n"
    "ds_read_b64_tr_b16 v[218:219], v247 offset:6144\n"
    "ds_read_b64_tr_b16 v[220:221], v247 offset:8192\n"
    "v_mfma_f32_16x16x32_bf16 v[172:175], v[196:199], v[140:143], v[172:175]\n"
    "ds_read_b64_tr_b16 v[222:223], v247 offset:10240\n"
    "v_cvt_pk_bf16_f32 v80, v160, v161\n"
    "v_mfma_f32_16x16x32_bf16 v[176:179], v[200:203], v[120:123], v[176:179]\n"
    "v_cvt_pk_bf16_f32 v81, v162, v163\n"
    "v_cvt_pk_bf16_f32 v84, v164, v165\n"
    "v_mfma_f32_16x16x32_bf16 v[180:183], v[200:203], v[124:127], v[180:183]\n"
    "v_cvt_pk_bf16_f32 v85, v166, v167\n"
    "s_waitcnt lgkmcnt(3)\n"
    "v_mfma_f32_16x16x32_bf16 v[176:179], v[204:207], v[128:131], v[176:179]\n"
    "v_mfma_f32_16x16x32_bf16 v[180:183], v[204:207], v[132:135], v[180:183]\n"
    "v_cvt_pk_bf16_f32 v82, v168, v169\n"
    "v_cvt_pk_bf16_f32 v83, v170, v171\n"
    "s_waitcnt vmcnt(10) lgkmcnt(0)\n"
    "s_barrier\n"
    "ds_read_b64_tr_b16 v[192:193], v244 offset:12288\n"
    "ds_read_b64_tr_b16 v[194:195], v244 offset:14336\n"
    "v_mfma_f32_16x16x32_bf16 v[176:179], v[208:211], v[136:139], v[176:179]\n"
    "ds_read_b64_tr_b16 v[196:197], v244 offset:16384\n"
    "ds_read_b64_tr_b16 v[198:199], v244 offset:18432\n"
    "v_mfma_f32_16x16x32_bf16 v[180:183], v[208:211], v[140:143], v[180:183]\n"
    "s_add_u32 m0, s34, 0x12000\n"
    "s_add_u32 s42, s72, 0x3c000\n"
    "v_mfma_f32_16x16x32_bf16 v[184:187], v[212:215], v[120:123], v[184:187]\n"
    "s_add_u32 s43, s72, 0x3e400\n"
    "s_add_u32 s44, s72, 0x40800\n"
    "v_mfma_f32_16x16x32_bf16 v[188:191], v[212:215], v[124:127], v[188:191]\n"
    "buffer_load_dwordx4 v254, s[24:27], s42 offen lds\n"
    "v_mfma_f32_16x16x32_bf16 v[184:187], v[216:219], v[128:131], v[184:187]\n"
    "buffer_load_dwordx4 v254, s[24:27], s43 offen offset:1024 lds\n"
    "v_mfma_f32_16x16x32_bf16 v[188:191], v[216:219], v[132:135], v[188:191]\n"
    "buffer_load_dwordx4 v254, s[24:27], s44 offen offset:2048 lds\n"
    "v_mfma_f32_16x16x32_bf16 v[184:187], v[220:223], v[136:139], v[184:187]\n"
    "ds_read_b64_tr_b16 v[200:201], v244 offset:20480\n"
    "ds_read_b64_tr_b16 v[202:203], v244 offset:22528\n"
    "v_mfma_f32_16x16x32_bf16 v[188:191], v[220:223], v[140:143], v[188:191]\n"
    "ds_read_b64_tr_b16 v[204:205], v245 offset:12288\n"
    "s_waitcnt lgkmcnt(3)\n"
    "v_mfma_f32_16x16x32_bf16 v[160:163], v[192:195], v[96:99], 0\n"
    "ds_read_b64_tr_b16 v[206:207], v245 offset:14336\n"
    "ds_read_b64_tr_b16 v[208:209], v245 offset:16384\n"
    "v_mfma_f32_16x16x32_bf16 v[164:167], v[192:195], v[100:103], 0\n"
    "ds_read_b64_tr_b16 v[210:211], v245 offset:18432\n"
    "ds_read_b64_tr_b16 v[212:213], v245 offset:20480\n"
    "v_mfma_f32_16x16x32_bf16 v[160:163], v[196:199], v[104:107], v[160:163]\n"
    "ds_read_b64_tr_b16 v[214:215], v245 offset:22528\n"
    "v_cvt_pk_bf16_f32 v86, v172, v173\n"
    "v_mfma_f32_16x16x32_bf16 v[164:167], v[196:199], v[108:111], v[164:167]\n"
    "v_cvt_pk_bf16_f32 v87, v174, v175\n"
    "v_cvt_pk_bf16_f32 v88, v176, v177\n"
    "s_waitcnt vmcnt(10) lgkmcnt(0)\n"
    "s_barrier\n"
    "ds_read_b64_tr_b16 v[216:217], v246 offset:12288\n"
    "ds_read_b64_tr_b16 v[218:219], v246 offset:14336\n"
    "v_mfma_f32_16x16x32_bf16 v[160:163], v[200:203], v[112:115], v[160:163]\n"
    "ds_read_b64_tr_b16 v[220:221], v246 offset:16384\n"
    "ds_read_b64_tr_b16 v[222:223], v246 offset:18432\n"
    "v_mfma_f32_16x16x32_bf16 v[164:167], v[200:203], v[116:119], v[164:167]\n"
    "ds_read_b64_tr_b16 v[192:193], v246 offset:20480\n"
    "ds_read_b64_tr_b16 v[194:195], v246 offset:22528\n"
    "v_mfma_f32_16x16x32_bf16 v[168:171], v[204:207], v[96:99], 0\n"
    "ds_read_b64_tr_b16 v[196:197], v247 offset:12288\n"
    "ds_read_b64_tr_b16 v[198:199], v247 offset:14336\n"
    "v_mfma_f32_16x16x32_bf16 v[172:175], v[204:207], v[100:103], 0\n"
    "ds_read_b64_tr_b16 v[200:201], v247 offset:16384\n"
    "ds_read_b64_tr_b16 v[202:203], v247 offset:18432\n"
    "v_mfma_f32_16x16x32_bf16 v[168:171], v[208:211], v[104:107], v[168:171]\n"
    "ds_read_b64_tr_b16 v[204:205], v247 offset:20480\n"
    "ds_read_b64_tr_b16 v[206:207], v247 offset:22528\n"
    "v_mfma_f32_16x16x32_bf16 v[172:175], v[208:211], v[108:111], v[172:175]\n"
    "ds_read_b64_tr_b16 v[208:209], v244 offset:24576\n"
    "ds_read_b64_tr_b16 v[210:211], v244 offset:26624\n"
    "v_mfma_f32_16x16x32_bf16 v[168:171], v[212:215], v[112:115], v[168:171]\n"
    "v_cvt_pk_bf16_f32 v89, v178, v179\n"
    "v_cvt_pk_bf16_f32 v92, v180, v181\n"
    "v_mfma_f32_16x16x32_bf16 v[172:175], v[212:215], v[116:119], v[172:175]\n"
    "ds_read_b64_tr_b16 v[212:213], v244 offset:28672\n"
    "v_cvt_pk_bf16_f32 v93, v182, v183\n"
    "s_waitcnt lgkmcnt(5)\n"
    "v_mfma_f32_16x16x32_bf16 v[176:179], v[216:219], v[96:99], 0\n"
    "ds_read_b64_tr_b16 v[214:215], v244 offset:30720\n"
    "v_cvt_pk_bf16_f32 v90, v184, v185\n"
    "v_mfma_f32_16x16x32_bf16 v[180:183], v[216:219], v[100:103], 0\n"
    "ds_read_b64_tr_b16 v[216:217], v244 offset:32768\n"
    "ds_read_b64_tr_b16 v[218:219], v244 offset:34816\n"
    "v_mfma_f32_16x16x32_bf16 v[176:179], v[220:223], v[104:107], v[176:179]\n"
    "v_cvt_pk_bf16_f32 v91, v186, v187\n"
    "v_cvt_pk_bf16_f32 v94, v188, v189\n"
    "v_mfma_f32_16x16x32_bf16 v[180:183], v[220:223], v[108:111], v[180:183]\n"
    "ds_read_b64_tr_b16 v[220:221], v245 offset:24576\n"
    "ds_read_b64_tr_b16 v[222:223], v245 offset:26624\n"
    "v_mfma_f32_16x16x32_bf16 v[176:179], v[192:195], v[112:115], v[176:179]\n"
    "v_cvt_pk_bf16_f32 v95, v190, v191\n"
    "v_mfma_f32_16x16x32_bf16 v[180:183], v[192:195], v[116:119], v[180:183]\n"
    "v_mfma_f32_16x16x32_bf16 v[184:187], v[196:199], v[96:99], 0\n"
    "v_mfma_f32_16x16x32_bf16 v[188:191], v[196:199], v[100:103], 0\n"
    "v_mfma_f32_16x16x32_bf16 v[184:187], v[200:203], v[104:107], v[184:187]\n"
    "v_mfma_f32_16x16x32_bf16 v[188:191], v[200:203], v[108:111], v[188:191]\n"
    "s_waitcnt lgkmcnt(0)\n"
    "v_mfma_f32_16x16x32_bf16 v[184:187], v[204:207], v[112:115], v[184:187]\n"
    "v_mfma_f32_16x16x32_bf16 v[188:191], v[204:207], v[116:119], v[188:191]\n"
    "s_waitcnt lgkmcnt(0)\n"
    "s_barrier\n"
    "ds_read_b64_tr_b16 v[192:193], v245 offset:28672\n"
    "ds_read_b64_tr_b16 v[194:195], v245 offset:30720\n"
    "v_mfma_f32_16x16x32_bf16 v[160:163], v[208:211], v[120:123], v[160:163]\n"
    "ds_read_b64_tr_b16 v[196:197], v245 offset:32768\n"
    "ds_read_b64_tr_b16 v[198:199], v245 offset:34816\n"
    "v_mfma_f32_16x16x32_bf16 v[164:167], v[208:211], v[124:127], v[164:167]\n"
    "s_add_u32 m0, s34, 0x15000\n"
    "s_add_u32 s42, s72, 0x5a000\n"
    "v_mfma_f32_16x16x32_bf16 v[160:163], v[212:215], v[128:131], v[160:163]\n"
    "s_add_u32 s43, s72, 0x5c400\n"
    "s_add_u32 s44, s72, 0x5e800\n"
    "v_mfma_f32_16x16x32_bf16 v[164:167], v[212:215], v[132:135], v[164:167]\n"
    "buffer_load_dwordx4 v254, s[24:27], s42 offen lds\n"
    "v_mfma_f32_16x16x32_bf16 v[160:163], v[216:219], v[136:139], v[160:163]\n"
    "buffer_load_dwordx4 v254, s[24:27], s43 offen offset:1024 lds\n"
    "v_mfma_f32_16x16x32_bf16 v[164:167], v[216:219], v[140:143], v[164:167]\n"
    "buffer_load_dwordx4 v254, s[24:27], s44 offen offset:2048 lds\n"
    "v_mfma_f32_16x16x32_bf16 v[168:171], v[220:223], v[120:123], v[168:171]\n"
    "s_mov_b32 s58, 0x3c000\n"
    "ds_read_b64_tr_b16 v[200:201], v246 offset:24576\n"
    "v_mfma_f32_16x16x32_bf16 v[172:175], v[220:223], v[124:127], v[172:175]\n"
    "ds_read_b64_tr_b16 v[202:203], v246 offset:26624\n"
    "s_waitcnt lgkmcnt(2)\n"
    "v_mfma_f32_16x16x32_bf16 v[168:171], v[192:195], v[128:131], v[168:171]\n"
    "buffer_load_dword v253, v242, s[24:27], s58 offen\n"
    "v_mfma_f32_16x16x32_bf16 v[172:175], v[192:195], v[132:135], v[172:175]\n"
    "ds_read_b64_tr_b16 v[204:205], v246 offset:28672\n"
    "ds_read_b64_tr_b16 v[206:207], v246 offset:30720\n"
    "v_mfma_f32_16x16x32_bf16 v[168:171], v[196:199], v[136:139], v[168:171]\n"
    "ds_read_b64_tr_b16 v[208:209], v246 offset:32768\n"
    "ds_read_b64_tr_b16 v[210:211], v246 offset:34816\n"
    "v_mfma_f32_16x16x32_bf16 v[172:175], v[196:199], v[140:143], v[172:175]\n"
    "ds_read_b64_tr_b16 v[212:213], v247 offset:24576\n"
    "ds_read_b64_tr_b16 v[214:215], v247 offset:26624\n"
    "s_waitcnt vmcnt(10) lgkmcnt(0)\n"
    "s_barrier\n"
    "ds_read_b64_tr_b16 v[216:217], v247 offset:28672\n"
    "ds_read_b64_tr_b16 v[218:219], v247 offset:30720\n"
    "v_mfma_f32_16x16x32_bf16 v[176:179], v[200:203], v[120:123], v[176:179]\n"
    "ds_read_b64_tr_b16 v[220:221], v247 offset:32768\n"
    "ds_read_b64_tr_b16 v[222:223], v247 offset:34816\n"
    "v_mfma_f32_16x16x32_bf16 v[180:183], v[200:203], v[124:127], v[180:183]\n"
    "ds_read_b64_tr_b16 v[192:193], v244 offset:36864\n"
    "ds_read_b64_tr_b16 v[194:195], v244 offset:38912\n"
    "v_mfma_f32_16x16x32_bf16 v[176:179], v[204:207], v[128:131], v[176:179]\n"
    "ds_read_b64_tr_b16 v[196:197], v244 offset:40960\n"
    "ds_read_b64_tr_b16 v[198:199], v244 offset:43008\n"
    "v_mfma_f32_16x16x32_bf16 v[180:183], v[204:207], v[132:135], v[180:183]\n"
    "ds_read_b64_tr_b16 v[200:201], v244 offset:45056\n"
    "ds_read_b64_tr_b16 v[202:203], v244 offset:47104\n"
    "v_mfma_f32_16x16x32_bf16 v[176:179], v[208:211], v[136:139], v[176:179]\n"
    "ds_read_b64_tr_b16 v[204:205], v245 offset:36864\n"
    "ds_read_b64_tr_b16 v[206:207], v245 offset:38912\n"
    "v_mfma_f32_16x16x32_bf16 v[180:183], v[208:211], v[140:143], v[180:183]\n"
    "ds_read_b64_tr_b16 v[208:209], v245 offset:40960\n"
    "ds_read_b64_tr_b16 v[210:211], v245 offset:43008\n"
    "v_mfma_f32_16x16x32_bf16 v[184:187], v[212:215], v[120:123], v[184:187]\n"
    "v_cvt_pk_bf16_f32 v144, v160, v161\n"
    "v_cvt_pk_bf16_f32 v145, v162, v163\n"
    "v_mfma_f32_16x16x32_bf16 v[188:191], v[212:215], v[124:127], v[188:191]\n"
    "ds_read_b64_tr_b16 v[212:213], v245 offset:45056\n"
    "s_waitcnt lgkmcnt(5)\n"
    "v_mfma_f32_16x16x32_bf16 v[184:187], v[216:219], v[128:131], v[184:187]\n"
    "ds_read_b64_tr_b16 v[214:215], v245 offset:47104\n"
    "v_cvt_pk_bf16_f32 v148, v164, v165\n"
    "v_mfma_f32_16x16x32_bf16 v[188:191], v[216:219], v[132:135], v[188:191]\n"
    "ds_read_b64_tr_b16 v[216:217], v246 offset:36864\n"
    "ds_read_b64_tr_b16 v[218:219], v246 offset:38912\n"
    "v_mfma_f32_16x16x32_bf16 v[184:187], v[220:223], v[136:139], v[184:187]\n"
    "v_cvt_pk_bf16_f32 v149, v166, v167\n"
    "v_cvt_pk_bf16_f32 v146, v168, v169\n"
    "v_mfma_f32_16x16x32_bf16 v[188:191], v[220:223], v[140:143], v[188:191]\n"
    "ds_read_b64_tr_b16 v[220:221], v246 offset:40960\n"
    "ds_read_b64_tr_b16 v[222:223], v246 offset:43008\n"
    "v_mfma_f32_16x16x32_bf16 v[160:163], v[192:195], v[96:99], 0\n"
    "v_cvt_pk_bf16_f32 v147, v170, v171\n"
    "v_cvt_pk_bf16_f32 v150, v172, v173\n"
    "v_mfma_f32_16x16x32_bf16 v[164:167], v[192:195], v[100:103], 0\n"
    "v_cvt_pk_bf16_f32 v151, v174, v175\n"
    "v_cvt_pk_bf16_f32 v152, v176, v177\n"
    "v_mfma_f32_16x16x32_bf16 v[160:163], v[196:199], v[104:107], v[160:163]\n"
    "v_cvt_pk_bf16_f32 v153, v178, v179\n"
    "v_cvt_pk_bf16_f32 v156, v180, v181\n"
    "v_mfma_f32_16x16x32_bf16 v[164:167], v[196:199], v[108:111], v[164:167]\n"
    "v_cvt_pk_bf16_f32 v157, v182, v183\n"
    "v_cvt_pk_bf16_f32 v154, v184, v185\n"
    "v_mfma_f32_16x16x32_bf16 v[160:163], v[200:203], v[112:115], v[160:163]\n"
    "v_cvt_pk_bf16_f32 v155, v186, v187\n"
    "v_cvt_pk_bf16_f32 v158, v188, v189\n"
    "v_mfma_f32_16x16x32_bf16 v[164:167], v[200:203], v[116:119], v[164:167]\n"
    "v_cvt_pk_bf16_f32 v159, v190, v191\n"
    "s_waitcnt lgkmcnt(0)\n"
    "v_mfma_f32_16x16x32_bf16 v[168:171], v[204:207], v[96:99], 0\n"
    "v_mfma_f32_16x16x32_bf16 v[172:175], v[204:207], v[100:103], 0\n"
    "s_waitcnt vmcnt(7) lgkmcnt(0)\n"
    "s_barrier\n"
    "ds_read_b64_tr_b16 v[192:193], v246 offset:45056\n"
    "ds_read_b64_tr_b16 v[194:195], v246 offset:47104\n"
    "v_mfma_f32_16x16x32_bf16 v[168:171], v[208:211], v[104:107], v[168:171]\n"
    "ds_read_b64_tr_b16 v[196:197], v247 offset:36864\n"
    "ds_read_b64_tr_b16 v[198:199], v247 offset:38912\n"
    "v_mfma_f32_16x16x32_bf16 v[172:175], v[208:211], v[108:111], v[172:175]\n"
    "s_add_u32 m0, s34, 0x18000\n"
    "s_add_u32 s42, s65, 0x3c000\n"
    "v_mfma_f32_16x16x32_bf16 v[168:171], v[212:215], v[112:115], v[168:171]\n"
    "s_add_u32 s43, s65, 0x3e400\n"
    "s_add_u32 s44, s65, 0x40800\n"
    "v_mfma_f32_16x16x32_bf16 v[172:175], v[212:215], v[116:119], v[172:175]\n"
    "buffer_load_dwordx4 v254, s[24:27], s42 offen lds\n"
    "v_mfma_f32_16x16x32_bf16 v[176:179], v[216:219], v[96:99], 0\n"
    "buffer_load_dwordx4 v254, s[24:27], s43 offen offset:1024 lds\n"
    "v_mfma_f32_16x16x32_bf16 v[180:183], v[216:219], v[100:103], 0\n"
    "buffer_load_dwordx4 v254, s[24:27], s44 offen offset:2048 lds\n"
    "v_mfma_f32_16x16x32_bf16 v[176:179], v[220:223], v[104:107], v[176:179]\n"
    "ds_read_b64_tr_b16 v[200:201], v247 offset:40960\n"
    "ds_read_b64_tr_b16 v[202:203], v247 offset:43008\n"
    "v_mfma_f32_16x16x32_bf16 v[180:183], v[220:223], v[108:111], v[180:183]\n"
    "ds_read_b64_tr_b16 v[204:205], v247 offset:45056\n"
    "s_waitcnt lgkmcnt(3)\n"
    "v_mfma_f32_16x16x32_bf16 v[176:179], v[192:195], v[112:115], v[176:179]\n"
    "ds_read_b64_tr_b16 v[206:207], v247 offset:47104\n"
    "ds_read_b64_tr_b16 v[208:209], v244 offset:49152\n"
    "v_mfma_f32_16x16x32_bf16 v[180:183], v[192:195], v[116:119], v[180:183]\n"
    "ds_read_b64_tr_b16 v[210:211], v244 offset:51200\n"
    "ds_read_b64_tr_b16 v[212:213], v244 offset:53248\n"
    "v_mfma_f32_16x16x32_bf16 v[184:187], v[196:199], v[96:99], 0\n"
    "ds_read_b64_tr_b16 v[214:215], v244 offset:55296\n"
    "v_mfma_f32_16x16x32_bf16 v[188:191], v[196:199], v[100:103], 0\n"
    "s_waitcnt lgkmcnt(0)\n"
    "s_barrier\n"
    "ds_read_b64_tr_b16 v[216:217], v244 offset:57344\n"
    "ds_read_b64_tr_b16 v[218:219], v244 offset:59392\n"
    "v_mfma_f32_16x16x32_bf16 v[184:187], v[200:203], v[104:107], v[184:187]\n"
    "ds_read_b64_tr_b16 v[220:221], v245 offset:49152\n"
    "ds_read_b64_tr_b16 v[222:223], v245 offset:51200\n"
    "v_mfma_f32_16x16x32_bf16 v[188:191], v[200:203], v[108:111], v[188:191]\n"
    "s_add_u32 m0, s34, 0x1b000\n"
    "s_add_u32 s42, s65, 0x5a000\n"
    "v_mfma_f32_16x16x32_bf16 v[184:187], v[204:207], v[112:115], v[184:187]\n"
    "s_add_u32 s43, s65, 0x5c400\n"
    "s_add_u32 s44, s65, 0x5e800\n"
    "v_mfma_f32_16x16x32_bf16 v[188:191], v[204:207], v[116:119], v[188:191]\n"
    "buffer_load_dwordx4 v254, s[24:27], s42 offen lds\n"
    "v_mfma_f32_16x16x32_bf16 v[160:163], v[208:211], v[120:123], v[160:163]\n"
    "buffer_load_dwordx4 v254, s[24:27], s43 offen offset:1024 lds\n"
    "v_mfma_f32_16x16x32_bf16 v[164:167], v[208:211], v[124:127], v[164:167]\n"
    "buffer_load_dwordx4 v254, s[24:27], s44 offen offset:2048 lds\n"
    "v_mfma_f32_16x16x32_bf16 v[160:163], v[212:215], v[128:131], v[160:163]\n"
    "ds_read_b64_tr_b16 v[192:193], v245 offset:53248\n"
    "ds_read_b64_tr_b16 v[194:195], v245 offset:55296\n"
    "v_mfma_f32_16x16x32_bf16 v[164:167], v[212:215], v[132:135], v[164:167]\n"
    "ds_read_b64_tr_b16 v[196:197], v245 offset:57344\n"
    "s_waitcnt lgkmcnt(3)\n"
    "v_mfma_f32_16x16x32_bf16 v[160:163], v[216:219], v[136:139], v[160:163]\n"
    "ds_read_b64_tr_b16 v[198:199], v245 offset:59392\n"
    "ds_read_b64_tr_b16 v[200:201], v246 offset:49152\n"
    "v_mfma_f32_16x16x32_bf16 v[164:167], v[216:219], v[140:143], v[164:167]\n"
    "ds_read_b64_tr_b16 v[202:203], v246 offset:51200\n"
    "ds_read_b64_tr_b16 v[204:205], v246 offset:53248\n"
    "v_mfma_f32_16x16x32_bf16 v[168:171], v[220:223], v[120:123], v[168:171]\n"
    "ds_read_b64_tr_b16 v[206:207], v246 offset:55296\n"
    "ds_read_b64_tr_b16 v[208:209], v246 offset:57344\n"
    "v_mfma_f32_16x16x32_bf16 v[172:175], v[220:223], v[124:127], v[172:175]\n"
    "ds_read_b64_tr_b16 v[210:211], v246 offset:59392\n"
    "s_waitcnt lgkmcnt(7)\n"
    "v_mfma_f32_16x16x32_bf16 v[168:171], v[192:195], v[128:131], v[168:171]\n"
    "ds_read_b64_tr_b16 v[212:213], v247 offset:49152\n"
    "ds_read_b64_tr_b16 v[214:215], v247 offset:51200\n"
    "v_mfma_f32_16x16x32_bf16 v[172:175], v[192:195], v[132:135], v[172:175]\n"
    "ds_read_b64_tr_b16 v[216:217], v247 offset:53248\n"
    "s_waitcnt lgkmcnt(6)\n"
    "v_mfma_f32_16x16x32_bf16 v[168:171], v[196:199], v[136:139], v[168:171]\n"
    "ds_read_b64_tr_b16 v[218:219], v247 offset:55296\n"
    "ds_read_b64_tr_b16 v[220:221], v247 offset:57344\n"
    "v_mfma_f32_16x16x32_bf16 v[172:175], v[196:199], v[140:143], v[172:175]\n"
    "ds_read_b64_tr_b16 v[222:223], v247 offset:59392\n"
    "v_mfma_f32_16x16x32_bf16 v[176:179], v[200:203], v[120:123], v[176:179]\n"
    "v_mfma_f32_16x16x32_bf16 v[180:183], v[200:203], v[124:127], v[180:183]\n"
    "s_waitcnt lgkmcnt(3)\n"
    "v_mfma_f32_16x16x32_bf16 v[176:179], v[204:207], v[128:131], v[176:179]\n"
    "v_mfma_f32_16x16x32_bf16 v[180:183], v[204:207], v[132:135], v[180:183]\n"
    "s_waitcnt lgkmcnt(0)\n"
    "s_barrier\n"
    "v_mfma_f32_16x16x32_bf16 v[176:179], v[208:211], v[136:139], v[176:179]\n"
    "v_mfma_f32_16x16x32_bf16 v[180:183], v[208:211], v[140:143], v[180:183]\n"
    "v_mfma_f32_16x16x32_bf16 v[184:187], v[212:215], v[120:123], v[184:187]\n"
    "v_mfma_f32_16x16x32_bf16 v[188:191], v[212:215], v[124:127], v[188:191]\n"
    "v_mfma_f32_16x16x32_bf16 v[184:187], v[216:219], v[128:131], v[184:187]\n"
    "v_mfma_f32_16x16x32_bf16 v[188:191], v[216:219], v[132:135], v[188:191]\n"
    "v_mfma_f32_16x16x32_bf16 v[184:187], v[220:223], v[136:139], v[184:187]\n"
    "v_mfma_f32_16x16x32_bf16 v[188:191], v[220:223], v[140:143], v[188:191]\n"
    "s_waitcnt lgkmcnt(0)\n"
    "s_barrier\n"
    "s_waitcnt vmcnt(10) lgkmcnt(0)\n"
    "s_barrier\n"
    "ds_read_b128 v[96:99], v252\n"
    "ds_read_b128 v[100:103], v252 offset:1024\n"
    "ds_read_b64_tr_b16 v[192:193], v244\n"
    "ds_read_b64_tr_b16 v[194:195], v244 offset:2048\n"
    "s_waitcnt lgkmcnt(0)\n"
    "v_mfma_f32_16x16x32_bf16 v[160:163], v[192:195], v[96:99], v[160:163]\n"
    "ds_read_b128 v[104:107], v252 offset:2048\n"
    "ds_read_b128 v[108:111], v252 offset:12288\n"
    "v_mfma_f32_16x16x32_bf16 v[164:167], v[192:195], v[100:103], v[164:167]\n"
    "ds_read_b128 v[112:115], v252 offset:13312\n"
    "ds_read_b128 v[116:119], v252 offset:14336\n"
    "ds_read_b64_tr_b16 v[196:197], v244 offset:4096\n"
    "ds_read_b64_tr_b16 v[198:199], v244 offset:6144\n"
    "s_waitcnt lgkmcnt(0)\n"
    "v_mfma_f32_16x16x32_bf16 v[160:163], v[196:199], v[104:107], v[160:163]\n"
    "ds_read_b64_tr_b16 v[200:201], v244 offset:8192\n"
    "ds_read_b64_tr_b16 v[202:203], v244 offset:10240\n"
    "v_mfma_f32_16x16x32_bf16 v[164:167], v[196:199], v[108:111], v[164:167]\n"
    "ds_read_b64_tr_b16 v[204:205], v245\n"
    "s_waitcnt lgkmcnt(1)\n"
    "v_mfma_f32_16x16x32_bf16 v[160:163], v[200:203], v[112:115], v[160:163]\n"
    "ds_read_b64_tr_b16 v[206:207], v245 offset:2048\n"
    "ds_read_b128 v[120:123], v252 offset:24576\n"
    "v_mfma_f32_16x16x32_bf16 v[164:167], v[200:203], v[116:119], v[164:167]\n"
    "ds_read_b128 v[124:127], v252 offset:25600\n"
    "s_waitcnt lgkmcnt(2)\n"
    "v_mfma_f32_16x16x32_bf16 v[168:171], v[204:207], v[96:99], v[168:171]\n"
    "ds_read_b128 v[128:131], v252 offset:26624\n"
    "ds_read_b128 v[132:135], v252 offset:36864\n"
    "v_mfma_f32_16x16x32_bf16 v[172:175], v[204:207], v[100:103], v[172:175]\n"
    "ds_read_b128 v[136:139], v252 offset:37888\n"
    "ds_read_b128 v[140:143], v252 offset:38912\n"
    "ds_read_b64_tr_b16 v[208:209], v245 offset:4096\n"
    "ds_read_b64_tr_b16 v[210:211], v245 offset:6144\n"
    "s_waitcnt lgkmcnt(0)\n"
    "v_mfma_f32_16x16x32_bf16 v[168:171], v[208:211], v[104:107], v[168:171]\n"
    "ds_read_b64_tr_b16 v[212:213], v245 offset:8192\n"
    "ds_read_b64_tr_b16 v[214:215], v245 offset:10240\n"
    "v_mfma_f32_16x16x32_bf16 v[172:175], v[208:211], v[108:111], v[172:175]\n"
    "ds_read_b64_tr_b16 v[216:217], v246\n"
    "s_waitcnt lgkmcnt(1)\n"
    "v_mfma_f32_16x16x32_bf16 v[168:171], v[212:215], v[112:115], v[168:171]\n"
    "ds_read_b64_tr_b16 v[218:219], v246 offset:2048\n"
    "ds_read_b64_tr_b16 v[220:221], v246 offset:4096\n"
    "v_mfma_f32_16x16x32_bf16 v[172:175], v[212:215], v[116:119], v[172:175]\n"
    "ds_read_b64_tr_b16 v[222:223], v246 offset:6144\n"
    "s_waitcnt lgkmcnt(2)\n"
    "v_mfma_f32_16x16x32_bf16 v[176:179], v[216:219], v[96:99], v[176:179]\n"
    "ds_read_b64_tr_b16 v[192:193], v246 offset:8192\n"
    "ds_read_b64_tr_b16 v[194:195], v246 offset:10240\n"
    "v_mfma_f32_16x16x32_bf16 v[180:183], v[216:219], v[100:103], v[180:183]\n"
    "ds_read_b64_tr_b16 v[196:197], v247\n"
    "s_waitcnt lgkmcnt(3)\n"
    "v_mfma_f32_16x16x32_bf16 v[176:179], v[220:223], v[104:107], v[176:179]\n"
    "ds_read_b64_tr_b16 v[198:199], v247 offset:2048\n"
    "ds_read_b64_tr_b16 v[200:201], v247 offset:4096\n"
    "v_mfma_f32_16x16x32_bf16 v[180:183], v[220:223], v[108:111], v[180:183]\n"
    "ds_read_b64_tr_b16 v[202:203], v247 offset:6144\n"
    "ds_read_b64_tr_b16 v[204:205], v247 offset:8192\n"
    "ds_read_b64_tr_b16 v[206:207], v247 offset:10240\n"
    "s_waitcnt vmcnt(7) lgkmcnt(0)\n"
    "s_barrier\n"
    "ds_read_b64_tr_b16 v[208:209], v244 offset:12288\n"
    "ds_read_b64_tr_b16 v[210:211], v244 offset:14336\n"
    "v_mfma_f32_16x16x32_bf16 v[176:179], v[192:195], v[112:115], v[176:179]\n"
    "ds_read_b64_tr_b16 v[212:213], v244 offset:16384\n"
    "ds_read_b64_tr_b16 v[214:215], v244 offset:18432\n"
    "v_mfma_f32_16x16x32_bf16 v[180:183], v[192:195], v[116:119], v[180:183]\n"
    "ds_read_b64_tr_b16 v[216:217], v244 offset:20480\n"
    "ds_read_b64_tr_b16 v[218:219], v244 offset:22528\n"
    "v_mfma_f32_16x16x32_bf16 v[184:187], v[196:199], v[96:99], v[184:187]\n"
    "ds_read_b64_tr_b16 v[220:221], v245 offset:12288\n"
    "ds_read_b64_tr_b16 v[222:223], v245 offset:14336\n"
    "v_mfma_f32_16x16x32_bf16 v[188:191], v[196:199], v[100:103], v[188:191]\n"
    "ds_read_b64_tr_b16 v[192:193], v245 offset:16384\n"
    "ds_read_b64_tr_b16 v[194:195], v245 offset:18432\n"
    "v_mfma_f32_16x16x32_bf16 v[184:187], v[200:203], v[104:107], v[184:187]\n"
    "ds_read_b64_tr_b16 v[196:197], v245 offset:20480\n"
    "ds_read_b64_tr_b16 v[198:199], v245 offset:22528\n"
    "v_mfma_f32_16x16x32_bf16 v[188:191], v[200:203], v[108:111], v[188:191]\n"
    "ds_read_b64_tr_b16 v[200:201], v246 offset:12288\n"
    "ds_read_b64_tr_b16 v[202:203], v246 offset:14336\n"
    "v_mfma_f32_16x16x32_bf16 v[184:187], v[204:207], v[112:115], v[184:187]\n"
    "v_lshrrev_b32_e32 v243, 2, v241\n"
    "v_lshlrev_b32_e64 v242, 5, v241\n"
    "v_mfma_f32_16x16x32_bf16 v[188:191], v[204:207], v[116:119], v[188:191]\n"
    "ds_read_b64_tr_b16 v[204:205], v246 offset:16384\n"
    "s_waitcnt lgkmcnt(5)\n"
    "v_mfma_f32_16x16x32_bf16 v[160:163], v[208:211], v[120:123], v[160:163]\n"
    "ds_read_b64_tr_b16 v[206:207], v246 offset:18432\n"
    "v_cmp_gt_u32_e32 vcc, 64, v243\n"
    "v_mfma_f32_16x16x32_bf16 v[164:167], v[208:211], v[124:127], v[164:167]\n"
    "ds_read_b64_tr_b16 v[208:209], v246 offset:20480\n"
    "ds_read_b64_tr_b16 v[210:211], v246 offset:22528\n"
    "v_mfma_f32_16x16x32_bf16 v[160:163], v[212:215], v[128:131], v[160:163]\n"
    "v_mov_b32_e32 v240, 2.0\n"
    "s_nop 0\n"
    "v_mfma_f32_16x16x32_bf16 v[164:167], v[212:215], v[132:135], v[164:167]\n"
    "ds_read_b64_tr_b16 v[212:213], v247 offset:12288\n"
    "ds_read_b64_tr_b16 v[214:215], v247 offset:14336\n"
    "v_mfma_f32_16x16x32_bf16 v[160:163], v[216:219], v[136:139], v[160:163]\n"
    "v_cndmask_b32_e32 v243, v240, v243, vcc\n"
    "v_cndmask_b32_e32 v242, v240, v242, vcc\n"
    "v_mfma_f32_16x16x32_bf16 v[164:167], v[216:219], v[140:143], v[164:167]\n"
    "ds_read_b64_tr_b16 v[216:217], v247 offset:16384\n"
    "ds_read_b64_tr_b16 v[218:219], v247 offset:18432\n"
    "v_mfma_f32_16x16x32_bf16 v[168:171], v[220:223], v[120:123], v[168:171]\n"
    "s_mov_b32 s58, 0x232000\n"
    "v_mfma_f32_16x16x32_bf16 v[172:175], v[220:223], v[124:127], v[172:175]\n"
    "ds_read_b64_tr_b16 v[220:221], v247 offset:20480\n"
    "ds_read_b64_tr_b16 v[222:223], v247 offset:22528\n"
    "v_mfma_f32_16x16x32_bf16 v[168:171], v[192:195], v[128:131], v[168:171]\n"
    "buffer_load_dword v240, v243, s[28:31], s58 offen sc1\n"
    "v_mfma_f32_16x16x32_bf16 v[172:175], v[192:195], v[132:135], v[172:175]\n"
    "v_add_u32_e32 v252, s59, v241\n"
    "s_waitcnt lgkmcnt(2)\n"
    "v_mfma_f32_16x16x32_bf16 v[168:171], v[196:199], v[136:139], v[168:171]\n"
    "s_lshl_b32 s98, s33, 2\n"
    "s_sub_u32 s98, s86, s98\n"
    "v_mfma_f32_16x16x32_bf16 v[172:175], v[196:199], v[140:143], v[172:175]\n"
    "v_cvt_pk_bf16_f32 v224, v160, v161\n"
    "v_cvt_pk_bf16_f32 v225, v162, v163\n"
    "v_mfma_f32_16x16x32_bf16 v[176:179], v[200:203], v[120:123], v[176:179]\n"
    "v_cvt_pk_bf16_f32 v228, v164, v165\n"
    "v_cvt_pk_bf16_f32 v229, v166, v167\n"
    "v_mfma_f32_16x16x32_bf16 v[180:183], v[200:203], v[124:127], v[180:183]\n"
    "v_lshlrev_b32_e32 v160, 16, v0\n"
    "v_and_b32_e32 v161, s47, v0\n"
    "v_mfma_f32_16x16x32_bf16 v[176:179], v[204:207], v[128:131], v[176:179]\n"
    "v_lshlrev_b32_e32 v162, 16, v1\n"
    "v_and_b32_e32 v163, s47, v1\n"
    "v_mfma_f32_16x16x32_bf16 v[180:183], v[204:207], v[132:135], v[180:183]\n"
    "v_lshlrev_b32_e32 v164, 16, v4\n"
    "v_and_b32_e32 v165, s47, v4\n"
    "s_waitcnt vmcnt(4) lgkmcnt(0)\n"
    "s_barrier\n"
    "ds_read_b64_tr_b16 v[192:193], v244 offset:24576\n"
    "ds_read_b64_tr_b16 v[194:195], v244 offset:26624\n"
    "v_mfma_f32_16x16x32_bf16 v[176:179], v[208:211], v[136:139], v[176:179]\n"
    "ds_read_b64_tr_b16 v[196:197], v244 offset:28672\n"
    "ds_read_b64_tr_b16 v[198:199], v244 offset:30720\n"
    "v_mfma_f32_16x16x32_bf16 v[180:183], v[208:211], v[140:143], v[180:183]\n"
    "ds_read_b64_tr_b16 v[200:201], v244 offset:32768\n"
    "ds_read_b64_tr_b16 v[202:203], v244 offset:34816\n"
    "v_mfma_f32_16x16x32_bf16 v[184:187], v[212:215], v[120:123], v[184:187]\n"
    "ds_read_b64_tr_b16 v[204:205], v245 offset:24576\n"
    "ds_read_b64_tr_b16 v[206:207], v245 offset:26624\n"
    "v_mfma_f32_16x16x32_bf16 v[188:191], v[212:215], v[124:127], v[188:191]\n"
    "ds_read_b64_tr_b16 v[208:209], v245 offset:28672\n"
    "ds_read_b64_tr_b16 v[210:211], v245 offset:30720\n"
    "v_mfma_f32_16x16x32_bf16 v[184:187], v[216:219], v[128:131], v[184:187]\n"
    "ds_read_b64_tr_b16 v[212:213], v245 offset:32768\n"
    "ds_read_b64_tr_b16 v[214:215], v245 offset:34816\n"
    "v_mfma_f32_16x16x32_bf16 v[188:191], v[216:219], v[132:135], v[188:191]\n"
    "ds_read_b64_tr_b16 v[216:217], v246 offset:24576\n"
    "ds_read_b64_tr_b16 v[218:219], v246 offset:26624\n"
    "v_lshlrev_b32_e32 v166, 16, v5\n"
    "v_and_b32_e32 v167, s47, v5\n"
    "v_mfma_f32_16x16x32_bf16 v[184:187], v[220:223], v[136:139], v[184:187]\n"
    "v_cvt_pk_bf16_f32 v226, v168, v169\n"
    "v_cvt_pk_bf16_f32 v227, v170, v171\n"
    "v_mfma_f32_16x16x32_bf16 v[188:191], v[220:223], v[140:143], v[188:191]\n"
    "ds_read_b64_tr_b16 v[220:221], v246 offset:28672\n"
    "s_waitcnt lgkmcnt(4)\n"
    "v_mfma_f32_16x16x32_bf16 v[160:163], v[192:195], v[96:99], v[160:163]\n"
    "ds_read_b64_tr_b16 v[222:223], v246 offset:30720\n"
    "v_cvt_pk_bf16_f32 v230, v172, v173\n"
    "v_mfma_f32_16x16x32_bf16 v[164:167], v[192:195], v[100:103], v[164:167]\n"
    "ds_read_b64_tr_b16 v[192:193], v246 offset:32768\n"
    "ds_read_b64_tr_b16 v[194:195], v246 offset:34816\n"
    "v_mfma_f32_16x16x32_bf16 v[160:163], v[196:199], v[104:107], v[160:163]\n"
    "v_cvt_pk_bf16_f32 v231, v174, v175\n"
    "s_add_u32 s42, s87, 0x1f800\n"
    "v_mfma_f32_16x16x32_bf16 v[164:167], v[196:199], v[108:111], v[164:167]\n"
    "ds_read_b64_tr_b16 v[196:197], v247 offset:24576\n"
    "ds_read_b64_tr_b16 v[198:199], v247 offset:26624\n"
    "s_cmp_eq_u32 s98, 2\n"
    "s_cselect_b32 s43, 2.0, s42\n"
    "buffer_store_dwordx4 v[228:231], v249, s[28:31], s43 offen sc1\n"
    "s_cselect_b64 exec, -1, 0\n"
    "ds_write_b128 v252, v[228:231] offset:37888\n"
    "s_mov_b64 exec, -1\n"
    "s_cmp_eq_u32 s98, 0\n"
    "s_cselect_b32 s43, 2.0, s42\n"
    "buffer_store_dwordx4 v[224:227], v248, s[28:31], s43 offen sc1\n"
    "s_cselect_b64 exec, -1, 0\n"
    "ds_write_b128 v252, v[224:227] offset:37888\n"
    "s_mov_b64 exec, -1\n"
    "v_lshlrev_b32_e32 v168, 16, v2\n"
    "v_and_b32_e32 v169, s47, v2\n"
    "v_lshlrev_b32_e32 v170, 16, v3\n"
    "v_and_b32_e32 v171, s47, v3\n"
    "v_lshlrev_b32_e32 v172, 16, v6\n"
    "v_and_b32_e32 v173, s47, v6\n"
    "v_lshlrev_b32_e32 v174, 16, v7\n"
    "v_and_b32_e64 v175, s47, v7\n"
    "v_mfma_f32_16x16x32_bf16 v[160:163], v[200:203], v[112:115], v[160:163]\n"
    "v_cvt_pk_bf16_f32 v232, v176, v177\n"
    "v_cvt_pk_bf16_f32 v233, v178, v179\n"
    "v_mfma_f32_16x16x32_bf16 v[164:167], v[200:203], v[116:119], v[164:167]\n"
    "ds_read_b64_tr_b16 v[200:201], v247 offset:28672\n"
    "ds_read_b64_tr_b16 v[202:203], v247 offset:30720\n"
    "v_mfma_f32_16x16x32_bf16 v[168:171], v[204:207], v[96:99], v[168:171]\n"
    "v_cvt_pk_bf16_f32 v236, v180, v181\n"
    "v_cvt_pk_bf16_f32 v237, v182, v183\n"
    "v_mfma_f32_16x16x32_bf16 v[172:175], v[204:207], v[100:103], v[172:175]\n"
    "ds_read_b64_tr_b16 v[204:205], v247 offset:32768\n"
    "ds_read_b64_tr_b16 v[206:207], v247 offset:34816\n"
    "v_mfma_f32_16x16x32_bf16 v[168:171], v[208:211], v[104:107], v[168:171]\n"
    "v_lshlrev_b32_e32 v176, 16, v8\n"
    "v_and_b32_e32 v177, s47, v8\n"
    "v_mfma_f32_16x16x32_bf16 v[172:175], v[208:211], v[108:111], v[172:175]\n"
    "v_lshlrev_b32_e32 v178, 16, v9\n"
    "v_and_b32_e32 v179, s47, v9\n"
    "v_lshlrev_b32_e32 v180, 16, v12\n"
    "v_and_b32_e32 v181, s47, v12\n"
    "v_lshlrev_b32_e32 v182, 16, v13\n"
    "v_and_b32_e32 v183, s47, v13\n"
    "s_waitcnt lgkmcnt(2)\n"
    "v_mfma_f32_16x16x32_bf16 v[168:171], v[212:215], v[112:115], v[168:171]\n"
    "v_cvt_pk_bf16_f32 v234, v184, v185\n"
    "v_cvt_pk_bf16_f32 v235, v186, v187\n"
    "v_mfma_f32_16x16x32_bf16 v[172:175], v[212:215], v[116:119], v[172:175]\n"
    "v_cvt_pk_bf16_f32 v238, v188, v189\n"
    "v_cvt_pk_bf16_f32 v239, v190, v191\n"
    "v_mfma_f32_16x16x32_bf16 v[176:179], v[216:219], v[96:99], v[176:179]\n"
    "s_cmp_eq_u32 s98, 3\n"
    "s_cselect_b32 s43, 2.0, s42\n"
    "buffer_store_dwordx4 v[236:239], v249, s[28:31], s43 offen offset:1024 sc1\n"
    "s_cselect_b64 exec, -1, 0\n"
    "ds_write_b128 v252, v[236:239] offset:37888\n"
    "s_mov_b64 exec, -1\n"
    "v_mfma_f32_16x16x32_bf16 v[180:183], v[216:219], v[100:103], v[180:183]\n"
    "s_cmp_eq_u32 s98, 1\n"
    "s_cselect_b32 s43, 2.0, s42\n"
    "buffer_store_dwordx4 v[232:235], v248, s[28:31], s43 offen offset:1024 sc1\n"
    "s_cselect_b64 exec, -1, 0\n"
    "ds_write_b128 v252, v[232:235] offset:37888\n"
    "s_mov_b64 exec, -1\n"
    "v_mfma_f32_16x16x32_bf16 v[176:179], v[220:223], v[104:107], v[176:179]\n"
    "v_lshlrev_b32_e32 v184, 16, v10\n"
    "v_and_b32_e64 v185, s47, v10\n"
    "v_mfma_f32_16x16x32_bf16 v[180:183], v[220:223], v[108:111], v[180:183]\n"
    "v_lshlrev_b32_e32 v186, 16, v11\n"
    "v_and_b32_e32 v187, s47, v11\n"
    "s_waitcnt vmcnt(5) lgkmcnt(0)\n"
    "s_barrier\n"
    "ds_read_b64_tr_b16 v[208:209], v244 offset:36864\n"
    "ds_read_b64_tr_b16 v[210:211], v244 offset:38912\n"
    "v_lshlrev_b32_e32 v188, 16, v14\n"
    "v_and_b32_e32 v189, s47, v14\n"
    "v_lshlrev_b32_e32 v190, 16, v15\n"
    "v_and_b32_e32 v191, s47, v15\n"
    "v_mfma_f32_16x16x32_bf16 v[176:179], v[192:195], v[112:115], v[176:179]\n"
    "ds_read_b64_tr_b16 v[212:213], v244 offset:40960\n"
    "ds_read_b64_tr_b16 v[214:215], v244 offset:43008\n"
    "v_mfma_f32_16x16x32_bf16 v[180:183], v[192:195], v[116:119], v[180:183]\n"
    "ds_read_b64_tr_b16 v[216:217], v244 offset:45056\n"
    "ds_read_b64_tr_b16 v[218:219], v244 offset:47104\n"
    "v_mfma_f32_16x16x32_bf16 v[184:187], v[196:199], v[96:99], v[184:187]\n"
    "ds_read_b64_tr_b16 v[220:221], v245 offset:36864\n"
    "ds_read_b64_tr_b16 v[222:223], v245 offset:38912\n"
    "v_mfma_f32_16x16x32_bf16 v[188:191], v[196:199], v[100:103], v[188:191]\n"
    "ds_read_b64_tr_b16 v[192:193], v245 offset:40960\n"
    "ds_read_b64_tr_b16 v[194:195], v245 offset:43008\n"
    "v_mfma_f32_16x16x32_bf16 v[184:187], v[200:203], v[104:107], v[184:187]\n"
    "ds_read_b64_tr_b16 v[196:197], v245 offset:45056\n"
    "ds_read_b64_tr_b16 v[198:199], v245 offset:47104\n"
    "v_mfma_f32_16x16x32_bf16 v[188:191], v[200:203], v[108:111], v[188:191]\n"
    "ds_read_b64_tr_b16 v[200:201], v246 offset:36864\n"
    "ds_read_b64_tr_b16 v[202:203], v246 offset:38912\n"
    "v_mfma_f32_16x16x32_bf16 v[184:187], v[204:207], v[112:115], v[184:187]\n"
    "v_mfma_f32_16x16x32_bf16 v[188:191], v[204:207], v[116:119], v[188:191]\n"
    "ds_read_b64_tr_b16 v[204:205], v246 offset:40960\n"
    "s_waitcnt lgkmcnt(5)\n"
    "v_mfma_f32_16x16x32_bf16 v[160:163], v[208:211], v[120:123], v[160:163]\n"
    "ds_read_b64_tr_b16 v[206:207], v246 offset:43008\n"
    "v_mfma_f32_16x16x32_bf16 v[164:167], v[208:211], v[124:127], v[164:167]\n"
    "ds_read_b64_tr_b16 v[208:209], v246 offset:45056\n"
    "ds_read_b64_tr_b16 v[210:211], v246 offset:47104\n"
    "v_mfma_f32_16x16x32_bf16 v[160:163], v[212:215], v[128:131], v[160:163]\n"
    "v_mfma_f32_16x16x32_bf16 v[164:167], v[212:215], v[132:135], v[164:167]\n"
    "ds_read_b64_tr_b16 v[212:213], v247 offset:36864\n"
    "ds_read_b64_tr_b16 v[214:215], v247 offset:38912\n"
    "v_mfma_f32_16x16x32_bf16 v[160:163], v[216:219], v[136:139], v[160:163]\n"
    "v_mfma_f32_16x16x32_bf16 v[164:167], v[216:219], v[140:143], v[164:167]\n"
    "ds_read_b64_tr_b16 v[216:217], v247 offset:40960\n"
    "ds_read_b64_tr_b16 v[218:219], v247 offset:43008\n"
    "v_mfma_f32_16x16x32_bf16 v[168:171], v[220:223], v[120:123], v[168:171]\n"
    "v_mfma_f32_16x16x32_bf16 v[172:175], v[220:223], v[124:127], v[172:175]\n"
    "ds_read_b64_tr_b16 v[220:221], v247 offset:45056\n"
    "ds_read_b64_tr_b16 v[222:223], v247 offset:47104\n"
    "v_mfma_f32_16x16x32_bf16 v[168:171], v[192:195], v[128:131], v[168:171]\n"
    "v_mfma_f32_16x16x32_bf16 v[172:175], v[192:195], v[132:135], v[172:175]\n"
    "v_cvt_pk_bf16_f32 v0, v160, v161\n"
    "s_waitcnt lgkmcnt(3)\n"
    "v_mfma_f32_16x16x32_bf16 v[168:171], v[196:199], v[136:139], v[168:171]\n"
    "v_cvt_pk_bf16_f32 v1, v162, v163\n"
    "v_cvt_pk_bf16_f32 v4, v164, v165\n"
    "v_mfma_f32_16x16x32_bf16 v[172:175], v[196:199], v[140:143], v[172:175]\n"
    "v_cvt_pk_bf16_f32 v5, v166, v167\n"
    "v_lshlrev_b32_e64 v160, 16, v16\n"
    "v_mfma_f32_16x16x32_bf16 v[176:179], v[200:203], v[120:123], v[176:179]\n"
    "v_and_b32_e32 v161, s47, v16\n"
    "v_lshlrev_b32_e32 v162, 16, v17\n"
    "v_mfma_f32_16x16x32_bf16 v[180:183], v[200:203], v[124:127], v[180:183]\n"
    "v_and_b32_e32 v163, s47, v17\n"
    "v_lshlrev_b32_e32 v164, 16, v20\n"
    "v_mfma_f32_16x16x32_bf16 v[176:179], v[204:207], v[128:131], v[176:179]\n"
    "v_and_b32_e32 v165, s47, v20\n"
    "v_lshlrev_b32_e32 v166, 16, v21\n"
    "v_mfma_f32_16x16x32_bf16 v[180:183], v[204:207], v[132:135], v[180:183]\n"
    "v_and_b32_e64 v167, s47, v21\n"
    "v_cvt_pk_bf16_f32 v2, v168, v169\n"
    "s_waitcnt lgkmcnt(0)\n"
    "s_barrier\n"
    "ds_read_b64_tr_b16 v[192:193], v244 offset:49152\n"
    "ds_read_b64_tr_b16 v[194:195], v244 offset:51200\n"
    "v_mfma_f32_16x16x32_bf16 v[176:179], v[208:211], v[136:139], v[176:179]\n"
    "ds_read_b64_tr_b16 v[196:197], v244 offset:53248\n"
    "ds_read_b64_tr_b16 v[198:199], v244 offset:55296\n"
    "v_mfma_f32_16x16x32_bf16 v[180:183], v[208:211], v[140:143], v[180:183]\n"
    "ds_read_b64_tr_b16 v[200:201], v244 offset:57344\n"
    "ds_read_b64_tr_b16 v[202:203], v244 offset:59392\n"
    "v_mfma_f32_16x16x32_bf16 v[184:187], v[212:215], v[120:123], v[184:187]\n"
    "ds_read_b64_tr_b16 v[204:205], v245 offset:49152\n"
    "ds_read_b64_tr_b16 v[206:207], v245 offset:51200\n"
    "v_mfma_f32_16x16x32_bf16 v[188:191], v[212:215], v[124:127], v[188:191]\n"
    "ds_read_b64_tr_b16 v[208:209], v245 offset:53248\n"
    "ds_read_b64_tr_b16 v[210:211], v245 offset:55296\n"
    "v_mfma_f32_16x16x32_bf16 v[184:187], v[216:219], v[128:131], v[184:187]\n"
    "ds_read_b64_tr_b16 v[212:213], v245 offset:57344\n"
    "ds_read_b64_tr_b16 v[214:215], v245 offset:59392\n"
    "v_mfma_f32_16x16x32_bf16 v[188:191], v[216:219], v[132:135], v[188:191]\n"
    "ds_read_b64_tr_b16 v[216:217], v246 offset:49152\n"
    "ds_read_b64_tr_b16 v[218:219], v246 offset:51200\n"
    "v_mfma_f32_16x16x32_bf16 v[184:187], v[220:223], v[136:139], v[184:187]\n"
    "v_cvt_pk_bf16_f32 v3, v170, v171\n"
    "v_cvt_pk_bf16_f32 v6, v172, v173\n"
    "v_mfma_f32_16x16x32_bf16 v[188:191], v[220:223], v[140:143], v[188:191]\n"
    "ds_read_b64_tr_b16 v[220:221], v246 offset:53248\n"
    "s_waitcnt lgkmcnt(5)\n"
    "v_mfma_f32_16x16x32_bf16 v[160:163], v[192:195], v[96:99], v[160:163]\n"
    "ds_read_b64_tr_b16 v[222:223], v246 offset:55296\n"
    "v_cvt_pk_bf16_f32 v7, v174, v175\n"
    "v_mfma_f32_16x16x32_bf16 v[164:167], v[192:195], v[100:103], v[164:167]\n"
    "ds_read_b64_tr_b16 v[192:193], v246 offset:57344\n"
    "ds_read_b64_tr_b16 v[194:195], v246 offset:59392\n"
    "v_mfma_f32_16x16x32_bf16 v[160:163], v[196:199], v[104:107], v[160:163]\n"
    "s_add_u32 s42, s87, 0x7000\n"
    "v_mfma_f32_16x16x32_bf16 v[164:167], v[196:199], v[108:111], v[164:167]\n"
    "ds_read_b64_tr_b16 v[196:197], v247 offset:49152\n"
    "ds_read_b64_tr_b16 v[198:199], v247 offset:51200\n"
    "s_cmp_eq_u32 s98, 2\n"
    "s_cselect_b32 s43, 2.0, s42\n"
    "buffer_store_dwordx4 v[4:7], v249, s[28:31], s43 offen sc1\n"
    "s_cselect_b64 exec, -1, 0\n"
    "ds_write_b128 v252, v[4:7] offset:38912\n"
    "s_mov_b64 exec, -1\n"
    "s_cmp_eq_u32 s98, 0\n"
    "s_cselect_b32 s43, 2.0, s42\n"
    "buffer_store_dwordx4 v[0:3], v248, s[28:31], s43 offen sc1\n"
    "s_cselect_b64 exec, -1, 0\n"
    "ds_write_b128 v252, v[0:3] offset:38912\n"
    "s_mov_b64 exec, -1\n"
    "v_lshlrev_b32_e32 v168, 16, v18\n"
    "v_and_b32_e32 v169, s47, v18\n"
    "v_lshlrev_b32_e32 v170, 16, v19\n"
    "v_and_b32_e32 v171, s47, v19\n"
    "v_lshlrev_b32_e32 v172, 16, v22\n"
    "v_and_b32_e32 v173, s47, v22\n"
    "v_lshlrev_b32_e32 v174, 16, v23\n"
    "v_and_b32_e64 v175, s47, v23\n"
    "v_mfma_f32_16x16x32_bf16 v[160:163], v[200:203], v[112:115], v[160:163]\n"
    "v_cvt_pk_bf16_f32 v8, v176, v177\n"
    "v_cvt_pk_bf16_f32 v9, v178, v179\n"
    "v_mfma_f32_16x16x32_bf16 v[164:167], v[200:203], v[116:119], v[164:167]\n"
    "ds_read_b64_tr_b16 v[200:201], v247 offset:53248\n"
    "ds_read_b64_tr_b16 v[202:203], v247 offset:55296\n"
    "v_mfma_f32_16x16x32_bf16 v[168:171], v[204:207], v[96:99], v[168:171]\n"
    "v_cvt_pk_bf16_f32 v12, v180, v181\n"
    "v_cvt_pk_bf16_f32 v13, v182, v183\n"
    "v_mfma_f32_16x16x32_bf16 v[172:175], v[204:207], v[100:103], v[172:175]\n"
    "ds_read_b64_tr_b16 v[204:205], v247 offset:57344\n"
    "ds_read_b64_tr_b16 v[206:207], v247 offset:59392\n"
    "v_mfma_f32_16x16x32_bf16 v[168:171], v[208:211], v[104:107], v[168:171]\n"
    "v_lshlrev_b32_e32 v176, 16, v24\n"
    "v_and_b32_e32 v177, s47, v24\n"
    "v_mfma_f32_16x16x32_bf16 v[172:175], v[208:211], v[108:111], v[172:175]\n"
    "v_lshlrev_b32_e32 v178, 16, v25\n"
    "v_and_b32_e32 v179, s47, v25\n"
    "v_lshlrev_b32_e32 v180, 16, v28\n"
    "v_and_b32_e32 v181, s47, v28\n"
    "v_lshlrev_b32_e32 v182, 16, v29\n"
    "v_and_b32_e32 v183, s47, v29\n"
    "s_waitcnt lgkmcnt(2)\n"
    "v_mfma_f32_16x16x32_bf16 v[168:171], v[212:215], v[112:115], v[168:171]\n"
    "s_waitcnt vmcnt(2)\n"
    "v_and_b32_e32 v240, 0xffffff00, v240\n"
    "v_add_u32_e32 v240, 1, v240\n"
    "s_mov_b64 exec, s[74:75]\n"
    "buffer_atomic_swap v240, v242, s[28:31], s94 offen\n"
    "s_mov_b64 exec, -1\n"
    "v_mfma_f32_16x16x32_bf16 v[172:175], v[212:215], v[116:119], v[172:175]\n"
    "v_cvt_pk_bf16_f32 v10, v184, v185\n"
    "v_cvt_pk_bf16_f32 v11, v186, v187\n"
    "v_mfma_f32_16x16x32_bf16 v[176:179], v[216:219], v[96:99], v[176:179]\n"
    "v_cvt_pk_bf16_f32 v14, v188, v189\n"
    "v_cvt_pk_bf16_f32 v15, v190, v191\n"
    "v_mfma_f32_16x16x32_bf16 v[180:183], v[216:219], v[100:103], v[180:183]\n"
    "s_cmp_eq_u32 s98, 3\n"
    "s_cselect_b32 s43, 2.0, s42\n"
    "buffer_store_dwordx4 v[12:15], v249, s[28:31], s43 offen offset:1024 sc1\n"
    "s_cselect_b64 exec, -1, 0\n"
    "ds_write_b128 v252, v[12:15] offset:38912\n"
    "s_mov_b64 exec, -1\n"
    "v_mfma_f32_16x16x32_bf16 v[176:179], v[220:223], v[104:107], v[176:179]\n"
    "s_cmp_eq_u32 s98, 1\n"
    "s_cselect_b32 s43, 2.0, s42\n"
    "buffer_store_dwordx4 v[8:11], v248, s[28:31], s43 offen offset:1024 sc1\n"
    "s_cselect_b64 exec, -1, 0\n"
    "ds_write_b128 v252, v[8:11] offset:38912\n"
    "s_mov_b64 exec, -1\n"
    "v_mfma_f32_16x16x32_bf16 v[180:183], v[220:223], v[108:111], v[180:183]\n"
    "v_lshlrev_b32_e32 v184, 16, v26\n"
    "v_and_b32_e32 v185, s47, v26\n"
    "s_waitcnt lgkmcnt(0)\n"
    "s_barrier\n"
    "v_add_u32_e32 v244, 0xffffa000, v244\n"
    "ds_read_b64_tr_b16 v[208:209], v244\n"
    "v_lshlrev_b32_e32 v186, 16, v27\n"
    "v_and_b32_e32 v187, s47, v27\n"
    "v_lshlrev_b32_e32 v188, 16, v30\n"
    "v_and_b32_e32 v189, s47, v30\n"
    "v_lshlrev_b32_e32 v190, 16, v31\n"
    "v_and_b32_e64 v191, s47, v31\n"
    "v_mfma_f32_16x16x32_bf16 v[176:179], v[192:195], v[112:115], v[176:179]\n"
    "ds_read_b64_tr_b16 v[210:211], v244 offset:2048\n"
    "ds_read_b64_tr_b16 v[212:213], v244 offset:4096\n"
    "v_mfma_f32_16x16x32_bf16 v[180:183], v[192:195], v[116:119], v[180:183]\n"
    "ds_read_b64_tr_b16 v[214:215], v244 offset:6144\n"
    "ds_read_b64_tr_b16 v[216:217], v244 offset:8192\n"
    "v_mfma_f32_16x16x32_bf16 v[184:187], v[196:199], v[96:99], v[184:187]\n"
    "ds_read_b64_tr_b16 v[218:219], v244 offset:10240\n"
    "v_add_u32_e32 v245, 0xffffa000, v245\n"
    "v_mfma_f32_16x16x32_bf16 v[188:191], v[196:199], v[100:103], v[188:191]\n"
    "ds_read_b64_tr_b16 v[220:221], v245\n"
    "ds_read_b64_tr_b16 v[222:223], v245 offset:2048\n"
    "v_mfma_f32_16x16x32_bf16 v[184:187], v[200:203], v[104:107], v[184:187]\n"
    "ds_read_b64_tr_b16 v[192:193], v245 offset:4096\n"
    "ds_read_b64_tr_b16 v[194:195], v245 offset:6144\n"
    "v_mfma_f32_16x16x32_bf16 v[188:191], v[200:203], v[108:111], v[188:191]\n"
    "ds_read_b64_tr_b16 v[196:197], v245 offset:8192\n"
    "ds_read_b64_tr_b16 v[198:199], v245 offset:10240\n"
    "v_mfma_f32_16x16x32_bf16 v[184:187], v[204:207], v[112:115], v[184:187]\n"
    "v_add_u32_e32 v246, 0xffffa000, v246\n"
    "ds_read_b64_tr_b16 v[200:201], v246\n"
    "v_mfma_f32_16x16x32_bf16 v[188:191], v[204:207], v[116:119], v[188:191]\n"
    "ds_read_b64_tr_b16 v[202:203], v246 offset:2048\n"
    "s_waitcnt lgkmcnt(6)\n"
    "v_mfma_f32_16x16x32_bf16 v[160:163], v[208:211], v[120:123], v[160:163]\n"
    "ds_read_b64_tr_b16 v[204:205], v246 offset:4096\n"
    "ds_read_b64_tr_b16 v[206:207], v246 offset:6144\n"
    "v_mfma_f32_16x16x32_bf16 v[164:167], v[208:211], v[124:127], v[164:167]\n"
    "ds_read_b64_tr_b16 v[208:209], v246 offset:8192\n"
    "ds_read_b64_tr_b16 v[210:211], v246 offset:10240\n"
    "v_mfma_f32_16x16x32_bf16 v[160:163], v[212:215], v[128:131], v[160:163]\n"
    "v_add_u32_e32 v247, 0xffffa000, v247\n"
    "v_mfma_f32_16x16x32_bf16 v[164:167], v[212:215], v[132:135], v[164:167]\n"
    "ds_read_b64_tr_b16 v[212:213], v247\n"
    "ds_read_b64_tr_b16 v[214:215], v247 offset:2048\n"
    "v_mfma_f32_16x16x32_bf16 v[160:163], v[216:219], v[136:139], v[160:163]\n"
    "v_mfma_f32_16x16x32_bf16 v[164:167], v[216:219], v[140:143], v[164:167]\n"
    "ds_read_b64_tr_b16 v[216:217], v247 offset:4096\n"
    "ds_read_b64_tr_b16 v[218:219], v247 offset:6144\n"
    "v_mfma_f32_16x16x32_bf16 v[168:171], v[220:223], v[120:123], v[168:171]\n"
    "v_mfma_f32_16x16x32_bf16 v[172:175], v[220:223], v[124:127], v[172:175]\n"
    "ds_read_b64_tr_b16 v[220:221], v247 offset:8192\n"
    "s_waitcnt lgkmcnt(4)\n"
    "v_mfma_f32_16x16x32_bf16 v[168:171], v[192:195], v[128:131], v[168:171]\n"
    "ds_read_b64_tr_b16 v[222:223], v247 offset:10240\n"
    "v_mfma_f32_16x16x32_bf16 v[172:175], v[192:195], v[132:135], v[172:175]\n"
    "v_cvt_pk_bf16_f32 v16, v160, v161\n"
    "v_cvt_pk_bf16_f32 v17, v162, v163\n"
    "v_mfma_f32_16x16x32_bf16 v[168:171], v[196:199], v[136:139], v[168:171]\n"
    "v_cvt_pk_bf16_f32 v20, v164, v165\n"
    "v_cvt_pk_bf16_f32 v21, v166, v167\n"
    "v_mfma_f32_16x16x32_bf16 v[172:175], v[196:199], v[140:143], v[172:175]\n"
    "v_lshlrev_b32_e32 v160, 16, v32\n"
    "v_and_b32_e32 v161, s47, v32\n"
    "v_mfma_f32_16x16x32_bf16 v[176:179], v[200:203], v[120:123], v[176:179]\n"
    "v_lshlrev_b32_e32 v162, 16, v33\n"
    "v_and_b32_e32 v163, s47, v33\n"
    "v_mfma_f32_16x16x32_bf16 v[180:183], v[200:203], v[124:127], v[180:183]\n"
    "v_lshlrev_b32_e32 v164, 16, v36\n"
    "v_and_b32_e32 v165, s47, v36\n"
    "v_mfma_f32_16x16x32_bf16 v[176:179], v[204:207], v[128:131], v[176:179]\n"
    "v_lshlrev_b32_e32 v166, 16, v37\n"
    "v_and_b32_e32 v167, s47, v37\n"
    "v_mfma_f32_16x16x32_bf16 v[180:183], v[204:207], v[132:135], v[180:183]\n"
    "v_cvt_pk_bf16_f32 v18, v168, v169\n"
    "v_cvt_pk_bf16_f32 v19, v170, v171\n"
    "s_waitcnt lgkmcnt(0)\n"
    "s_barrier\n"
    "ds_read_b64_tr_b16 v[192:193], v244 offset:12288\n"
    "ds_read_b64_tr_b16 v[194:195], v244 offset:14336\n"
    "v_mfma_f32_16x16x32_bf16 v[176:179], v[208:211], v[136:139], v[176:179]\n"
    "ds_read_b64_tr_b16 v[196:197], v244 offset:16384\n"
    "ds_read_b64_tr_b16 v[198:199], v244 offset:18432\n"
    "v_mfma_f32_16x16x32_bf16 v[180:183], v[208:211], v[140:143], v[180:183]\n"
    "ds_read_b64_tr_b16 v[200:201], v244 offset:20480\n"
    "ds_read_b64_tr_b16 v[202:203], v244 offset:22528\n"
    "v_mfma_f32_16x16x32_bf16 v[184:187], v[212:215], v[120:123], v[184:187]\n"
    "ds_read_b64_tr_b16 v[204:205], v245 offset:12288\n"
    "ds_read_b64_tr_b16 v[206:207], v245 offset:14336\n"
    "v_mfma_f32_16x16x32_bf16 v[188:191], v[212:215], v[124:127], v[188:191]\n"
    "ds_read_b64_tr_b16 v[208:209], v245 offset:16384\n"
    "ds_read_b64_tr_b16 v[210:211], v245 offset:18432\n"
    "v_mfma_f32_16x16x32_bf16 v[184:187], v[216:219], v[128:131], v[184:187]\n"
    "ds_read_b64_tr_b16 v[212:213], v245 offset:20480\n"
    "ds_read_b64_tr_b16 v[214:215], v245 offset:22528\n"
    "v_mfma_f32_16x16x32_bf16 v[188:191], v[216:219], v[132:135], v[188:191]\n"
    "ds_read_b64_tr_b16 v[216:217], v246 offset:12288\n"
    "ds_read_b64_tr_b16 v[218:219], v246 offset:14336\n"
    "v_mfma_f32_16x16x32_bf16 v[184:187], v[220:223], v[136:139], v[184:187]\n"
    "v_cvt_pk_bf16_f32 v22, v172, v173\n"
    "v_cvt_pk_bf16_f32 v23, v174, v175\n"
    "v_mfma_f32_16x16x32_bf16 v[188:191], v[220:223], v[140:143], v[188:191]\n"
    "ds_read_b64_tr_b16 v[220:221], v246 offset:16384\n"
    "s_waitcnt lgkmcnt(5)\n"
    "v_mfma_f32_16x16x32_bf16 v[160:163], v[192:195], v[96:99], v[160:163]\n"
    "ds_read_b64_tr_b16 v[222:223], v246 offset:18432\n"
    "s_add_u32 s42, s87, 0xa800\n"
    "v_mfma_f32_16x16x32_bf16 v[164:167], v[192:195], v[100:103], v[164:167]\n"
    "ds_read_b64_tr_b16 v[192:193], v246 offset:20480\n"
    "ds_read_b64_tr_b16 v[194:195], v246 offset:22528\n"
    "v_mfma_f32_16x16x32_bf16 v[160:163], v[196:199], v[104:107], v[160:163]\n"
    "s_cmp_eq_u32 s98, 2\n"
    "s_cselect_b32 s43, 2.0, s42\n"
    "buffer_store_dwordx4 v[20:23], v249, s[28:31], s43 offen sc1\n"
    "s_cselect_b64 exec, -1, 0\n"
    "ds_write_b128 v252, v[20:23] offset:39936\n"
    "s_mov_b64 exec, -1\n"
    "v_mfma_f32_16x16x32_bf16 v[164:167], v[196:199], v[108:111], v[164:167]\n"
    "ds_read_b64_tr_b16 v[196:197], v247 offset:12288\n"
    "ds_read_b64_tr_b16 v[198:199], v247 offset:14336\n"
    "s_cmp_eq_u32 s98, 0\n"
    "s_cselect_b32 s43, 2.0, s42\n"
    "buffer_store_dwordx4 v[16:19], v248, s[28:31], s43 offen sc1\n"
    "s_cselect_b64 exec, -1, 0\n"
    "ds_write_b128 v252, v[16:19] offset:39936\n"
    "s_mov_b64 exec, -1\n"
    "v_lshlrev_b32_e32 v168, 16, v34\n"
    "v_and_b32_e32 v169, s47, v34\n"
    "v_lshlrev_b32_e32 v170, 16, v35\n"
    "v_and_b32_e32 v171, s47, v35\n"
    "v_lshlrev_b32_e32 v172, 16, v38\n"
    "v_and_b32_e32 v173, s47, v38\n"
    "v_lshlrev_b32_e32 v174, 16, v39\n"
    "v_and_b32_e64 v175, s47, v39\n"
    "v_mfma_f32_16x16x32_bf16 v[160:163], v[200:203], v[112:115], v[160:163]\n"
    "v_cvt_pk_bf16_f32 v24, v176, v177\n"
    "v_cvt_pk_bf16_f32 v25, v178, v179\n"
    "v_mfma_f32_16x16x32_bf16 v[164:167], v[200:203], v[116:119], v[164:167]\n"
    "ds_read_b64_tr_b16 v[200:201], v247 offset:16384\n"
    "ds_read_b64_tr_b16 v[202:203], v247 offset:18432\n"
    "v_mfma_f32_16x16x32_bf16 v[168:171], v[204:207], v[96:99], v[168:171]\n"
    "v_cvt_pk_bf16_f32 v28, v180, v181\n"
    "v_cvt_pk_bf16_f32 v29, v182, v183\n"
    "v_mfma_f32_16x16x32_bf16 v[172:175], v[204:207], v[100:103], v[172:175]\n"
    "ds_read_b64_tr_b16 v[204:205], v247 offset:20480\n"
    "ds_read_b64_tr_b16 v[206:207], v247 offset:22528\n"
    "v_mfma_f32_16x16x32_bf16 v[168:171], v[208:211], v[104:107], v[168:171]\n"
    "v_lshlrev_b32_e32 v176, 16, v40\n"
    "v_and_b32_e32 v177, s47, v40\n"
    "v_mfma_f32_16x16x32_bf16 v[172:175], v[208:211], v[108:111], v[172:175]\n"
    "v_lshlrev_b32_e32 v178, 16, v41\n"
    "v_and_b32_e32 v179, s47, v41\n"
    "v_lshlrev_b32_e32 v180, 16, v44\n"
    "v_and_b32_e32 v181, s47, v44\n"
    "v_lshlrev_b32_e32 v182, 16, v45\n"
    "v_and_b32_e32 v183, s47, v45\n"
    "s_waitcnt lgkmcnt(2)\n"
    "v_mfma_f32_16x16x32_bf16 v[168:171], v[212:215], v[112:115], v[168:171]\n"
    "s_waitcnt vmcnt(2)\n"
    "v_add_u32_e32 v240, 1, v240\n"
    "s_mov_b64 exec, s[74:75]\n"
    "buffer_atomic_swap v240, v242, s[28:31], s94 offen\n"
    "s_mov_b64 exec, -1\n"
    "v_mfma_f32_16x16x32_bf16 v[172:175], v[212:215], v[116:119], v[172:175]\n"
    "v_cvt_pk_bf16_f32 v26, v184, v185\n"
    "v_cvt_pk_bf16_f32 v27, v186, v187\n"
    "v_mfma_f32_16x16x32_bf16 v[176:179], v[216:219], v[96:99], v[176:179]\n"
    "v_cvt_pk_bf16_f32 v30, v188, v189\n"
    "v_cvt_pk_bf16_f32 v31, v190, v191\n"
    "v_mfma_f32_16x16x32_bf16 v[180:183], v[216:219], v[100:103], v[180:183]\n"
    "s_cmp_eq_u32 s98, 3\n"
    "s_cselect_b32 s43, 2.0, s42\n"
    "buffer_store_dwordx4 v[28:31], v249, s[28:31], s43 offen offset:1024 sc1\n"
    "s_cselect_b64 exec, -1, 0\n"
    "ds_write_b128 v252, v[28:31] offset:39936\n"
    "s_mov_b64 exec, -1\n"
    "v_mfma_f32_16x16x32_bf16 v[176:179], v[220:223], v[104:107], v[176:179]\n"
    "s_cmp_eq_u32 s98, 1\n"
    "s_cselect_b32 s43, 2.0, s42\n"
    "buffer_store_dwordx4 v[24:27], v248, s[28:31], s43 offen offset:1024 sc1\n"
    "s_cselect_b64 exec, -1, 0\n"
    "ds_write_b128 v252, v[24:27] offset:39936\n"
    "s_mov_b64 exec, -1\n"
    "v_mfma_f32_16x16x32_bf16 v[180:183], v[220:223], v[108:111], v[180:183]\n"
    "v_lshlrev_b32_e32 v184, 16, v42\n"
    "v_and_b32_e32 v185, s47, v42\n"
    "s_waitcnt lgkmcnt(0)\n"
    "s_barrier\n"
    "v_add_u32_e32 v244, 0xffff4000, v244\n"
    "ds_read_b64_tr_b16 v[208:209], v244\n"
    "v_lshlrev_b32_e32 v186, 16, v43\n"
    "v_and_b32_e32 v187, s47, v43\n"
    "v_lshlrev_b32_e32 v188, 16, v46\n"
    "v_and_b32_e32 v189, s47, v46\n"
    "v_lshlrev_b32_e32 v190, 16, v47\n"
    "v_and_b32_e64 v191, s47, v47\n"
    "v_mfma_f32_16x16x32_bf16 v[176:179], v[192:195], v[112:115], v[176:179]\n"
    "ds_read_b64_tr_b16 v[210:211], v244 offset:2048\n"
    "ds_read_b64_tr_b16 v[212:213], v244 offset:4096\n"
    "v_mfma_f32_16x16x32_bf16 v[180:183], v[192:195], v[116:119], v[180:183]\n"
    "ds_read_b64_tr_b16 v[214:215], v244 offset:6144\n"
    "ds_read_b64_tr_b16 v[216:217], v244 offset:8192\n"
    "v_mfma_f32_16x16x32_bf16 v[184:187], v[196:199], v[96:99], v[184:187]\n"
    "ds_read_b64_tr_b16 v[218:219], v244 offset:10240\n"
    "v_add_u32_e32 v245, 0xffff4000, v245\n"
    "v_mfma_f32_16x16x32_bf16 v[188:191], v[196:199], v[100:103], v[188:191]\n"
    "ds_read_b64_tr_b16 v[220:221], v245\n"
    "ds_read_b64_tr_b16 v[222:223], v245 offset:2048\n"
    "v_mfma_f32_16x16x32_bf16 v[184:187], v[200:203], v[104:107], v[184:187]\n"
    "ds_read_b64_tr_b16 v[192:193], v245 offset:4096\n"
    "ds_read_b64_tr_b16 v[194:195], v245 offset:6144\n"
    "v_mfma_f32_16x16x32_bf16 v[188:191], v[200:203], v[108:111], v[188:191]\n"
    "ds_read_b64_tr_b16 v[196:197], v245 offset:8192\n"
    "ds_read_b64_tr_b16 v[198:199], v245 offset:10240\n"
    "v_mfma_f32_16x16x32_bf16 v[184:187], v[204:207], v[112:115], v[184:187]\n"
    "v_add_u32_e32 v246, 0xffff4000, v246\n"
    "ds_read_b64_tr_b16 v[200:201], v246\n"
    "v_mfma_f32_16x16x32_bf16 v[188:191], v[204:207], v[116:119], v[188:191]\n"
    "ds_read_b64_tr_b16 v[202:203], v246 offset:2048\n"
    "s_waitcnt lgkmcnt(6)\n"
    "v_mfma_f32_16x16x32_bf16 v[160:163], v[208:211], v[120:123], v[160:163]\n"
    "ds_read_b64_tr_b16 v[204:205], v246 offset:4096\n"
    "ds_read_b64_tr_b16 v[206:207], v246 offset:6144\n"
    "v_mfma_f32_16x16x32_bf16 v[164:167], v[208:211], v[124:127], v[164:167]\n"
    "ds_read_b64_tr_b16 v[208:209], v246 offset:8192\n"
    "ds_read_b64_tr_b16 v[210:211], v246 offset:10240\n"
    "v_mfma_f32_16x16x32_bf16 v[160:163], v[212:215], v[128:131], v[160:163]\n"
    "v_add_u32_e32 v247, 0xffff4000, v247\n"
    "v_mfma_f32_16x16x32_bf16 v[164:167], v[212:215], v[132:135], v[164:167]\n"
    "ds_read_b64_tr_b16 v[212:213], v247\n"
    "ds_read_b64_tr_b16 v[214:215], v247 offset:2048\n"
    "v_mfma_f32_16x16x32_bf16 v[160:163], v[216:219], v[136:139], v[160:163]\n"
    "v_mfma_f32_16x16x32_bf16 v[164:167], v[216:219], v[140:143], v[164:167]\n"
    "ds_read_b64_tr_b16 v[216:217], v247 offset:4096\n"
    "ds_read_b64_tr_b16 v[218:219], v247 offset:6144\n"
    "v_mfma_f32_16x16x32_bf16 v[168:171], v[220:223], v[120:123], v[168:171]\n"
    "v_mfma_f32_16x16x32_bf16 v[172:175], v[220:223], v[124:127], v[172:175]\n"
    "ds_read_b64_tr_b16 v[220:221], v247 offset:8192\n"
    "s_waitcnt lgkmcnt(4)\n"
    "v_mfma_f32_16x16x32_bf16 v[168:171], v[192:195], v[128:131], v[168:171]\n"
    "ds_read_b64_tr_b16 v[222:223], v247 offset:10240\n"
    "v_mfma_f32_16x16x32_bf16 v[172:175], v[192:195], v[132:135], v[172:175]\n"
    "v_cvt_pk_bf16_f32 v32, v160, v161\n"
    "v_cvt_pk_bf16_f32 v33, v162, v163\n"
    "v_mfma_f32_16x16x32_bf16 v[168:171], v[196:199], v[136:139], v[168:171]\n"
    "v_cvt_pk_bf16_f32 v36, v164, v165\n"
    "v_cvt_pk_bf16_f32 v37, v166, v167\n"
    "v_mfma_f32_16x16x32_bf16 v[172:175], v[196:199], v[140:143], v[172:175]\n"
    "v_lshlrev_b32_e32 v160, 16, v48\n"
    "v_and_b32_e32 v161, s47, v48\n"
    "v_mfma_f32_16x16x32_bf16 v[176:179], v[200:203], v[120:123], v[176:179]\n"
    "v_lshlrev_b32_e32 v162, 16, v49\n"
    "v_and_b32_e32 v163, s47, v49\n"
    "v_mfma_f32_16x16x32_bf16 v[180:183], v[200:203], v[124:127], v[180:183]\n"
    "v_lshlrev_b32_e32 v164, 16, v52\n"
    "v_and_b32_e32 v165, s47, v52\n"
    "v_mfma_f32_16x16x32_bf16 v[176:179], v[204:207], v[128:131], v[176:179]\n"
    "v_lshlrev_b32_e32 v166, 16, v53\n"
    "v_and_b32_e32 v167, s47, v53\n"
    "v_mfma_f32_16x16x32_bf16 v[180:183], v[204:207], v[132:135], v[180:183]\n"
    "v_cvt_pk_bf16_f32 v34, v168, v169\n"
    "v_cvt_pk_bf16_f32 v35, v170, v171\n"
    "s_waitcnt lgkmcnt(0)\n"
    "s_barrier\n"
    "ds_read_b64_tr_b16 v[192:193], v244 offset:12288\n"
    "ds_read_b64_tr_b16 v[194:195], v244 offset:14336\n"
    "v_mfma_f32_16x16x32_bf16 v[176:179], v[208:211], v[136:139], v[176:179]\n"
    "ds_read_b64_tr_b16 v[196:197], v244 offset:16384\n"
    "ds_read_b64_tr_b16 v[198:199], v244 offset:18432\n"
    "v_mfma_f32_16x16x32_bf16 v[180:183], v[208:211], v[140:143], v[180:183]\n"
    "ds_read_b64_tr_b16 v[200:201], v244 offset:20480\n"
    "ds_read_b64_tr_b16 v[202:203], v244 offset:22528\n"
    "v_mfma_f32_16x16x32_bf16 v[184:187], v[212:215], v[120:123], v[184:187]\n"
    "ds_read_b64_tr_b16 v[204:205], v245 offset:12288\n"
    "ds_read_b64_tr_b16 v[206:207], v245 offset:14336\n"
    "v_mfma_f32_16x16x32_bf16 v[188:191], v[212:215], v[124:127], v[188:191]\n"
    "ds_read_b64_tr_b16 v[208:209], v245 offset:16384\n"
    "ds_read_b64_tr_b16 v[210:211], v245 offset:18432\n"
    "v_mfma_f32_16x16x32_bf16 v[184:187], v[216:219], v[128:131], v[184:187]\n"
    "ds_read_b64_tr_b16 v[212:213], v245 offset:20480\n"
    "ds_read_b64_tr_b16 v[214:215], v245 offset:22528\n"
    "v_mfma_f32_16x16x32_bf16 v[188:191], v[216:219], v[132:135], v[188:191]\n"
    "ds_read_b64_tr_b16 v[216:217], v246 offset:12288\n"
    "ds_read_b64_tr_b16 v[218:219], v246 offset:14336\n"
    "v_mfma_f32_16x16x32_bf16 v[184:187], v[220:223], v[136:139], v[184:187]\n"
    "v_cvt_pk_bf16_f32 v38, v172, v173\n"
    "v_cvt_pk_bf16_f32 v39, v174, v175\n"
    "v_mfma_f32_16x16x32_bf16 v[188:191], v[220:223], v[140:143], v[188:191]\n"
    "ds_read_b64_tr_b16 v[220:221], v246 offset:16384\n"
    "s_waitcnt lgkmcnt(5)\n"
    "v_mfma_f32_16x16x32_bf16 v[160:163], v[192:195], v[96:99], v[160:163]\n"
    "ds_read_b64_tr_b16 v[222:223], v246 offset:18432\n"
    "s_add_u32 s42, s87, 0xe000\n"
    "v_mfma_f32_16x16x32_bf16 v[164:167], v[192:195], v[100:103], v[164:167]\n"
    "ds_read_b64_tr_b16 v[192:193], v246 offset:20480\n"
    "ds_read_b64_tr_b16 v[194:195], v246 offset:22528\n"
    "v_mfma_f32_16x16x32_bf16 v[160:163], v[196:199], v[104:107], v[160:163]\n"
    "s_cmp_eq_u32 s98, 2\n"
    "s_cselect_b32 s43, 2.0, s42\n"
    "buffer_store_dwordx4 v[36:39], v249, s[28:31], s43 offen sc1\n"
    "s_cselect_b64 exec, -1, 0\n"
    "ds_write_b128 v252, v[36:39] offset:40960\n"
    "s_mov_b64 exec, -1\n"
    "v_mfma_f32_16x16x32_bf16 v[164:167], v[196:199], v[108:111], v[164:167]\n"
    "ds_read_b64_tr_b16 v[196:197], v247 offset:12288\n"
    "ds_read_b64_tr_b16 v[198:199], v247 offset:14336\n"
    "s_cmp_eq_u32 s98, 0\n"
    "s_cselect_b32 s43, 2.0, s42\n"
    "buffer_store_dwordx4 v[32:35], v248, s[28:31], s43 offen sc1\n"
    "s_cselect_b64 exec, -1, 0\n"
    "ds_write_b128 v252, v[32:35] offset:40960\n"
    "s_mov_b64 exec, -1\n"
    "v_lshlrev_b32_e32 v168, 16, v50\n"
    "v_and_b32_e32 v169, s47, v50\n"
    "v_lshlrev_b32_e32 v170, 16, v51\n"
    "v_and_b32_e32 v171, s47, v51\n"
    "v_lshlrev_b32_e32 v172, 16, v54\n"
    "v_and_b32_e32 v173, s47, v54\n"
    "v_lshlrev_b32_e32 v174, 16, v55\n"
    "v_and_b32_e64 v175, s47, v55\n"
    "v_mfma_f32_16x16x32_bf16 v[160:163], v[200:203], v[112:115], v[160:163]\n"
    "v_cvt_pk_bf16_f32 v40, v176, v177\n"
    "v_cvt_pk_bf16_f32 v41, v178, v179\n"
    "v_mfma_f32_16x16x32_bf16 v[164:167], v[200:203], v[116:119], v[164:167]\n"
    "ds_read_b64_tr_b16 v[200:201], v247 offset:16384\n"
    "ds_read_b64_tr_b16 v[202:203], v247 offset:18432\n"
    "v_mfma_f32_16x16x32_bf16 v[168:171], v[204:207], v[96:99], v[168:171]\n"
    "v_cvt_pk_bf16_f32 v44, v180, v181\n"
    "v_cvt_pk_bf16_f32 v45, v182, v183\n"
    "v_mfma_f32_16x16x32_bf16 v[172:175], v[204:207], v[100:103], v[172:175]\n"
    "ds_read_b64_tr_b16 v[204:205], v247 offset:20480\n"
    "ds_read_b64_tr_b16 v[206:207], v247 offset:22528\n"
    "v_mfma_f32_16x16x32_bf16 v[168:171], v[208:211], v[104:107], v[168:171]\n"
    "v_lshlrev_b32_e32 v176, 16, v56\n"
    "v_and_b32_e32 v177, s47, v56\n"
    "v_mfma_f32_16x16x32_bf16 v[172:175], v[208:211], v[108:111], v[172:175]\n"
    "v_lshlrev_b32_e32 v178, 16, v57\n"
    "v_and_b32_e32 v179, s47, v57\n"
    "v_lshlrev_b32_e32 v180, 16, v60\n"
    "v_and_b32_e32 v181, s47, v60\n"
    "v_lshlrev_b32_e32 v182, 16, v61\n"
    "v_and_b32_e32 v183, s47, v61\n"
    "s_waitcnt lgkmcnt(2)\n"
    "v_mfma_f32_16x16x32_bf16 v[168:171], v[212:215], v[112:115], v[168:171]\n"
    "s_waitcnt vmcnt(2)\n"
    "v_add_u32_e32 v240, 1, v240\n"
    "s_mov_b64 exec, s[74:75]\n"
    "buffer_atomic_swap v240, v242, s[28:31], s94 offen\n"
    "s_mov_b64 exec, -1\n"
    "v_mfma_f32_16x16x32_bf16 v[172:175], v[212:215], v[116:119], v[172:175]\n"
    "v_cvt_pk_bf16_f32 v42, v184, v185\n"
    "v_cvt_pk_bf16_f32 v43, v186, v187\n"
    "v_mfma_f32_16x16x32_bf16 v[176:179], v[216:219], v[96:99], v[176:179]\n"
    "v_cvt_pk_bf16_f32 v46, v188, v189\n"
    "v_cvt_pk_bf16_f32 v47, v190, v191\n"
    "v_mfma_f32_16x16x32_bf16 v[180:183], v[216:219], v[100:103], v[180:183]\n"
    "s_cmp_eq_u32 s98, 3\n"
    "s_cselect_b32 s43, 2.0, s42\n"
    "buffer_store_dwordx4 v[44:47], v249, s[28:31], s43 offen offset:1024 sc1\n"
    "s_cselect_b64 exec, -1, 0\n"
    "ds_write_b128 v252, v[44:47] offset:40960\n"
    "s_mov_b64 exec, -1\n"
    "v_mfma_f32_16x16x32_bf16 v[176:179], v[220:223], v[104:107], v[176:179]\n"
    "s_cmp_eq_u32 s98, 1\n"
    "s_cselect_b32 s43, 2.0, s42\n"
    "buffer_store_dwordx4 v[40:43], v248, s[28:31], s43 offen offset:1024 sc1\n"
    "s_cselect_b64 exec, -1, 0\n"
    "ds_write_b128 v252, v[40:43] offset:40960\n"
    "s_mov_b64 exec, -1\n"
    "v_mfma_f32_16x16x32_bf16 v[180:183], v[220:223], v[108:111], v[180:183]\n"
    "v_lshlrev_b32_e32 v184, 16, v58\n"
    "v_and_b32_e32 v185, s47, v58\n"
    "s_waitcnt lgkmcnt(0)\n"
    "s_barrier\n"
    "ds_read_b64_tr_b16 v[208:209], v244 offset:24576\n"
    "ds_read_b64_tr_b16 v[210:211], v244 offset:26624\n"
    "v_lshlrev_b32_e32 v186, 16, v59\n"
    "v_and_b32_e32 v187, s47, v59\n"
    "v_lshlrev_b32_e32 v188, 16, v62\n"
    "v_and_b32_e32 v189, s47, v62\n"
    "v_lshlrev_b32_e32 v190, 16, v63\n"
    "v_and_b32_e64 v191, s47, v63\n"
    "v_mfma_f32_16x16x32_bf16 v[176:179], v[192:195], v[112:115], v[176:179]\n"
    "ds_read_b64_tr_b16 v[212:213], v244 offset:28672\n"
    "ds_read_b64_tr_b16 v[214:215], v244 offset:30720\n"
    "v_mfma_f32_16x16x32_bf16 v[180:183], v[192:195], v[116:119], v[180:183]\n"
    "ds_read_b64_tr_b16 v[216:217], v244 offset:32768\n"
    "ds_read_b64_tr_b16 v[218:219], v244 offset:34816\n"
    "v_mfma_f32_16x16x32_bf16 v[184:187], v[196:199], v[96:99], v[184:187]\n"
    "ds_read_b64_tr_b16 v[220:221], v245 offset:24576\n"
    "ds_read_b64_tr_b16 v[222:223], v245 offset:26624\n"
    "v_mfma_f32_16x16x32_bf16 v[188:191], v[196:199], v[100:103], v[188:191]\n"
    "ds_read_b64_tr_b16 v[192:193], v245 offset:28672\n"
    "ds_read_b64_tr_b16 v[194:195], v245 offset:30720\n"
    "v_mfma_f32_16x16x32_bf16 v[184:187], v[200:203], v[104:107], v[184:187]\n"
    "ds_read_b64_tr_b16 v[196:197], v245 offset:32768\n"
    "ds_read_b64_tr_b16 v[198:199], v245 offset:34816\n"
    "v_mfma_f32_16x16x32_bf16 v[188:191], v[200:203], v[108:111], v[188:191]\n"
    "ds_read_b64_tr_b16 v[200:201], v246 offset:24576\n"
    "ds_read_b64_tr_b16 v[202:203], v246 offset:26624\n"
    "v_mfma_f32_16x16x32_bf16 v[184:187], v[204:207], v[112:115], v[184:187]\n"
    "v_mfma_f32_16x16x32_bf16 v[188:191], v[204:207], v[116:119], v[188:191]\n"
    "ds_read_b64_tr_b16 v[204:205], v246 offset:28672\n"
    "s_waitcnt lgkmcnt(5)\n"
    "v_mfma_f32_16x16x32_bf16 v[160:163], v[208:211], v[120:123], v[160:163]\n"
    "ds_read_b64_tr_b16 v[206:207], v246 offset:30720\n"
    "v_mfma_f32_16x16x32_bf16 v[164:167], v[208:211], v[124:127], v[164:167]\n"
    "ds_read_b64_tr_b16 v[208:209], v246 offset:32768\n"
    "ds_read_b64_tr_b16 v[210:211], v246 offset:34816\n"
    "v_mfma_f32_16x16x32_bf16 v[160:163], v[212:215], v[128:131], v[160:163]\n"
    "v_mfma_f32_16x16x32_bf16 v[164:167], v[212:215], v[132:135], v[164:167]\n"
    "ds_read_b64_tr_b16 v[212:213], v247 offset:24576\n"
    "ds_read_b64_tr_b16 v[214:215], v247 offset:26624\n"
    "v_mfma_f32_16x16x32_bf16 v[160:163], v[216:219], v[136:139], v[160:163]\n"
    "v_mfma_f32_16x16x32_bf16 v[164:167], v[216:219], v[140:143], v[164:167]\n"
    "ds_read_b64_tr_b16 v[216:217], v247 offset:28672\n"
    "ds_read_b64_tr_b16 v[218:219], v247 offset:30720\n"
    "v_mfma_f32_16x16x32_bf16 v[168:171], v[220:223], v[120:123], v[168:171]\n"
    "v_mfma_f32_16x16x32_bf16 v[172:175], v[220:223], v[124:127], v[172:175]\n"
    "ds_read_b64_tr_b16 v[220:221], v247 offset:32768\n"
    "ds_read_b64_tr_b16 v[222:223], v247 offset:34816\n"
    "v_mfma_f32_16x16x32_bf16 v[168:171], v[192:195], v[128:131], v[168:171]\n"
    "v_mfma_f32_16x16x32_bf16 v[172:175], v[192:195], v[132:135], v[172:175]\n"
    "v_cvt_pk_bf16_f32 v48, v160, v161\n"
    "s_waitcnt lgkmcnt(3)\n"
    "v_mfma_f32_16x16x32_bf16 v[168:171], v[196:199], v[136:139], v[168:171]\n"
    "v_cvt_pk_bf16_f32 v49, v162, v163\n"
    "v_cvt_pk_bf16_f32 v52, v164, v165\n"
    "v_mfma_f32_16x16x32_bf16 v[172:175], v[196:199], v[140:143], v[172:175]\n"
    "v_cvt_pk_bf16_f32 v53, v166, v167\n"
    "v_lshlrev_b32_e64 v160, 16, v64\n"
    "v_mfma_f32_16x16x32_bf16 v[176:179], v[200:203], v[120:123], v[176:179]\n"
    "v_and_b32_e32 v161, s47, v64\n"
    "v_lshlrev_b32_e32 v162, 16, v65\n"
    "v_mfma_f32_16x16x32_bf16 v[180:183], v[200:203], v[124:127], v[180:183]\n"
    "v_and_b32_e32 v163, s47, v65\n"
    "v_lshlrev_b32_e32 v164, 16, v68\n"
    "v_mfma_f32_16x16x32_bf16 v[176:179], v[204:207], v[128:131], v[176:179]\n"
    "v_and_b32_e32 v165, s47, v68\n"
    "v_lshlrev_b32_e32 v166, 16, v69\n"
    "v_mfma_f32_16x16x32_bf16 v[180:183], v[204:207], v[132:135], v[180:183]\n"
    "v_and_b32_e64 v167, s47, v69\n"
    "v_cvt_pk_bf16_f32 v50, v168, v169\n"
    "s_waitcnt lgkmcnt(0)\n"
    "s_barrier\n"
    "v_add_u32_e32 v244, 0x12000, v244\n"
    "ds_read_b64_tr_b16 v[192:193], v244\n"
    "v_mfma_f32_16x16x32_bf16 v[176:179], v[208:211], v[136:139], v[176:179]\n"
    "ds_read_b64_tr_b16 v[194:195], v244 offset:2048\n"
    "ds_read_b64_tr_b16 v[196:197], v244 offset:4096\n"
    "v_mfma_f32_16x16x32_bf16 v[180:183], v[208:211], v[140:143], v[180:183]\n"
    "ds_read_b64_tr_b16 v[198:199], v244 offset:6144\n"
    "ds_read_b64_tr_b16 v[200:201], v244 offset:8192\n"
    "v_mfma_f32_16x16x32_bf16 v[184:187], v[212:215], v[120:123], v[184:187]\n"
    "ds_read_b64_tr_b16 v[202:203], v244 offset:10240\n"
    "v_add_u32_e32 v245, 0x12000, v245\n"
    "v_mfma_f32_16x16x32_bf16 v[188:191], v[212:215], v[124:127], v[188:191]\n"
    "ds_read_b64_tr_b16 v[204:205], v245\n"
    "ds_read_b64_tr_b16 v[206:207], v245 offset:2048\n"
    "v_mfma_f32_16x16x32_bf16 v[184:187], v[216:219], v[128:131], v[184:187]\n"
    "ds_read_b64_tr_b16 v[208:209], v245 offset:4096\n"
    "ds_read_b64_tr_b16 v[210:211], v245 offset:6144\n"
    "v_mfma_f32_16x16x32_bf16 v[188:191], v[216:219], v[132:135], v[188:191]\n"
    "ds_read_b64_tr_b16 v[212:213], v245 offset:8192\n"
    "ds_read_b64_tr_b16 v[214:215], v245 offset:10240\n"
    "v_mfma_f32_16x16x32_bf16 v[184:187], v[220:223], v[136:139], v[184:187]\n"
    "v_add_u32_e32 v246, 0x12000, v246\n"
    "ds_read_b64_tr_b16 v[216:217], v246\n"
    "v_mfma_f32_16x16x32_bf16 v[188:191], v[220:223], v[140:143], v[188:191]\n"
    "ds_read_b64_tr_b16 v[218:219], v246 offset:2048\n"
    "s_waitcnt lgkmcnt(6)\n"
    "v_mfma_f32_16x16x32_bf16 v[160:163], v[192:195], v[96:99], v[160:163]\n"
    "ds_read_b64_tr_b16 v[220:221], v246 offset:4096\n"
    "ds_read_b64_tr_b16 v[222:223], v246 offset:6144\n"
    "v_mfma_f32_16x16x32_bf16 v[164:167], v[192:195], v[100:103], v[164:167]\n"
    "ds_read_b64_tr_b16 v[192:193], v246 offset:8192\n"
    "ds_read_b64_tr_b16 v[194:195], v246 offset:10240\n"
    "v_mfma_f32_16x16x32_bf16 v[160:163], v[196:199], v[104:107], v[160:163]\n"
    "v_add_u32_e32 v247, 0x12000, v247\n"
    "v_cvt_pk_bf16_f32 v51, v170, v171\n"
    "v_mfma_f32_16x16x32_bf16 v[164:167], v[196:199], v[108:111], v[164:167]\n"
    "ds_read_b64_tr_b16 v[196:197], v247\n"
    "ds_read_b64_tr_b16 v[198:199], v247 offset:2048\n"
    "v_cvt_pk_bf16_f32 v54, v172, v173\n"
    "v_cvt_pk_bf16_f32 v55, v174, v175\n"
    "s_add_u32 s42, s87, 0x11800\n"
    "s_cmp_eq_u32 s98, 2\n"
    "s_cselect_b32 s43, 2.0, s42\n"
    "buffer_store_dwordx4 v[52:55], v249, s[28:31], s43 offen sc1\n"
    "s_cselect_b64 exec, -1, 0\n"
    "ds_write_b128 v252, v[52:55] offset:41984\n"
    "s_mov_b64 exec, -1\n"
    "s_cmp_eq_u32 s98, 0\n"
    "s_cselect_b32 s43, 2.0, s42\n"
    "buffer_store_dwordx4 v[48:51], v248, s[28:31], s43 offen sc1\n"
    "s_cselect_b64 exec, -1, 0\n"
    "ds_write_b128 v252, v[48:51] offset:41984\n"
    "s_mov_b64 exec, -1\n"
    "v_lshlrev_b32_e32 v168, 16, v66\n"
    "v_and_b32_e32 v169, s47, v66\n"
    "v_lshlrev_b32_e32 v170, 16, v67\n"
    "v_and_b32_e32 v171, s47, v67\n"
    "v_lshlrev_b32_e32 v172, 16, v70\n"
    "v_and_b32_e32 v173, s47, v70\n"
    "v_lshlrev_b32_e32 v174, 16, v71\n"
    "v_and_b32_e64 v175, s47, v71\n"
    "v_mfma_f32_16x16x32_bf16 v[160:163], v[200:203], v[112:115], v[160:163]\n"
    "v_cvt_pk_bf16_f32 v56, v176, v177\n"
    "v_cvt_pk_bf16_f32 v57, v178, v179\n"
    "v_mfma_f32_16x16x32_bf16 v[164:167], v[200:203], v[116:119], v[164:167]\n"
    "ds_read_b64_tr_b16 v[200:201], v247 offset:4096\n"
    "ds_read_b64_tr_b16 v[202:203], v247 offset:6144\n"
    "v_mfma_f32_16x16x32_bf16 v[168:171], v[204:207], v[96:99], v[168:171]\n"
    "v_cvt_pk_bf16_f32 v60, v180, v181\n"
    "v_cvt_pk_bf16_f32 v61, v182, v183\n"
    "v_mfma_f32_16x16x32_bf16 v[172:175], v[204:207], v[100:103], v[172:175]\n"
    "ds_read_b64_tr_b16 v[204:205], v247 offset:8192\n"
    "s_waitcnt lgkmcnt(3)\n"
    "v_mfma_f32_16x16x32_bf16 v[168:171], v[208:211], v[104:107], v[168:171]\n"
    "ds_read_b64_tr_b16 v[206:207], v247 offset:10240\n"
    "v_lshlrev_b32_e32 v176, 16, v72\n"
    "v_mfma_f32_16x16x32_bf16 v[172:175], v[208:211], v[108:111], v[172:175]\n"
    "v_and_b32_e32 v177, s47, v72\n"
    "v_lshlrev_b32_e32 v178, 16, v73\n"
    "v_and_b32_e32 v179, s47, v73\n"
    "v_lshlrev_b32_e32 v180, 16, v76\n"
    "v_and_b32_e32 v181, s47, v76\n"
    "v_lshlrev_b32_e32 v182, 16, v77\n"
    "v_and_b32_e64 v183, s47, v77\n"
    "v_mfma_f32_16x16x32_bf16 v[168:171], v[212:215], v[112:115], v[168:171]\n"
    "s_waitcnt vmcnt(2)\n"
    "v_add_u32_e32 v240, 1, v240\n"
    "s_mov_b64 exec, s[74:75]\n"
    "buffer_atomic_swap v240, v242, s[28:31], s94 offen\n"
    "s_mov_b64 exec, -1\n"
    "v_mfma_f32_16x16x32_bf16 v[172:175], v[212:215], v[116:119], v[172:175]\n"
    "v_cvt_pk_bf16_f32 v58, v184, v185\n"
    "v_cvt_pk_bf16_f32 v59, v186, v187\n"
    "v_mfma_f32_16x16x32_bf16 v[176:179], v[216:219], v[96:99], v[176:179]\n"
    "v_cvt_pk_bf16_f32 v62, v188, v189\n"
    "v_cvt_pk_bf16_f32 v63, v190, v191\n"
    "v_mfma_f32_16x16x32_bf16 v[180:183], v[216:219], v[100:103], v[180:183]\n"
    "s_cmp_eq_u32 s98, 3\n"
    "s_cselect_b32 s43, 2.0, s42\n"
    "buffer_store_dwordx4 v[60:63], v249, s[28:31], s43 offen offset:1024 sc1\n"
    "s_cselect_b64 exec, -1, 0\n"
    "ds_write_b128 v252, v[60:63] offset:41984\n"
    "s_mov_b64 exec, -1\n"
    "v_mfma_f32_16x16x32_bf16 v[176:179], v[220:223], v[104:107], v[176:179]\n"
    "s_cmp_eq_u32 s98, 1\n"
    "s_cselect_b32 s43, 2.0, s42\n"
    "buffer_store_dwordx4 v[56:59], v248, s[28:31], s43 offen offset:1024 sc1\n"
    "s_cselect_b64 exec, -1, 0\n"
    "ds_write_b128 v252, v[56:59] offset:41984\n"
    "s_mov_b64 exec, -1\n"
    "v_mfma_f32_16x16x32_bf16 v[180:183], v[220:223], v[108:111], v[180:183]\n"
    "v_lshlrev_b32_e32 v184, 16, v74\n"
    "v_and_b32_e32 v185, s47, v74\n"
    "s_waitcnt lgkmcnt(0)\n"
    "s_barrier\n"
    "ds_read_b64_tr_b16 v[208:209], v244 offset:12288\n"
    "ds_read_b64_tr_b16 v[210:211], v244 offset:14336\n"
    "v_lshlrev_b32_e32 v186, 16, v75\n"
    "v_and_b32_e32 v187, s47, v75\n"
    "v_lshlrev_b32_e32 v188, 16, v78\n"
    "v_and_b32_e32 v189, s47, v78\n"
    "v_lshlrev_b32_e32 v190, 16, v79\n"
    "v_and_b32_e32 v191, s47, v79\n"
    "v_mfma_f32_16x16x32_bf16 v[176:179], v[192:195], v[112:115], v[176:179]\n"
    "ds_read_b64_tr_b16 v[212:213], v244 offset:16384\n"
    "ds_read_b64_tr_b16 v[214:215], v244 offset:18432\n"
    "v_mfma_f32_16x16x32_bf16 v[180:183], v[192:195], v[116:119], v[180:183]\n"
    "ds_read_b64_tr_b16 v[216:217], v244 offset:20480\n"
    "ds_read_b64_tr_b16 v[218:219], v244 offset:22528\n"
    "v_mfma_f32_16x16x32_bf16 v[184:187], v[196:199], v[96:99], v[184:187]\n"
    "ds_read_b64_tr_b16 v[220:221], v245 offset:12288\n"
    "ds_read_b64_tr_b16 v[222:223], v245 offset:14336\n"
    "v_mfma_f32_16x16x32_bf16 v[188:191], v[196:199], v[100:103], v[188:191]\n"
    "ds_read_b64_tr_b16 v[192:193], v245 offset:16384\n"
    "ds_read_b64_tr_b16 v[194:195], v245 offset:18432\n"
    "v_mfma_f32_16x16x32_bf16 v[184:187], v[200:203], v[104:107], v[184:187]\n"
    "ds_read_b64_tr_b16 v[196:197], v245 offset:20480\n"
    "ds_read_b64_tr_b16 v[198:199], v245 offset:22528\n"
    "v_mfma_f32_16x16x32_bf16 v[188:191], v[200:203], v[108:111], v[188:191]\n"
    "ds_read_b64_tr_b16 v[200:201], v246 offset:12288\n"
    "ds_read_b64_tr_b16 v[202:203], v246 offset:14336\n"
    "v_mfma_f32_16x16x32_bf16 v[184:187], v[204:207], v[112:115], v[184:187]\n"
    "v_mfma_f32_16x16x32_bf16 v[188:191], v[204:207], v[116:119], v[188:191]\n"
    "ds_read_b64_tr_b16 v[204:205], v246 offset:16384\n"
    "s_waitcnt lgkmcnt(5)\n"
    "v_mfma_f32_16x16x32_bf16 v[160:163], v[208:211], v[120:123], v[160:163]\n"
    "ds_read_b64_tr_b16 v[206:207], v246 offset:18432\n"
    "v_mfma_f32_16x16x32_bf16 v[164:167], v[208:211], v[124:127], v[164:167]\n"
    "ds_read_b64_tr_b16 v[208:209], v246 offset:20480\n"
    "ds_read_b64_tr_b16 v[210:211], v246 offset:22528\n"
    "v_mfma_f32_16x16x32_bf16 v[160:163], v[212:215], v[128:131], v[160:163]\n"
    "v_mfma_f32_16x16x32_bf16 v[164:167], v[212:215], v[132:135], v[164:167]\n"
    "ds_read_b64_tr_b16 v[212:213], v247 offset:12288\n"
    "ds_read_b64_tr_b16 v[214:215], v247 offset:14336\n"
    "v_mfma_f32_16x16x32_bf16 v[160:163], v[216:219], v[136:139], v[160:163]\n"
    "v_mfma_f32_16x16x32_bf16 v[164:167], v[216:219], v[140:143], v[164:167]\n"
    "ds_read_b64_tr_b16 v[216:217], v247 offset:16384\n"
    "ds_read_b64_tr_b16 v[218:219], v247 offset:18432\n"
    "v_mfma_f32_16x16x32_bf16 v[168:171], v[220:223], v[120:123], v[168:171]\n"
    "v_mfma_f32_16x16x32_bf16 v[172:175], v[220:223], v[124:127], v[172:175]\n"
    "ds_read_b64_tr_b16 v[220:221], v247 offset:20480\n"
    "ds_read_b64_tr_b16 v[222:223], v247 offset:22528\n"
    "v_mfma_f32_16x16x32_bf16 v[168:171], v[192:195], v[128:131], v[168:171]\n"
    "v_mfma_f32_16x16x32_bf16 v[172:175], v[192:195], v[132:135], v[172:175]\n"
    "v_cvt_pk_bf16_f32 v64, v160, v161\n"
    "s_waitcnt lgkmcnt(3)\n"
    "v_mfma_f32_16x16x32_bf16 v[168:171], v[196:199], v[136:139], v[168:171]\n"
    "v_cvt_pk_bf16_f32 v65, v162, v163\n"
    "v_cvt_pk_bf16_f32 v68, v164, v165\n"
    "v_mfma_f32_16x16x32_bf16 v[172:175], v[196:199], v[140:143], v[172:175]\n"
    "v_cvt_pk_bf16_f32 v69, v166, v167\n"
    "v_lshlrev_b32_e64 v160, 16, v80\n"
    "v_mfma_f32_16x16x32_bf16 v[176:179], v[200:203], v[120:123], v[176:179]\n"
    "v_and_b32_e32 v161, s47, v80\n"
    "v_lshlrev_b32_e32 v162, 16, v81\n"
    "v_mfma_f32_16x16x32_bf16 v[180:183], v[200:203], v[124:127], v[180:183]\n"
    "v_and_b32_e32 v163, s47, v81\n"
    "v_lshlrev_b32_e32 v164, 16, v84\n"
    "v_mfma_f32_16x16x32_bf16 v[176:179], v[204:207], v[128:131], v[176:179]\n"
    "v_and_b32_e32 v165, s47, v84\n"
    "v_lshlrev_b32_e32 v166, 16, v85\n"
    "v_mfma_f32_16x16x32_bf16 v[180:183], v[204:207], v[132:135], v[180:183]\n"
    "v_and_b32_e64 v167, s47, v85\n"
    "v_cvt_pk_bf16_f32 v66, v168, v169\n"
    "s_waitcnt lgkmcnt(0)\n"
    "s_barrier\n"
    "ds_read_b64_tr_b16 v[192:193], v244 offset:24576\n"
    "ds_read_b64_tr_b16 v[194:195], v244 offset:26624\n"
    "v_mfma_f32_16x16x32_bf16 v[176:179], v[208:211], v[136:139], v[176:179]\n"
    "ds_read_b64_tr_b16 v[196:197], v244 offset:28672\n"
    "ds_read_b64_tr_b16 v[198:199], v244 offset:30720\n"
    "v_mfma_f32_16x16x32_bf16 v[180:183], v[208:211], v[140:143], v[180:183]\n"
    "ds_read_b64_tr_b16 v[200:201], v244 offset:32768\n"
    "ds_read_b64_tr_b16 v[202:203], v244 offset:34816\n"
    "v_mfma_f32_16x16x32_bf16 v[184:187], v[212:215], v[120:123], v[184:187]\n"
    "ds_read_b64_tr_b16 v[204:205], v245 offset:24576\n"
    "ds_read_b64_tr_b16 v[206:207], v245 offset:26624\n"
    "v_mfma_f32_16x16x32_bf16 v[188:191], v[212:215], v[124:127], v[188:191]\n"
    "ds_read_b64_tr_b16 v[208:209], v245 offset:28672\n"
    "ds_read_b64_tr_b16 v[210:211], v245 offset:30720\n"
    "v_mfma_f32_16x16x32_bf16 v[184:187], v[216:219], v[128:131], v[184:187]\n"
    "ds_read_b64_tr_b16 v[212:213], v245 offset:32768\n"
    "ds_read_b64_tr_b16 v[214:215], v245 offset:34816\n"
    "v_mfma_f32_16x16x32_bf16 v[188:191], v[216:219], v[132:135], v[188:191]\n"
    "ds_read_b64_tr_b16 v[216:217], v246 offset:24576\n"
    "ds_read_b64_tr_b16 v[218:219], v246 offset:26624\n"
    "v_mfma_f32_16x16x32_bf16 v[184:187], v[220:223], v[136:139], v[184:187]\n"
    "v_cvt_pk_bf16_f32 v67, v170, v171\n"
    "v_cvt_pk_bf16_f32 v70, v172, v173\n"
    "v_mfma_f32_16x16x32_bf16 v[188:191], v[220:223], v[140:143], v[188:191]\n"
    "ds_read_b64_tr_b16 v[220:221], v246 offset:28672\n"
    "s_waitcnt lgkmcnt(5)\n"
    "v_mfma_f32_16x16x32_bf16 v[160:163], v[192:195], v[96:99], v[160:163]\n"
    "ds_read_b64_tr_b16 v[222:223], v246 offset:30720\n"
    "v_cvt_pk_bf16_f32 v71, v174, v175\n"
    "v_mfma_f32_16x16x32_bf16 v[164:167], v[192:195], v[100:103], v[164:167]\n"
    "ds_read_b64_tr_b16 v[192:193], v246 offset:32768\n"
    "ds_read_b64_tr_b16 v[194:195], v246 offset:34816\n"
    "v_mfma_f32_16x16x32_bf16 v[160:163], v[196:199], v[104:107], v[160:163]\n"
    "s_add_u32 s42, s87, 0x15000\n"
    "v_mfma_f32_16x16x32_bf16 v[164:167], v[196:199], v[108:111], v[164:167]\n"
    "ds_read_b64_tr_b16 v[196:197], v247 offset:24576\n"
    "ds_read_b64_tr_b16 v[198:199], v247 offset:26624\n"
    "s_cmp_eq_u32 s98, 2\n"
    "s_cselect_b32 s43, 2.0, s42\n"
    "buffer_store_dwordx4 v[68:71], v249, s[28:31], s43 offen sc1\n"
    "s_cselect_b64 exec, -1, 0\n"
    "ds_write_b128 v252, v[68:71] offset:43008\n"
    "s_mov_b64 exec, -1\n"
    "s_cmp_eq_u32 s98, 0\n"
    "s_cselect_b32 s43, 2.0, s42\n"
    "buffer_store_dwordx4 v[64:67], v248, s[28:31], s43 offen sc1\n"
    "s_cselect_b64 exec, -1, 0\n"
    "ds_write_b128 v252, v[64:67] offset:43008\n"
    "s_mov_b64 exec, -1\n"
    "v_lshlrev_b32_e32 v168, 16, v82\n"
    "v_and_b32_e32 v169, s47, v82\n"
    "v_lshlrev_b32_e32 v170, 16, v83\n"
    "v_and_b32_e32 v171, s47, v83\n"
    "v_lshlrev_b32_e32 v172, 16, v86\n"
    "v_and_b32_e32 v173, s47, v86\n"
    "v_lshlrev_b32_e32 v174, 16, v87\n"
    "v_and_b32_e64 v175, s47, v87\n"
    "v_mfma_f32_16x16x32_bf16 v[160:163], v[200:203], v[112:115], v[160:163]\n"
    "v_cvt_pk_bf16_f32 v72, v176, v177\n"
    "v_cvt_pk_bf16_f32 v73, v178, v179\n"
    "v_mfma_f32_16x16x32_bf16 v[164:167], v[200:203], v[116:119], v[164:167]\n"
    "ds_read_b64_tr_b16 v[200:201], v247 offset:28672\n"
    "ds_read_b64_tr_b16 v[202:203], v247 offset:30720\n"
    "v_mfma_f32_16x16x32_bf16 v[168:171], v[204:207], v[96:99], v[168:171]\n"
    "v_cvt_pk_bf16_f32 v76, v180, v181\n"
    "v_cvt_pk_bf16_f32 v77, v182, v183\n"
    "v_mfma_f32_16x16x32_bf16 v[172:175], v[204:207], v[100:103], v[172:175]\n"
    "ds_read_b64_tr_b16 v[204:205], v247 offset:32768\n"
    "ds_read_b64_tr_b16 v[206:207], v247 offset:34816\n"
    "v_mfma_f32_16x16x32_bf16 v[168:171], v[208:211], v[104:107], v[168:171]\n"
    "v_lshlrev_b32_e32 v176, 16, v88\n"
    "v_and_b32_e32 v177, s47, v88\n"
    "v_mfma_f32_16x16x32_bf16 v[172:175], v[208:211], v[108:111], v[172:175]\n"
    "v_lshlrev_b32_e32 v178, 16, v89\n"
    "v_and_b32_e32 v179, s47, v89\n"
    "v_lshlrev_b32_e32 v180, 16, v92\n"
    "v_and_b32_e32 v181, s47, v92\n"
    "v_lshlrev_b32_e32 v182, 16, v93\n"
    "v_and_b32_e32 v183, s47, v93\n"
    "s_waitcnt lgkmcnt(2)\n"
    "v_mfma_f32_16x16x32_bf16 v[168:171], v[212:215], v[112:115], v[168:171]\n"
    "s_waitcnt vmcnt(2)\n"
    "v_add_u32_e32 v240, 1, v240\n"
    "s_mov_b64 exec, s[74:75]\n"
    "buffer_atomic_swap v240, v242, s[28:31], s94 offen\n"
    "s_mov_b64 exec, -1\n"
    "v_mfma_f32_16x16x32_bf16 v[172:175], v[212:215], v[116:119], v[172:175]\n"
    "v_cvt_pk_bf16_f32 v74, v184, v185\n"
    "v_cvt_pk_bf16_f32 v75, v186, v187\n"
    "v_mfma_f32_16x16x32_bf16 v[176:179], v[216:219], v[96:99], v[176:179]\n"
    "v_cvt_pk_bf16_f32 v78, v188, v189\n"
    "v_cvt_pk_bf16_f32 v79, v190, v191\n"
    "v_mfma_f32_16x16x32_bf16 v[180:183], v[216:219], v[100:103], v[180:183]\n"
    "s_cmp_eq_u32 s98, 3\n"
    "s_cselect_b32 s43, 2.0, s42\n"
    "buffer_store_dwordx4 v[76:79], v249, s[28:31], s43 offen offset:1024 sc1\n"
    "s_cselect_b64 exec, -1, 0\n"
    "ds_write_b128 v252, v[76:79] offset:43008\n"
    "s_mov_b64 exec, -1\n"
    "v_mfma_f32_16x16x32_bf16 v[176:179], v[220:223], v[104:107], v[176:179]\n"
    "s_cmp_eq_u32 s98, 1\n"
    "s_cselect_b32 s43, 2.0, s42\n"
    "buffer_store_dwordx4 v[72:75], v248, s[28:31], s43 offen offset:1024 sc1\n"
    "s_cselect_b64 exec, -1, 0\n"
    "ds_write_b128 v252, v[72:75] offset:43008\n"
    "s_mov_b64 exec, -1\n"
    "v_mfma_f32_16x16x32_bf16 v[180:183], v[220:223], v[108:111], v[180:183]\n"
    "v_lshlrev_b32_e32 v184, 16, v90\n"
    "v_and_b32_e64 v185, s47, v90\n"
    "ds_read_b64_tr_b16 v[208:209], v244 offset:36864\n"
    "ds_read_b64_tr_b16 v[210:211], v244 offset:38912\n"
    "v_lshlrev_b32_e32 v186, 16, v91\n"
    "v_and_b32_e32 v187, s47, v91\n"
    "v_lshlrev_b32_e32 v188, 16, v94\n"
    "v_and_b32_e32 v189, s47, v94\n"
    "v_lshlrev_b32_e32 v190, 16, v95\n"
    "v_and_b32_e32 v191, s47, v95\n"
    "v_mfma_f32_16x16x32_bf16 v[176:179], v[192:195], v[112:115], v[176:179]\n"
    "ds_read_b64_tr_b16 v[212:213], v244 offset:40960\n"
    "ds_read_b64_tr_b16 v[214:215], v244 offset:43008\n"
    "v_mfma_f32_16x16x32_bf16 v[180:183], v[192:195], v[116:119], v[180:183]\n"
    "ds_read_b64_tr_b16 v[216:217], v244 offset:45056\n"
    "ds_read_b64_tr_b16 v[218:219], v244 offset:47104\n"
    "v_mfma_f32_16x16x32_bf16 v[184:187], v[196:199], v[96:99], v[184:187]\n"
    "ds_read_b64_tr_b16 v[220:221], v245 offset:36864\n"
    "ds_read_b64_tr_b16 v[222:223], v245 offset:38912\n"
    "v_mfma_f32_16x16x32_bf16 v[188:191], v[196:199], v[100:103], v[188:191]\n"
    "ds_read_b64_tr_b16 v[192:193], v245 offset:40960\n"
    "ds_read_b64_tr_b16 v[194:195], v245 offset:43008\n"
    "v_mfma_f32_16x16x32_bf16 v[184:187], v[200:203], v[104:107], v[184:187]\n"
    "ds_read_b64_tr_b16 v[196:197], v245 offset:45056\n"
    "ds_read_b64_tr_b16 v[198:199], v245 offset:47104\n"
    "v_mfma_f32_16x16x32_bf16 v[188:191], v[200:203], v[108:111], v[188:191]\n"
    "ds_read_b64_tr_b16 v[200:201], v246 offset:36864\n"
    "s_waitcnt lgkmcnt(7)\n"
    "v_mfma_f32_16x16x32_bf16 v[184:187], v[204:207], v[112:115], v[184:187]\n"
    "ds_read_b64_tr_b16 v[202:203], v246 offset:38912\n"
    "v_mfma_f32_16x16x32_bf16 v[188:191], v[204:207], v[116:119], v[188:191]\n"
    "ds_read_b64_tr_b16 v[204:205], v246 offset:40960\n"
    "ds_read_b64_tr_b16 v[206:207], v246 offset:43008\n"
    "v_mfma_f32_16x16x32_bf16 v[160:163], v[208:211], v[120:123], v[160:163]\n"
    "v_mfma_f32_16x16x32_bf16 v[164:167], v[208:211], v[124:127], v[164:167]\n"
    "ds_read_b64_tr_b16 v[208:209], v246 offset:45056\n"
    "ds_read_b64_tr_b16 v[210:211], v246 offset:47104\n"
    "v_mfma_f32_16x16x32_bf16 v[160:163], v[212:215], v[128:131], v[160:163]\n"
    "v_mfma_f32_16x16x32_bf16 v[164:167], v[212:215], v[132:135], v[164:167]\n"
    "ds_read_b64_tr_b16 v[212:213], v247 offset:36864\n"
    "ds_read_b64_tr_b16 v[214:215], v247 offset:38912\n"
    "v_mfma_f32_16x16x32_bf16 v[160:163], v[216:219], v[136:139], v[160:163]\n"
    "v_mfma_f32_16x16x32_bf16 v[164:167], v[216:219], v[140:143], v[164:167]\n"
    "ds_read_b64_tr_b16 v[216:217], v247 offset:40960\n"
    "s_waitcnt lgkmcnt(4)\n"
    "v_mfma_f32_16x16x32_bf16 v[168:171], v[220:223], v[120:123], v[168:171]\n"
    "ds_read_b64_tr_b16 v[218:219], v247 offset:43008\n"
    "v_mfma_f32_16x16x32_bf16 v[172:175], v[220:223], v[124:127], v[172:175]\n"
    "ds_read_b64_tr_b16 v[220:221], v247 offset:45056\n"
    "ds_read_b64_tr_b16 v[222:223], v247 offset:47104\n"
    "v_mfma_f32_16x16x32_bf16 v[168:171], v[192:195], v[128:131], v[168:171]\n"
    "v_mfma_f32_16x16x32_bf16 v[172:175], v[192:195], v[132:135], v[172:175]\n"
    "v_cvt_pk_bf16_f32 v80, v160, v161\n"
    "v_cvt_pk_bf16_f32 v81, v162, v163\n"
    "v_mfma_f32_16x16x32_bf16 v[168:171], v[196:199], v[136:139], v[168:171]\n"
    "v_cvt_pk_bf16_f32 v84, v164, v165\n"
    "v_cvt_pk_bf16_f32 v85, v166, v167\n"
    "v_mfma_f32_16x16x32_bf16 v[172:175], v[196:199], v[140:143], v[172:175]\n"
    "v_lshlrev_b32_e32 v160, 16, v144\n"
    "v_and_b32_e32 v161, s47, v144\n"
    "v_mfma_f32_16x16x32_bf16 v[176:179], v[200:203], v[120:123], v[176:179]\n"
    "v_lshlrev_b32_e32 v162, 16, v145\n"
    "v_and_b32_e32 v163, s47, v145\n"
    "v_mfma_f32_16x16x32_bf16 v[180:183], v[200:203], v[124:127], v[180:183]\n"
    "v_lshlrev_b32_e32 v164, 16, v148\n"
    "v_and_b32_e32 v165, s47, v148\n"
    "v_mfma_f32_16x16x32_bf16 v[176:179], v[204:207], v[128:131], v[176:179]\n"
    "v_lshlrev_b32_e32 v166, 16, v149\n"
    "v_and_b32_e32 v167, s47, v149\n"
    "v_mfma_f32_16x16x32_bf16 v[180:183], v[204:207], v[132:135], v[180:183]\n"
    "v_cvt_pk_bf16_f32 v82, v168, v169\n"
    "v_cvt_pk_bf16_f32 v83, v170, v171\n"
    "ds_read_b64_tr_b16 v[192:193], v244 offset:49152\n"
    "ds_read_b64_tr_b16 v[194:195], v244 offset:51200\n"
    "s_waitcnt lgkmcnt(2)\n"
    "v_mfma_f32_16x16x32_bf16 v[176:179], v[208:211], v[136:139], v[176:179]\n"
    "ds_read_b64_tr_b16 v[196:197], v244 offset:53248\n"
    "ds_read_b64_tr_b16 v[198:199], v244 offset:55296\n"
    "v_mfma_f32_16x16x32_bf16 v[180:183], v[208:211], v[140:143], v[180:183]\n"
    "ds_read_b64_tr_b16 v[200:201], v244 offset:57344\n"
    "ds_read_b64_tr_b16 v[202:203], v244 offset:59392\n"
    "v_mfma_f32_16x16x32_bf16 v[184:187], v[212:215], v[120:123], v[184:187]\n"
    "ds_read_b64_tr_b16 v[204:205], v245 offset:49152\n"
    "ds_read_b64_tr_b16 v[206:207], v245 offset:51200\n"
    "v_mfma_f32_16x16x32_bf16 v[188:191], v[212:215], v[124:127], v[188:191]\n"
    "ds_read_b64_tr_b16 v[208:209], v245 offset:53248\n"
    "ds_read_b64_tr_b16 v[210:211], v245 offset:55296\n"
    "v_mfma_f32_16x16x32_bf16 v[184:187], v[216:219], v[128:131], v[184:187]\n"
    "ds_read_b64_tr_b16 v[212:213], v245 offset:57344\n"
    "ds_read_b64_tr_b16 v[214:215], v245 offset:59392\n"
    "v_mfma_f32_16x16x32_bf16 v[188:191], v[216:219], v[132:135], v[188:191]\n"
    "ds_read_b64_tr_b16 v[216:217], v246 offset:49152\n"
    "ds_read_b64_tr_b16 v[218:219], v246 offset:51200\n"
    "v_mfma_f32_16x16x32_bf16 v[184:187], v[220:223], v[136:139], v[184:187]\n"
    "v_cvt_pk_bf16_f32 v86, v172, v173\n"
    "v_cvt_pk_bf16_f32 v87, v174, v175\n"
    "v_mfma_f32_16x16x32_bf16 v[188:191], v[220:223], v[140:143], v[188:191]\n"
    "ds_read_b64_tr_b16 v[220:221], v246 offset:53248\n"
    "s_waitcnt lgkmcnt(5)\n"
    "v_mfma_f32_16x16x32_bf16 v[160:163], v[192:195], v[96:99], v[160:163]\n"
    "ds_read_b64_tr_b16 v[222:223], v246 offset:55296\n"
    "s_add_u32 s42, s87, 0x18800\n"
    "v_mfma_f32_16x16x32_bf16 v[164:167], v[192:195], v[100:103], v[164:167]\n"
    "ds_read_b64_tr_b16 v[192:193], v246 offset:57344\n"
    "ds_read_b64_tr_b16 v[194:195], v246 offset:59392\n"
    "v_mfma_f32_16x16x32_bf16 v[160:163], v[196:199], v[104:107], v[160:163]\n"
    "s_cmp_eq_u32 s98, 2\n"
    "s_cselect_b32 s43, 2.0, s42\n"
    "buffer_store_dwordx4 v[84:87], v249, s[28:31], s43 offen sc1\n"
    "s_cselect_b64 exec, -1, 0\n"
    "ds_write_b128 v252, v[84:87] offset:44032\n"
    "s_mov_b64 exec, -1\n"
    "v_mfma_f32_16x16x32_bf16 v[164:167], v[196:199], v[108:111], v[164:167]\n"
    "ds_read_b64_tr_b16 v[196:197], v247 offset:49152\n"
    "ds_read_b64_tr_b16 v[198:199], v247 offset:51200\n"
    "s_cmp_eq_u32 s98, 0\n"
    "s_cselect_b32 s43, 2.0, s42\n"
    "buffer_store_dwordx4 v[80:83], v248, s[28:31], s43 offen sc1\n"
    "s_cselect_b64 exec, -1, 0\n"
    "ds_write_b128 v252, v[80:83] offset:44032\n"
    "s_mov_b64 exec, -1\n"
    "v_lshlrev_b32_e32 v168, 16, v146\n"
    "v_and_b32_e32 v169, s47, v146\n"
    "v_lshlrev_b32_e32 v170, 16, v147\n"
    "v_and_b32_e32 v171, s47, v147\n"
    "v_lshlrev_b32_e32 v172, 16, v150\n"
    "v_and_b32_e32 v173, s47, v150\n"
    "v_lshlrev_b32_e32 v174, 16, v151\n"
    "v_and_b32_e32 v175, s47, v151\n"
    "v_mfma_f32_16x16x32_bf16 v[160:163], v[200:203], v[112:115], v[160:163]\n"
    "v_cvt_pk_bf16_f32 v88, v176, v177\n"
    "v_cvt_pk_bf16_f32 v89, v178, v179\n"
    "v_mfma_f32_16x16x32_bf16 v[164:167], v[200:203], v[116:119], v[164:167]\n"
    "ds_read_b64_tr_b16 v[200:201], v247 offset:53248\n"
    "ds_read_b64_tr_b16 v[202:203], v247 offset:55296\n"
    "v_mfma_f32_16x16x32_bf16 v[168:171], v[204:207], v[96:99], v[168:171]\n"
    "v_cvt_pk_bf16_f32 v92, v180, v181\n"
    "v_cvt_pk_bf16_f32 v93, v182, v183\n"
    "v_mfma_f32_16x16x32_bf16 v[172:175], v[204:207], v[100:103], v[172:175]\n"
    "ds_read_b64_tr_b16 v[204:205], v247 offset:57344\n"
    "ds_read_b64_tr_b16 v[206:207], v247 offset:59392\n"
    "v_mfma_f32_16x16x32_bf16 v[168:171], v[208:211], v[104:107], v[168:171]\n"
    "v_lshlrev_b32_e32 v176, 16, v152\n"
    "v_and_b32_e32 v177, s47, v152\n"
    "v_mfma_f32_16x16x32_bf16 v[172:175], v[208:211], v[108:111], v[172:175]\n"
    "v_lshlrev_b32_e32 v178, 16, v153\n"
    "v_and_b32_e32 v179, s47, v153\n"
    "v_lshlrev_b32_e32 v180, 16, v156\n"
    "v_and_b32_e32 v181, s47, v156\n"
    "v_lshlrev_b32_e32 v182, 16, v157\n"
    "v_and_b32_e32 v183, s47, v157\n"
    "s_waitcnt lgkmcnt(2)\n"
    "v_mfma_f32_16x16x32_bf16 v[168:171], v[212:215], v[112:115], v[168:171]\n"
    "s_waitcnt vmcnt(2)\n"
    "v_add_u32_e32 v240, 1, v240\n"
    "s_mov_b64 exec, s[74:75]\n"
    "buffer_atomic_swap v240, v242, s[28:31], s94 offen\n"
    "s_mov_b64 exec, -1\n"
    "v_mfma_f32_16x16x32_bf16 v[172:175], v[212:215], v[116:119], v[172:175]\n"
    "v_cvt_pk_bf16_f32 v90, v184, v185\n"
    "v_cvt_pk_bf16_f32 v91, v186, v187\n"
    "v_mfma_f32_16x16x32_bf16 v[176:179], v[216:219], v[96:99], v[176:179]\n"
    "v_cvt_pk_bf16_f32 v94, v188, v189\n"
    "v_cvt_pk_bf16_f32 v95, v190, v191\n"
    "v_mfma_f32_16x16x32_bf16 v[180:183], v[216:219], v[100:103], v[180:183]\n"
    "s_cmp_eq_u32 s98, 3\n"
    "s_cselect_b32 s43, 2.0, s42\n"
    "buffer_store_dwordx4 v[92:95], v249, s[28:31], s43 offen offset:1024 sc1\n"
    "s_cselect_b64 exec, -1, 0\n"
    "ds_write_b128 v252, v[92:95] offset:44032\n"
    "s_mov_b64 exec, -1\n"
    "v_mfma_f32_16x16x32_bf16 v[176:179], v[220:223], v[104:107], v[176:179]\n"
    "s_cmp_eq_u32 s98, 1\n"
    "s_cselect_b32 s43, 2.0, s42\n"
    "buffer_store_dwordx4 v[88:91], v248, s[28:31], s43 offen offset:1024 sc1\n"
    "s_cselect_b64 exec, -1, 0\n"
    "ds_write_b128 v252, v[88:91] offset:44032\n"
    "s_mov_b64 exec, -1\n"
    "v_mfma_f32_16x16x32_bf16 v[180:183], v[220:223], v[108:111], v[180:183]\n"
    "v_lshlrev_b32_e32 v184, 16, v154\n"
    "v_and_b32_e64 v185, s47, v154\n"
    "v_add_u32_e32 v244, 0xffffa000, v244\n"
    "ds_read_b64_tr_b16 v[208:209], v244\n"
    "v_lshlrev_b32_e32 v186, 16, v155\n"
    "v_and_b32_e32 v187, s47, v155\n"
    "v_lshlrev_b32_e32 v188, 16, v158\n"
    "v_and_b32_e32 v189, s47, v158\n"
    "v_lshlrev_b32_e32 v190, 16, v159\n"
    "v_and_b32_e32 v191, s47, v159\n"
    "v_mfma_f32_16x16x32_bf16 v[176:179], v[192:195], v[112:115], v[176:179]\n"
    "ds_read_b64_tr_b16 v[210:211], v244 offset:2048\n"
    "ds_read_b64_tr_b16 v[212:213], v244 offset:4096\n"
    "v_mfma_f32_16x16x32_bf16 v[180:183], v[192:195], v[116:119], v[180:183]\n"
    "ds_read_b64_tr_b16 v[214:215], v244 offset:6144\n"
    "ds_read_b64_tr_b16 v[216:217], v244 offset:8192\n"
    "v_mfma_f32_16x16x32_bf16 v[184:187], v[196:199], v[96:99], v[184:187]\n"
    "ds_read_b64_tr_b16 v[218:219], v244 offset:10240\n"
    "v_add_u32_e32 v245, 0xffffa000, v245\n"
    "v_mfma_f32_16x16x32_bf16 v[188:191], v[196:199], v[100:103], v[188:191]\n"
    "ds_read_b64_tr_b16 v[220:221], v245\n"
    "ds_read_b64_tr_b16 v[222:223], v245 offset:2048\n"
    "v_mfma_f32_16x16x32_bf16 v[184:187], v[200:203], v[104:107], v[184:187]\n"
    "ds_read_b64_tr_b16 v[192:193], v245 offset:4096\n"
    "ds_read_b64_tr_b16 v[194:195], v245 offset:6144\n"
    "v_mfma_f32_16x16x32_bf16 v[188:191], v[200:203], v[108:111], v[188:191]\n"
    "ds_read_b64_tr_b16 v[196:197], v245 offset:8192\n"
    "s_waitcnt lgkmcnt(6)\n"
    "v_mfma_f32_16x16x32_bf16 v[184:187], v[204:207], v[112:115], v[184:187]\n"
    "ds_read_b64_tr_b16 v[198:199], v245 offset:10240\n"
    "v_add_u32_e32 v246, 0xffffa000, v246\n"
    "v_mfma_f32_16x16x32_bf16 v[188:191], v[204:207], v[116:119], v[188:191]\n"
    "ds_read_b64_tr_b16 v[200:201], v246\n"
    "ds_read_b64_tr_b16 v[202:203], v246 offset:2048\n"
    "v_mfma_f32_16x16x32_bf16 v[160:163], v[208:211], v[120:123], v[160:163]\n"
    "ds_read_b64_tr_b16 v[204:205], v246 offset:4096\n"
    "ds_read_b64_tr_b16 v[206:207], v246 offset:6144\n"
    "v_mfma_f32_16x16x32_bf16 v[164:167], v[208:211], v[124:127], v[164:167]\n"
    "ds_read_b64_tr_b16 v[208:209], v246 offset:8192\n"
    "ds_read_b64_tr_b16 v[210:211], v246 offset:10240\n"
    "v_mfma_f32_16x16x32_bf16 v[160:163], v[212:215], v[128:131], v[160:163]\n"
    "v_add_u32_e32 v247, 0xffffa000, v247\n"
    "ds_read_b128 v[0:3], v250\n"
    "v_mfma_f32_16x16x32_bf16 v[164:167], v[212:215], v[132:135], v[164:167]\n"
    "ds_read_b64_tr_b16 v[212:213], v247\n"
    "s_waitcnt lgkmcnt(6)\n"
    "v_mfma_f32_16x16x32_bf16 v[160:163], v[216:219], v[136:139], v[160:163]\n"
    "ds_read_b64_tr_b16 v[214:215], v247 offset:2048\n"
    "ds_read_b128 v[4:7], v251\n"
    "v_mfma_f32_16x16x32_bf16 v[164:167], v[216:219], v[140:143], v[164:167]\n"
    "ds_read_b64_tr_b16 v[216:217], v247 offset:4096\n"
    "ds_read_b64_tr_b16 v[218:219], v247 offset:6144\n"
    "v_mfma_f32_16x16x32_bf16 v[168:171], v[220:223], v[120:123], v[168:171]\n"
    "ds_read_b128 v[8:11], v250 offset:1024\n"
    "ds_read_b128 v[12:15], v251 offset:1024\n"
    "v_mfma_f32_16x16x32_bf16 v[172:175], v[220:223], v[124:127], v[172:175]\n"
    "ds_read_b64_tr_b16 v[220:221], v247 offset:8192\n"
    "ds_read_b64_tr_b16 v[222:223], v247 offset:10240\n"
    "v_mfma_f32_16x16x32_bf16 v[168:171], v[192:195], v[128:131], v[168:171]\n"
    "v_mfma_f32_16x16x32_bf16 v[172:175], v[192:195], v[132:135], v[172:175]\n"
    "v_cvt_pk_bf16_f32 v144, v160, v161\n"
    "v_cvt_pk_bf16_f32 v145, v162, v163\n"
    "v_mfma_f32_16x16x32_bf16 v[168:171], v[196:199], v[136:139], v[168:171]\n"
    "v_cvt_pk_bf16_f32 v148, v164, v165\n"
    "v_cvt_pk_bf16_f32 v149, v166, v167\n"
    "v_mfma_f32_16x16x32_bf16 v[172:175], v[196:199], v[140:143], v[172:175]\n"
    "s_waitcnt lgkmcnt(0)\n"
    "v_lshlrev_b32_e32 v160, 16, v0\n"
    "v_and_b32_e64 v161, s47, v0\n"
    "v_mfma_f32_16x16x32_bf16 v[176:179], v[200:203], v[120:123], v[176:179]\n"
    "v_lshlrev_b32_e32 v162, 16, v1\n"
    "v_and_b32_e32 v163, s47, v1\n"
    "v_mfma_f32_16x16x32_bf16 v[180:183], v[200:203], v[124:127], v[180:183]\n"
    "v_lshlrev_b32_e32 v164, 16, v4\n"
    "v_and_b32_e32 v165, s47, v4\n"
    "v_mfma_f32_16x16x32_bf16 v[176:179], v[204:207], v[128:131], v[176:179]\n"
    "v_lshlrev_b32_e32 v166, 16, v5\n"
    "v_and_b32_e32 v167, s47, v5\n"
    "v_mfma_f32_16x16x32_bf16 v[180:183], v[204:207], v[132:135], v[180:183]\n"
    "v_cvt_pk_bf16_f32 v146, v168, v169\n"
    "v_cvt_pk_bf16_f32 v147, v170, v171\n"
    "ds_read_b64_tr_b16 v[192:193], v244 offset:12288\n"
    "ds_read_b64_tr_b16 v[194:195], v244 offset:14336\n"
    "v_mfma_f32_16x16x32_bf16 v[176:179], v[208:211], v[136:139], v[176:179]\n"
    "ds_read_b64_tr_b16 v[196:197], v244 offset:16384\n"
    "ds_read_b64_tr_b16 v[198:199], v244 offset:18432\n"
    "v_mfma_f32_16x16x32_bf16 v[180:183], v[208:211], v[140:143], v[180:183]\n"
    "ds_read_b64_tr_b16 v[200:201], v244 offset:20480\n"
    "ds_read_b64_tr_b16 v[202:203], v244 offset:22528\n"
    "v_mfma_f32_16x16x32_bf16 v[184:187], v[212:215], v[120:123], v[184:187]\n"
    "ds_read_b64_tr_b16 v[204:205], v245 offset:12288\n"
    "ds_read_b64_tr_b16 v[206:207], v245 offset:14336\n"
    "v_mfma_f32_16x16x32_bf16 v[188:191], v[212:215], v[124:127], v[188:191]\n"
    "ds_read_b64_tr_b16 v[208:209], v245 offset:16384\n"
    "ds_read_b64_tr_b16 v[210:211], v245 offset:18432\n"
    "v_mfma_f32_16x16x32_bf16 v[184:187], v[216:219], v[128:131], v[184:187]\n"
    "ds_read_b64_tr_b16 v[212:213], v245 offset:20480\n"
    "ds_read_b64_tr_b16 v[214:215], v245 offset:22528\n"
    "v_mfma_f32_16x16x32_bf16 v[188:191], v[216:219], v[132:135], v[188:191]\n"
    "ds_read_b64_tr_b16 v[216:217], v246 offset:12288\n"
    "ds_read_b64_tr_b16 v[218:219], v246 offset:14336\n"
    "v_mfma_f32_16x16x32_bf16 v[184:187], v[220:223], v[136:139], v[184:187]\n"
    "v_cvt_pk_bf16_f32 v150, v172, v173\n"
    "v_cvt_pk_bf16_f32 v151, v174, v175\n"
    "v_mfma_f32_16x16x32_bf16 v[188:191], v[220:223], v[140:143], v[188:191]\n"
    "ds_read_b64_tr_b16 v[220:221], v246 offset:16384\n"
    "s_waitcnt lgkmcnt(5)\n"
    "v_mfma_f32_16x16x32_bf16 v[160:163], v[192:195], v[96:99], v[160:163]\n"
    "ds_read_b64_tr_b16 v[222:223], v246 offset:18432\n"
    "s_add_u32 s42, s87, 0x1c000\n"
    "v_mfma_f32_16x16x32_bf16 v[164:167], v[192:195], v[100:103], v[164:167]\n"
    "ds_read_b64_tr_b16 v[192:193], v246 offset:20480\n"
    "ds_read_b64_tr_b16 v[194:195], v246 offset:22528\n"
    "v_mfma_f32_16x16x32_bf16 v[160:163], v[196:199], v[104:107], v[160:163]\n"
    "s_cmp_eq_u32 s98, 2\n"
    "s_cselect_b32 s43, 2.0, s42\n"
    "buffer_store_dwordx4 v[148:151], v249, s[28:31], s43 offen sc1\n"
    "s_cselect_b64 exec, -1, 0\n"
    "ds_write_b128 v252, v[148:151] offset:45056\n"
    "s_mov_b64 exec, -1\n"
    "v_mfma_f32_16x16x32_bf16 v[164:167], v[196:199], v[108:111], v[164:167]\n"
    "ds_read_b64_tr_b16 v[196:197], v247 offset:12288\n"
    "ds_read_b64_tr_b16 v[198:199], v247 offset:14336\n"
    "s_cmp_eq_u32 s98, 0\n"
    "s_cselect_b32 s43, 2.0, s42\n"
    "buffer_store_dwordx4 v[144:147], v248, s[28:31], s43 offen sc1\n"
    "s_cselect_b64 exec, -1, 0\n"
    "ds_write_b128 v252, v[144:147] offset:45056\n"
    "s_mov_b64 exec, -1\n"
    "v_lshlrev_b32_e32 v168, 16, v2\n"
    "v_and_b32_e32 v169, s47, v2\n"
    "v_lshlrev_b32_e32 v170, 16, v3\n"
    "v_and_b32_e32 v171, s47, v3\n"
    "v_lshlrev_b32_e32 v172, 16, v6\n"
    "v_and_b32_e32 v173, s47, v6\n"
    "v_lshlrev_b32_e32 v174, 16, v7\n"
    "v_and_b32_e64 v175, s47, v7\n"
    "v_mfma_f32_16x16x32_bf16 v[160:163], v[200:203], v[112:115], v[160:163]\n"
    "v_cvt_pk_bf16_f32 v152, v176, v177\n"
    "v_cvt_pk_bf16_f32 v153, v178, v179\n"
    "v_mfma_f32_16x16x32_bf16 v[164:167], v[200:203], v[116:119], v[164:167]\n"
    "ds_read_b64_tr_b16 v[200:201], v247 offset:16384\n"
    "ds_read_b64_tr_b16 v[202:203], v247 offset:18432\n"
    "v_mfma_f32_16x16x32_bf16 v[168:171], v[204:207], v[96:99], v[168:171]\n"
    "v_cvt_pk_bf16_f32 v156, v180, v181\n"
    "v_cvt_pk_bf16_f32 v157, v182, v183\n"
    "v_mfma_f32_16x16x32_bf16 v[172:175], v[204:207], v[100:103], v[172:175]\n"
    "ds_read_b64_tr_b16 v[204:205], v247 offset:20480\n"
    "ds_read_b64_tr_b16 v[206:207], v247 offset:22528\n"
    "v_mfma_f32_16x16x32_bf16 v[168:171], v[208:211], v[104:107], v[168:171]\n"
    "v_lshlrev_b32_e32 v176, 16, v8\n"
    "v_and_b32_e32 v177, s47, v8\n"
    "v_mfma_f32_16x16x32_bf16 v[172:175], v[208:211], v[108:111], v[172:175]\n"
    "v_lshlrev_b32_e32 v178, 16, v9\n"
    "v_and_b32_e32 v179, s47, v9\n"
    "v_lshlrev_b32_e32 v180, 16, v12\n"
    "v_and_b32_e32 v181, s47, v12\n"
    "v_lshlrev_b32_e32 v182, 16, v13\n"
    "v_and_b32_e32 v183, s47, v13\n"
    "s_waitcnt lgkmcnt(2)\n"
    "v_mfma_f32_16x16x32_bf16 v[168:171], v[212:215], v[112:115], v[168:171]\n"
    "s_waitcnt vmcnt(2)\n"
    "v_add_u32_e32 v240, 1, v240\n"
    "s_mov_b64 exec, s[74:75]\n"
    "buffer_atomic_swap v240, v242, s[28:31], s94 offen\n"
    "s_mov_b64 exec, -1\n"
    "v_mfma_f32_16x16x32_bf16 v[172:175], v[212:215], v[116:119], v[172:175]\n"
    "v_cvt_pk_bf16_f32 v154, v184, v185\n"
    "v_cvt_pk_bf16_f32 v155, v186, v187\n"
    "v_mfma_f32_16x16x32_bf16 v[176:179], v[216:219], v[96:99], v[176:179]\n"
    "v_cvt_pk_bf16_f32 v158, v188, v189\n"
    "v_cvt_pk_bf16_f32 v159, v190, v191\n"
    "v_mfma_f32_16x16x32_bf16 v[180:183], v[216:219], v[100:103], v[180:183]\n"
    "s_cmp_eq_u32 s98, 3\n"
    "s_cselect_b32 s43, 2.0, s42\n"
    "buffer_store_dwordx4 v[156:159], v249, s[28:31], s43 offen offset:1024 sc1\n"
    "s_cselect_b64 exec, -1, 0\n"
    "ds_write_b128 v252, v[156:159] offset:45056\n"
    "s_mov_b64 exec, -1\n"
    "v_mfma_f32_16x16x32_bf16 v[176:179], v[220:223], v[104:107], v[176:179]\n"
    "s_cmp_eq_u32 s98, 1\n"
    "s_cselect_b32 s43, 2.0, s42\n"
    "buffer_store_dwordx4 v[152:155], v248, s[28:31], s43 offen offset:1024 sc1\n"
    "s_cselect_b64 exec, -1, 0\n"
    "ds_write_b128 v252, v[152:155] offset:45056\n"
    "s_mov_b64 exec, -1\n"
    "v_mfma_f32_16x16x32_bf16 v[180:183], v[220:223], v[108:111], v[180:183]\n"
    "v_lshlrev_b32_e32 v184, 16, v10\n"
    "v_and_b32_e64 v185, s47, v10\n"
    "v_add_u32_e32 v244, 0xffff4000, v244\n"
    "ds_read_b64_tr_b16 v[208:209], v244\n"
    "v_lshlrev_b32_e32 v186, 16, v11\n"
    "v_and_b32_e32 v187, s47, v11\n"
    "v_lshlrev_b32_e32 v188, 16, v14\n"
    "v_and_b32_e32 v189, s47, v14\n"
    "v_lshlrev_b32_e32 v190, 16, v15\n"
    "v_and_b32_e32 v191, s47, v15\n"
    "v_mfma_f32_16x16x32_bf16 v[176:179], v[192:195], v[112:115], v[176:179]\n"
    "ds_read_b64_tr_b16 v[210:211], v244 offset:2048\n"
    "ds_read_b64_tr_b16 v[212:213], v244 offset:4096\n"
    "v_mfma_f32_16x16x32_bf16 v[180:183], v[192:195], v[116:119], v[180:183]\n"
    "ds_read_b64_tr_b16 v[214:215], v244 offset:6144\n"
    "ds_read_b64_tr_b16 v[216:217], v244 offset:8192\n"
    "v_mfma_f32_16x16x32_bf16 v[184:187], v[196:199], v[96:99], v[184:187]\n"
    "ds_read_b64_tr_b16 v[218:219], v244 offset:10240\n"
    "v_add_u32_e32 v245, 0xffff4000, v245\n"
    "v_mfma_f32_16x16x32_bf16 v[188:191], v[196:199], v[100:103], v[188:191]\n"
    "ds_read_b64_tr_b16 v[220:221], v245\n"
    "ds_read_b64_tr_b16 v[222:223], v245 offset:2048\n"
    "v_mfma_f32_16x16x32_bf16 v[184:187], v[200:203], v[104:107], v[184:187]\n"
    "ds_read_b64_tr_b16 v[192:193], v245 offset:4096\n"
    "ds_read_b64_tr_b16 v[194:195], v245 offset:6144\n"
    "v_mfma_f32_16x16x32_bf16 v[188:191], v[200:203], v[108:111], v[188:191]\n"
    "ds_read_b64_tr_b16 v[196:197], v245 offset:8192\n"
    "s_waitcnt lgkmcnt(6)\n"
    "v_mfma_f32_16x16x32_bf16 v[184:187], v[204:207], v[112:115], v[184:187]\n"
    "ds_read_b64_tr_b16 v[198:199], v245 offset:10240\n"
    "v_add_u32_e32 v246, 0xffff4000, v246\n"
    "v_mfma_f32_16x16x32_bf16 v[188:191], v[204:207], v[116:119], v[188:191]\n"
    "ds_read_b64_tr_b16 v[200:201], v246\n"
    "ds_read_b64_tr_b16 v[202:203], v246 offset:2048\n"
    "v_mfma_f32_16x16x32_bf16 v[160:163], v[208:211], v[120:123], v[160:163]\n"
    "ds_read_b64_tr_b16 v[204:205], v246 offset:4096\n"
    "ds_read_b64_tr_b16 v[206:207], v246 offset:6144\n"
    "v_mfma_f32_16x16x32_bf16 v[164:167], v[208:211], v[124:127], v[164:167]\n"
    "ds_read_b64_tr_b16 v[208:209], v246 offset:8192\n"
    "ds_read_b64_tr_b16 v[210:211], v246 offset:10240\n"
    "v_mfma_f32_16x16x32_bf16 v[160:163], v[212:215], v[128:131], v[160:163]\n"
    "v_add_u32_e32 v247, 0xffff4000, v247\n"
    "ds_read_b128 v[16:19], v250 offset:14336\n"
    "v_mfma_f32_16x16x32_bf16 v[164:167], v[212:215], v[132:135], v[164:167]\n"
    "ds_read_b64_tr_b16 v[212:213], v247\n"
    "s_waitcnt lgkmcnt(6)\n"
    "v_mfma_f32_16x16x32_bf16 v[160:163], v[216:219], v[136:139], v[160:163]\n"
    "ds_read_b64_tr_b16 v[214:215], v247 offset:2048\n"
    "ds_read_b128 v[20:23], v251 offset:14336\n"
    "v_mfma_f32_16x16x32_bf16 v[164:167], v[216:219], v[140:143], v[164:167]\n"
    "ds_read_b64_tr_b16 v[216:217], v247 offset:4096\n"
    "ds_read_b64_tr_b16 v[218:219], v247 offset:6144\n"
    "v_mfma_f32_16x16x32_bf16 v[168:171], v[220:223], v[120:123], v[168:171]\n"
    "ds_read_b128 v[24:27], v250 offset:15360\n"
    "ds_read_b128 v[28:31], v251 offset:15360\n"
    "v_mfma_f32_16x16x32_bf16 v[172:175], v[220:223], v[124:127], v[172:175]\n"
    "ds_read_b64_tr_b16 v[220:221], v247 offset:8192\n"
    "ds_read_b64_tr_b16 v[222:223], v247 offset:10240\n"
    "v_mfma_f32_16x16x32_bf16 v[168:171], v[192:195], v[128:131], v[168:171]\n"
    "v_mfma_f32_16x16x32_bf16 v[172:175], v[192:195], v[132:135], v[172:175]\n"
    "v_cvt_pk_bf16_f32 v0, v160, v161\n"
    "v_cvt_pk_bf16_f32 v1, v162, v163\n"
    "v_mfma_f32_16x16x32_bf16 v[168:171], v[196:199], v[136:139], v[168:171]\n"
    "v_cvt_pk_bf16_f32 v4, v164, v165\n"
    "v_cvt_pk_bf16_f32 v5, v166, v167\n"
    "v_mfma_f32_16x16x32_bf16 v[172:175], v[196:199], v[140:143], v[172:175]\n"
    "s_waitcnt lgkmcnt(0)\n"
    "v_lshlrev_b32_e32 v160, 16, v16\n"
    "v_and_b32_e64 v161, s47, v16\n"
    "v_mfma_f32_16x16x32_bf16 v[176:179], v[200:203], v[120:123], v[176:179]\n"
    "v_lshlrev_b32_e32 v162, 16, v17\n"
    "v_and_b32_e32 v163, s47, v17\n"
    "v_mfma_f32_16x16x32_bf16 v[180:183], v[200:203], v[124:127], v[180:183]\n"
    "v_lshlrev_b32_e32 v164, 16, v20\n"
    "v_and_b32_e32 v165, s47, v20\n"
    "v_mfma_f32_16x16x32_bf16 v[176:179], v[204:207], v[128:131], v[176:179]\n"
    "v_lshlrev_b32_e32 v166, 16, v21\n"
    "v_and_b32_e32 v167, s47, v21\n"
    "v_mfma_f32_16x16x32_bf16 v[180:183], v[204:207], v[132:135], v[180:183]\n"
    "v_cvt_pk_bf16_f32 v2, v168, v169\n"
    "v_cvt_pk_bf16_f32 v3, v170, v171\n"
    "ds_read_b64_tr_b16 v[192:193], v244 offset:12288\n"
    "ds_read_b64_tr_b16 v[194:195], v244 offset:14336\n"
    "v_mfma_f32_16x16x32_bf16 v[176:179], v[208:211], v[136:139], v[176:179]\n"
    "ds_read_b64_tr_b16 v[196:197], v244 offset:16384\n"
    "ds_read_b64_tr_b16 v[198:199], v244 offset:18432\n"
    "v_mfma_f32_16x16x32_bf16 v[180:183], v[208:211], v[140:143], v[180:183]\n"
    "ds_read_b64_tr_b16 v[200:201], v244 offset:20480\n"
    "ds_read_b64_tr_b16 v[202:203], v244 offset:22528\n"
    "v_mfma_f32_16x16x32_bf16 v[184:187], v[212:215], v[120:123], v[184:187]\n"
    "ds_read_b64_tr_b16 v[204:205], v245 offset:12288\n"
    "ds_read_b64_tr_b16 v[206:207], v245 offset:14336\n"
    "v_mfma_f32_16x16x32_bf16 v[188:191], v[212:215], v[124:127], v[188:191]\n"
    "ds_read_b64_tr_b16 v[208:209], v245 offset:16384\n"
    "ds_read_b64_tr_b16 v[210:211], v245 offset:18432\n"
    "v_mfma_f32_16x16x32_bf16 v[184:187], v[216:219], v[128:131], v[184:187]\n"
    "ds_read_b64_tr_b16 v[212:213], v245 offset:20480\n"
    "ds_read_b64_tr_b16 v[214:215], v245 offset:22528\n"
    "v_mfma_f32_16x16x32_bf16 v[188:191], v[216:219], v[132:135], v[188:191]\n"
    "ds_read_b64_tr_b16 v[216:217], v246 offset:12288\n"
    "ds_read_b64_tr_b16 v[218:219], v246 offset:14336\n"
    "v_mfma_f32_16x16x32_bf16 v[184:187], v[220:223], v[136:139], v[184:187]\n"
    "v_cvt_pk_bf16_f32 v6, v172, v173\n"
    "v_cvt_pk_bf16_f32 v7, v174, v175\n"
    "v_mfma_f32_16x16x32_bf16 v[188:191], v[220:223], v[140:143], v[188:191]\n"
    "ds_read_b64_tr_b16 v[220:221], v246 offset:16384\n"
    "s_waitcnt lgkmcnt(5)\n"
    "v_mfma_f32_16x16x32_bf16 v[160:163], v[192:195], v[96:99], v[160:163]\n"
    "ds_read_b64_tr_b16 v[222:223], v246 offset:18432\n"
    "s_add_u32 s42, s87, 0x0\n"
    "v_mfma_f32_16x16x32_bf16 v[164:167], v[192:195], v[100:103], v[164:167]\n"
    "ds_read_b64_tr_b16 v[192:193], v246 offset:20480\n"
    "ds_read_b64_tr_b16 v[194:195], v246 offset:22528\n"
    "v_mfma_f32_16x16x32_bf16 v[160:163], v[196:199], v[104:107], v[160:163]\n"
    "s_cmp_eq_u32 s98, 2\n"
    "s_cselect_b32 s43, 2.0, s42\n"
    "buffer_store_dwordx4 v[4:7], v249, s[28:31], s43 offen sc1\n"
    "s_cselect_b64 exec, -1, 0\n"
    "ds_write_b128 v252, v[4:7] offset:46080\n"
    "s_mov_b64 exec, -1\n"
    "v_mfma_f32_16x16x32_bf16 v[164:167], v[196:199], v[108:111], v[164:167]\n"
    "ds_read_b64_tr_b16 v[196:197], v247 offset:12288\n"
    "ds_read_b64_tr_b16 v[198:199], v247 offset:14336\n"
    "s_cmp_eq_u32 s98, 0\n"
    "s_cselect_b32 s43, 2.0, s42\n"
    "buffer_store_dwordx4 v[0:3], v248, s[28:31], s43 offen sc1\n"
    "s_cselect_b64 exec, -1, 0\n"
    "ds_write_b128 v252, v[0:3] offset:46080\n"
    "s_mov_b64 exec, -1\n"
    "v_lshlrev_b32_e32 v168, 16, v18\n"
    "v_and_b32_e32 v169, s47, v18\n"
    "v_lshlrev_b32_e32 v170, 16, v19\n"
    "v_and_b32_e32 v171, s47, v19\n"
    "v_lshlrev_b32_e32 v172, 16, v22\n"
    "v_and_b32_e32 v173, s47, v22\n"
    "v_lshlrev_b32_e32 v174, 16, v23\n"
    "v_and_b32_e32 v175, s47, v23\n"
    "v_mfma_f32_16x16x32_bf16 v[160:163], v[200:203], v[112:115], v[160:163]\n"
    "v_cvt_pk_bf16_f32 v8, v176, v177\n"
    "v_cvt_pk_bf16_f32 v9, v178, v179\n"
    "v_mfma_f32_16x16x32_bf16 v[164:167], v[200:203], v[116:119], v[164:167]\n"
    "ds_read_b64_tr_b16 v[200:201], v247 offset:16384\n"
    "ds_read_b64_tr_b16 v[202:203], v247 offset:18432\n"
    "v_mfma_f32_16x16x32_bf16 v[168:171], v[204:207], v[96:99], v[168:171]\n"
    "v_cvt_pk_bf16_f32 v12, v180, v181\n"
    "v_cvt_pk_bf16_f32 v13, v182, v183\n"
    "v_mfma_f32_16x16x32_bf16 v[172:175], v[204:207], v[100:103], v[172:175]\n"
    "ds_read_b64_tr_b16 v[204:205], v247 offset:20480\n"
    "ds_read_b64_tr_b16 v[206:207], v247 offset:22528\n"
    "v_mfma_f32_16x16x32_bf16 v[168:171], v[208:211], v[104:107], v[168:171]\n"
    "v_lshlrev_b32_e32 v176, 16, v24\n"
    "v_and_b32_e32 v177, s47, v24\n"
    "v_mfma_f32_16x16x32_bf16 v[172:175], v[208:211], v[108:111], v[172:175]\n"
    "v_lshlrev_b32_e32 v178, 16, v25\n"
    "v_and_b32_e32 v179, s47, v25\n"
    "v_lshlrev_b32_e32 v180, 16, v28\n"
    "v_and_b32_e32 v181, s47, v28\n"
    "v_lshlrev_b32_e32 v182, 16, v29\n"
    "v_and_b32_e32 v183, s47, v29\n"
    "s_waitcnt lgkmcnt(2)\n"
    "v_mfma_f32_16x16x32_bf16 v[168:171], v[212:215], v[112:115], v[168:171]\n"
    "s_waitcnt vmcnt(2)\n"
    "v_add_u32_e32 v240, 1, v240\n"
    "s_mov_b64 exec, s[74:75]\n"
    "buffer_atomic_swap v240, v242, s[28:31], s94 offen\n"
    "s_mov_b64 exec, -1\n"
    "v_mfma_f32_16x16x32_bf16 v[172:175], v[212:215], v[116:119], v[172:175]\n"
    "v_cvt_pk_bf16_f32 v10, v184, v185\n"
    "v_cvt_pk_bf16_f32 v11, v186, v187\n"
    "v_mfma_f32_16x16x32_bf16 v[176:179], v[216:219], v[96:99], v[176:179]\n"
    "v_cvt_pk_bf16_f32 v14, v188, v189\n"
    "v_cvt_pk_bf16_f32 v15, v190, v191\n"
    "v_mfma_f32_16x16x32_bf16 v[180:183], v[216:219], v[100:103], v[180:183]\n"
    "s_cmp_eq_u32 s98, 3\n"
    "s_cselect_b32 s43, 2.0, s42\n"
    "buffer_store_dwordx4 v[12:15], v249, s[28:31], s43 offen offset:1024 sc1\n"
    "s_cselect_b64 exec, -1, 0\n"
    "ds_write_b128 v252, v[12:15] offset:46080\n"
    "s_mov_b64 exec, -1\n"
    "v_mfma_f32_16x16x32_bf16 v[176:179], v[220:223], v[104:107], v[176:179]\n"
    "s_cmp_eq_u32 s98, 1\n"
    "s_cselect_b32 s43, 2.0, s42\n"
    "buffer_store_dwordx4 v[8:11], v248, s[28:31], s43 offen offset:1024 sc1\n"
    "s_cselect_b64 exec, -1, 0\n"
    "ds_write_b128 v252, v[8:11] offset:46080\n"
    "s_mov_b64 exec, -1\n"
    "v_mfma_f32_16x16x32_bf16 v[180:183], v[220:223], v[108:111], v[180:183]\n"
    "v_lshlrev_b32_e32 v184, 16, v26\n"
    "v_and_b32_e64 v185, s47, v26\n"
    "ds_read_b64_tr_b16 v[208:209], v244 offset:24576\n"
    "ds_read_b64_tr_b16 v[210:211], v244 offset:26624\n"
    "v_lshlrev_b32_e32 v186, 16, v27\n"
    "v_and_b32_e32 v187, s47, v27\n"
    "v_lshlrev_b32_e32 v188, 16, v30\n"
    "v_and_b32_e32 v189, s47, v30\n"
    "v_lshlrev_b32_e32 v190, 16, v31\n"
    "v_and_b32_e32 v191, s47, v31\n"
    "v_mfma_f32_16x16x32_bf16 v[176:179], v[192:195], v[112:115], v[176:179]\n"
    "ds_read_b64_tr_b16 v[212:213], v244 offset:28672\n"
    "ds_read_b64_tr_b16 v[214:215], v244 offset:30720\n"
    "v_mfma_f32_16x16x32_bf16 v[180:183], v[192:195], v[116:119], v[180:183]\n"
    "ds_read_b64_tr_b16 v[216:217], v244 offset:32768\n"
    "ds_read_b64_tr_b16 v[218:219], v244 offset:34816\n"
    "v_mfma_f32_16x16x32_bf16 v[184:187], v[196:199], v[96:99], v[184:187]\n"
    "ds_read_b64_tr_b16 v[220:221], v245 offset:24576\n"
    "ds_read_b64_tr_b16 v[222:223], v245 offset:26624\n"
    "v_mfma_f32_16x16x32_bf16 v[188:191], v[196:199], v[100:103], v[188:191]\n"
    "ds_read_b64_tr_b16 v[192:193], v245 offset:28672\n"
    "ds_read_b64_tr_b16 v[194:195], v245 offset:30720\n"
    "v_mfma_f32_16x16x32_bf16 v[184:187], v[200:203], v[104:107], v[184:187]\n"
    "ds_read_b64_tr_b16 v[196:197], v245 offset:32768\n"
    "ds_read_b64_tr_b16 v[198:199], v245 offset:34816\n"
    "v_mfma_f32_16x16x32_bf16 v[188:191], v[200:203], v[108:111], v[188:191]\n"
    "ds_read_b64_tr_b16 v[200:201], v246 offset:24576\n"
    "s_waitcnt lgkmcnt(7)\n"
    "v_mfma_f32_16x16x32_bf16 v[184:187], v[204:207], v[112:115], v[184:187]\n"
    "ds_read_b64_tr_b16 v[202:203], v246 offset:26624\n"
    "v_mfma_f32_16x16x32_bf16 v[188:191], v[204:207], v[116:119], v[188:191]\n"
    "ds_read_b64_tr_b16 v[204:205], v246 offset:28672\n"
    "ds_read_b64_tr_b16 v[206:207], v246 offset:30720\n"
    "v_mfma_f32_16x16x32_bf16 v[160:163], v[208:211], v[120:123], v[160:163]\n"
    "v_mfma_f32_16x16x32_bf16 v[164:167], v[208:211], v[124:127], v[164:167]\n"
    "ds_read_b64_tr_b16 v[208:209], v246 offset:32768\n"
    "ds_read_b64_tr_b16 v[210:211], v246 offset:34816\n"
    "v_mfma_f32_16x16x32_bf16 v[160:163], v[212:215], v[128:131], v[160:163]\n"
    "v_mfma_f32_16x16x32_bf16 v[164:167], v[212:215], v[132:135], v[164:167]\n"
    "ds_read_b64_tr_b16 v[212:213], v247 offset:24576\n"
    "ds_read_b64_tr_b16 v[214:215], v247 offset:26624\n"
    "v_mfma_f32_16x16x32_bf16 v[160:163], v[216:219], v[136:139], v[160:163]\n"
    "v_mfma_f32_16x16x32_bf16 v[164:167], v[216:219], v[140:143], v[164:167]\n"
    "ds_read_b64_tr_b16 v[216:217], v247 offset:28672\n"
    "s_waitcnt lgkmcnt(4)\n"
    "v_mfma_f32_16x16x32_bf16 v[168:171], v[220:223], v[120:123], v[168:171]\n"
    "ds_read_b64_tr_b16 v[218:219], v247 offset:30720\n"
    "v_mfma_f32_16x16x32_bf16 v[172:175], v[220:223], v[124:127], v[172:175]\n"
    "ds_read_b64_tr_b16 v[220:221], v247 offset:32768\n"
    "ds_read_b64_tr_b16 v[222:223], v247 offset:34816\n"
    "v_mfma_f32_16x16x32_bf16 v[168:171], v[192:195], v[128:131], v[168:171]\n"
    "v_mfma_f32_16x16x32_bf16 v[172:175], v[192:195], v[132:135], v[172:175]\n"
    "v_cvt_pk_bf16_f32 v16, v160, v161\n"
    "v_cvt_pk_bf16_f32 v17, v162, v163\n"
    "v_mfma_f32_16x16x32_bf16 v[168:171], v[196:199], v[136:139], v[168:171]\n"
    "v_cvt_pk_bf16_f32 v20, v164, v165\n"
    "v_cvt_pk_bf16_f32 v21, v166, v167\n"
    "v_mfma_f32_16x16x32_bf16 v[172:175], v[196:199], v[140:143], v[172:175]\n"
    "v_mfma_f32_16x16x32_bf16 v[176:179], v[200:203], v[120:123], v[176:179]\n"
    "v_mfma_f32_16x16x32_bf16 v[180:183], v[200:203], v[124:127], v[180:183]\n"
    "v_mfma_f32_16x16x32_bf16 v[176:179], v[204:207], v[128:131], v[176:179]\n"
    "v_mfma_f32_16x16x32_bf16 v[180:183], v[204:207], v[132:135], v[180:183]\n"
    "s_nop 0\n"
    "v_cvt_pk_bf16_f32 v18, v168, v169\n"
    "v_cvt_pk_bf16_f32 v19, v170, v171\n"
    "s_waitcnt lgkmcnt(0)\n"
    "v_mfma_f32_16x16x32_bf16 v[176:179], v[208:211], v[136:139], v[176:179]\n"
    "v_cvt_pk_bf16_f32 v22, v172, v173\n"
    "v_cvt_pk_bf16_f32 v23, v174, v175\n"
    "v_mfma_f32_16x16x32_bf16 v[180:183], v[208:211], v[140:143], v[180:183]\n"
    "s_add_u32 s42, s87, 0x3800\n"
    "v_mfma_f32_16x16x32_bf16 v[184:187], v[212:215], v[120:123], v[184:187]\n"
    "s_cmp_eq_u32 s98, 2\n"
    "s_cselect_b32 s43, 2.0, s42\n"
    "buffer_store_dwordx4 v[20:23], v249, s[28:31], s43 offen sc1\n"
    "s_cselect_b64 exec, -1, 0\n"
    "ds_write_b128 v252, v[20:23] offset:47104\n"
    "s_mov_b64 exec, -1\n"
    "v_mfma_f32_16x16x32_bf16 v[188:191], v[212:215], v[124:127], v[188:191]\n"
    "s_cmp_eq_u32 s98, 0\n"
    "s_cselect_b32 s43, 2.0, s42\n"
    "buffer_store_dwordx4 v[16:19], v248, s[28:31], s43 offen sc1\n"
    "s_cselect_b64 exec, -1, 0\n"
    "ds_write_b128 v252, v[16:19] offset:47104\n"
    "s_mov_b64 exec, -1\n"
    "v_mfma_f32_16x16x32_bf16 v[184:187], v[216:219], v[128:131], v[184:187]\n"
    "v_mfma_f32_16x16x32_bf16 v[188:191], v[216:219], v[132:135], v[188:191]\n"
    "v_cvt_pk_bf16_f32 v24, v176, v177\n"
    "v_cvt_pk_bf16_f32 v25, v178, v179\n"
    "v_mfma_f32_16x16x32_bf16 v[184:187], v[220:223], v[136:139], v[184:187]\n"
    "v_cvt_pk_bf16_f32 v28, v180, v181\n"
    "v_cvt_pk_bf16_f32 v29, v182, v183\n"
    "v_mfma_f32_16x16x32_bf16 v[188:191], v[220:223], v[140:143], v[188:191]\n"
    "s_waitcnt vmcnt(2)\n"
    "v_add_u32_e32 v240, 1, v240\n"
    "s_mov_b64 exec, s[74:75]\n"
    "buffer_atomic_swap v240, v242, s[28:31], s94 offen\n"
    "s_mov_b64 exec, -1\n"
    "v_cvt_pk_bf16_f32 v26, v184, v185\n"
    "v_cvt_pk_bf16_f32 v27, v186, v187\n"
    "s_nop 0\n"
    "v_cvt_pk_bf16_f32 v30, v188, v189\n"
    "v_cvt_pk_bf16_f32 v31, v190, v191\n"
    "s_cmp_eq_u32 s98, 3\n"
    "s_cselect_b32 s43, 2.0, s42\n"
    "buffer_store_dwordx4 v[28:31], v249, s[28:31], s43 offen offset:1024 sc1\n"
    "s_cselect_b64 exec, -1, 0\n"
    "ds_write_b128 v252, v[28:31] offset:47104\n"
    "s_mov_b64 exec, -1\n"
    "s_cmp_eq_u32 s98, 1\n"
    "s_cselect_b32 s43, 2.0, s42\n"
    "buffer_store_dwordx4 v[24:27], v248, s[28:31], s43 offen offset:1024 sc1\n"
    "s_cselect_b64 exec, -1, 0\n"
    "ds_write_b128 v252, v[24:27] offset:47104\n"
    "s_mov_b64 exec, -1\n"
    "s_waitcnt vmcnt(0) lgkmcnt(0)\n"
    "v_add_u32_e32 v240, 1, v240\n"
    "s_mov_b64 exec, s[74:75]\n"
    "buffer_atomic_swap v240, v242, s[28:31], s94 offen\n"
    "s_mov_b64 exec, -1\n"
    "v_readlane_b32 s95, v240, s86\n"
    "s_mov_b32 s58, 0x232000\n"
    "s_mov_b64 exec, 0xffff\n"
    "s_nop 2\n"
    "s_sub_u32 s95, s95, 9\n"
    ".Lfz_AV:\n"
    "v_mov_b32_e32 v0, 0\n"
    "buffer_atomic_or v0, v243, s[28:31], s92 offen sc0\n"
    "s_waitcnt vmcnt(0)\n"
    "buffer_load_dword v1, v243, s[28:31], s58 offen sc1\n"
    "v_subrev_u32_e32 v0, s95, v0\n"
    "v_cmp_le_u32_e64 s[98:99], 10, v0\n"
    "s_sub_u32 s96, s96, 1\n"
    "s_waitcnt vmcnt(0)\n"
    "v_and_b32_e32 v1, 0xffffff00, v1\n"
    "v_add_u32_e32 v1, 10, v1\n"
    "v_cmp_ne_u32_e32 vcc, v1, v240\n"
    "v_mov_b32_e32 v240, v1\n"
    "s_nop 3\n"
    "s_cbranch_vccz .Lfz_AW\n"
    "s_mov_b64 exec, s[74:75]\n"
    "buffer_atomic_swap v240, v242, s[28:31], s94 offen\n"
    "s_mov_b64 exec, 0xffff\n"
    ".Lfz_AW:\n"
    "s_cmp_eq_u32 s96, 0\n"
    "s_cselect_b64 s[98:99], 0, s[98:99]\n"
    "s_cmp_lg_u64 s[98:99], 0\n"
    "s_cbranch_scc0 .Lfz_END\n"
    "s_sleep 16\n"
    "s_branch .Lfz_AV\n"
    ".Lfz_END:\n"
    "s_mov_b64 exec, -1\n"
    "s_branch .Lfz_EXIT\n"
    ".Lfz_R1:\n"
    "s_nop 7\n"
    "v_mov_b32_e32 v228, v226\n"
    "v_mov_b32_e32 v229, v227\n"
    "s_nop 0\n"
    "v_permlane32_swap_b32_e32 v226, v228\n"
    "v_permlane32_swap_b32_e32 v227, v229\n"
    "v_max_f32_e32 v226, v226, v228\n"
    "v_max_f32_e32 v227, v227, v229\n"
    "v_mov_b32_e32 v228, v226\n"
    "v_mov_b32_e32 v229, v227\n"
    "s_nop 0\n"
    "v_permlane16_swap_b32_e32 v226, v228\n"
    "v_permlane16_swap_b32_e32 v227, v229\n"
    "v_max_f32_e32 v226, v226, v228\n"
    "v_max_f32_e32 v227, v227, v229\n"
    "v_max_f32_e32 v226, 0, v226\n"
    "v_max_f32_e64 v227, 0, v227\n"
    "v_exp_f32_e64 v228, -v226\n"
    "v_exp_f32_e64 v230, -v227\n"
    "v_sub_f32_e32 v144, v144, v226\n"
    "v_sub_f32_e32 v145, v145, v226\n"
    "v_sub_f32_e32 v146, v146, v226\n"
    "v_sub_f32_e32 v147, v147, v226\n"
    "v_sub_f32_e32 v152, v152, v226\n"
    "v_sub_f32_e32 v153, v153, v226\n"
    "v_sub_f32_e32 v154, v154, v226\n"
    "v_sub_f32_e32 v155, v155, v226\n"
    "v_sub_f32_e32 v216, v216, v226\n"
    "v_sub_f32_e32 v217, v217, v226\n"
    "v_sub_f32_e32 v218, v218, v226\n"
    "v_sub_f32_e32 v219, v219, v226\n"
    "v_mul_f32_e64 v224, v224, v228\n"
    "v_pk_mul_f32 v[0:1], v[0:1], v[228:229] op_sel_hi:[1,0]\n"
    "v_pk_mul_f32 v[2:3], v[2:3], v[228:229] op_sel_hi:[1,0]\n"
    "v_pk_mul_f32 v[8:9], v[8:9], v[228:229] op_sel_hi:[1,0]\n"
    "v_pk_mul_f32 v[10:11], v[10:11], v[228:229] op_sel_hi:[1,0]\n"
    "v_pk_mul_f32 v[16:17], v[16:17], v[228:229] op_sel_hi:[1,0]\n"
    "v_pk_mul_f32 v[18:19], v[18:19], v[228:229] op_sel_hi:[1,0]\n"
    "v_pk_mul_f32 v[24:25], v[24:25], v[228:229] op_sel_hi:[1,0]\n"
    "v_pk_mul_f32 v[26:27], v[26:27], v[228:229] op_sel_hi:[1,0]\n"
    "v_pk_mul_f32 v[32:33], v[32:33], v[228:229] op_sel_hi:[1,0]\n"
    "v_pk_mul_f32 v[34:35], v[34:35], v[228:229] op_sel_hi:[1,0]\n"
    "v_pk_mul_f32 v[40:41], v[40:41], v[228:229] op_sel_hi:[1,0]\n"
    "v_pk_mul_f32 v[42:43], v[42:43], v[228:229] op_sel_hi:[1,0]\n"
    "v_pk_mul_f32 v[48:49], v[48:49], v[228:229] op_sel_hi:[1,0]\n"
    "v_pk_mul_f32 v[50:51], v[50:51], v[228:229] op_sel_hi:[1,0]\n"
    "v_pk_mul_f32 v[56:57], v[56:57], v[228:229] op_sel_hi:[1,0]\n"
    "v_pk_mul_f32 v[58:59], v[58:59], v[228:229] op_sel_hi:[1,0]\n"
    "v_pk_mul_f32 v[64:65], v[64:65], v[228:229] op_sel_hi:[1,0]\n"
    "v_pk_mul_f32 v[66:67], v[66:67], v[228:229] op_sel_hi:[1,0]\n"
    "v_pk_mul_f32 v[72:73], v[72:73], v[228:229] op_sel_hi:[1,0]\n"
    "v_pk_mul_f32 v[74:75], v[74:75], v[228:229] op_sel_hi:[1,0]\n"
    "v_pk_mul_f32 v[80:81], v[80:81], v[228:229] op_sel_hi:[1,0]\n"
    "v_pk_mul_f32 v[82:83], v[82:83], v[228:229] op_sel_hi:[1,0]\n"
    "v_pk_mul_f32 v[88:89], v[88:89], v[228:229] op_sel_hi:[1,0]\n"
    "v_pk_mul_f32 v[90:91], v[90:91], v[228:229] op_sel_hi:[1,0]\n"
    "v_sub_f32_e32 v148, v148, v227\n"
    "v_sub_f32_e32 v149, v149, v227\n"
    "v_sub_f32_e32 v150, v150, v227\n"
    "v_sub_f32_e32 v151, v151, v227\n"
    "v_sub_f32_e32 v156, v156, v227\n"
    "v_sub_f32_e32 v157, v157, v227\n"
    "v_sub_f32_e32 v158, v158, v227\n"
    "v_sub_f32_e32 v159, v159, v227\n"
    "v_sub_f32_e32 v220, v220, v227\n"
    "v_sub_f32_e32 v221, v221, v227\n"
    "v_sub_f32_e32 v222, v222, v227\n"
    "v_sub_f32_e32 v223, v223, v227\n"
    "v_mul_f32_e64 v225, v225, v230\n"
    "v_pk_mul_f32 v[4:5], v[4:5], v[230:231] op_sel_hi:[1,0]\n"
    "v_pk_mul_f32 v[6:7], v[6:7], v[230:231] op_sel_hi:[1,0]\n"
    "v_pk_mul_f32 v[12:13], v[12:13], v[230:231] op_sel_hi:[1,0]\n"
    "v_pk_mul_f32 v[14:15], v[14:15], v[230:231] op_sel_hi:[1,0]\n"
    "v_pk_mul_f32 v[20:21], v[20:21], v[230:231] op_sel_hi:[1,0]\n"
    "v_pk_mul_f32 v[22:23], v[22:23], v[230:231] op_sel_hi:[1,0]\n"
    "v_pk_mul_f32 v[28:29], v[28:29], v[230:231] op_sel_hi:[1,0]\n"
    "v_pk_mul_f32 v[30:31], v[30:31], v[230:231] op_sel_hi:[1,0]\n"
    "v_pk_mul_f32 v[36:37], v[36:37], v[230:231] op_sel_hi:[1,0]\n"
    "v_pk_mul_f32 v[38:39], v[38:39], v[230:231] op_sel_hi:[1,0]\n"
    "v_pk_mul_f32 v[44:45], v[44:45], v[230:231] op_sel_hi:[1,0]\n"
    "v_pk_mul_f32 v[46:47], v[46:47], v[230:231] op_sel_hi:[1,0]\n"
    "v_pk_mul_f32 v[52:53], v[52:53], v[230:231] op_sel_hi:[1,0]\n"
    "v_pk_mul_f32 v[54:55], v[54:55], v[230:231] op_sel_hi:[1,0]\n"
    "v_pk_mul_f32 v[60:61], v[60:61], v[230:231] op_sel_hi:[1,0]\n"
    "v_pk_mul_f32 v[62:63], v[62:63], v[230:231] op_sel_hi:[1,0]\n"
    "v_pk_mul_f32 v[68:69], v[68:69], v[230:231] op_sel_hi:[1,0]\n"
    "v_pk_mul_f32 v[70:71], v[70:71], v[230:231] op_sel_hi:[1,0]\n"
    "v_pk_mul_f32 v[76:77], v[76:77], v[230:231] op_sel_hi:[1,0]\n"
    "v_pk_mul_f32 v[78:79], v[78:79], v[230:231] op_sel_hi:[1,0]\n"
    "v_pk_mul_f32 v[84:85], v[84:85], v[230:231] op_sel_hi:[1,0]\n"
    "v_pk_mul_f32 v[86:87], v[86:87], v[230:231] op_sel_hi:[1,0]\n"
    "v_pk_mul_f32 v[92:93], v[92:93], v[230:231] op_sel_hi:[1,0]\n"
    "v_pk_mul_f32 v[94:95], v[94:95], v[230:231] op_sel_hi:[1,0]\n"
    "s_nop 1\n"
    "s_branch .Lfz_RR1\n"
    ".Lfz_R2:\n"
    "s_nop 7\n"
    "v_mov_b32_e32 v228, v226\n"
    "v_mov_b32_e32 v229, v227\n"
    "s_nop 0\n"
    "v_permlane32_swap_b32_e32 v226, v228\n"
    "v_permlane32_swap_b32_e32 v227, v229\n"
    "v_max_f32_e32 v226, v226, v228\n"
    "v_max_f32_e32 v227, v227, v229\n"
    "v_mov_b32_e32 v228, v226\n"
    "v_mov_b32_e32 v229, v227\n"
    "s_nop 0\n"
    "v_permlane16_swap_b32_e32 v226, v228\n"
    "v_permlane16_swap_b32_e32 v227, v229\n"
    "v_max_f32_e32 v226, v226, v228\n"
    "v_max_f32_e32 v227, v227, v229\n"
    "v_max_f32_e32 v226, 0, v226\n"
    "v_max_f32_e64 v227, 0, v227\n"
    "v_exp_f32_e64 v228, -v226\n"
    "v_exp_f32_e64 v230, -v227\n"
    "v_sub_f32_e32 v160, v160, v226\n"
    "v_sub_f32_e32 v161, v161, v226\n"
    "v_sub_f32_e32 v162, v162, v226\n"
    "v_sub_f32_e32 v163, v163, v226\n"
    "v_sub_f32_e32 v168, v168, v226\n"
    "v_sub_f32_e32 v169, v169, v226\n"
    "v_sub_f32_e32 v170, v170, v226\n"
    "v_sub_f32_e32 v171, v171, v226\n"
    "v_sub_f32_e32 v216, v216, v226\n"
    "v_sub_f32_e32 v217, v217, v226\n"
    "v_sub_f32_e32 v218, v218, v226\n"
    "v_sub_f32_e32 v219, v219, v226\n"
    "v_mul_f32_e64 v224, v224, v228\n"
    "v_pk_mul_f32 v[0:1], v[0:1], v[228:229] op_sel_hi:[1,0]\n"
    "v_pk_mul_f32 v[2:3], v[2:3], v[228:229] op_sel_hi:[1,0]\n"
    "v_pk_mul_f32 v[8:9], v[8:9], v[228:229] op_sel_hi:[1,0]\n"
    "v_pk_mul_f32 v[10:11], v[10:11], v[228:229] op_sel_hi:[1,0]\n"
    "v_pk_mul_f32 v[16:17], v[16:17], v[228:229] op_sel_hi:[1,0]\n"
    "v_pk_mul_f32 v[18:19], v[18:19], v[228:229] op_sel_hi:[1,0]\n"
    "v_pk_mul_f32 v[24:25], v[24:25], v[228:229] op_sel_hi:[1,0]\n"
    "v_pk_mul_f32 v[26:27], v[26:27], v[228:229] op_sel_hi:[1,0]\n"
    "v_pk_mul_f32 v[32:33], v[32:33], v[228:229] op_sel_hi:[1,0]\n"
    "v_pk_mul_f32 v[34:35], v[34:35], v[228:229] op_sel_hi:[1,0]\n"
    "v_pk_mul_f32 v[40:41], v[40:41], v[228:229] op_sel_hi:[1,0]\n"
    "v_pk_mul_f32 v[42:43], v[42:43], v[228:229] op_sel_hi:[1,0]\n"
    "v_pk_mul_f32 v[48:49], v[48:49], v[228:229] op_sel_hi:[1,0]\n"
    "v_pk_mul_f32 v[50:51], v[50:51], v[228:229] op_sel_hi:[1,0]\n"
    "v_pk_mul_f32 v[56:57], v[56:57], v[228:229] op_sel_hi:[1,0]\n"
    "v_pk_mul_f32 v[58:59], v[58:59], v[228:229] op_sel_hi:[1,0]\n"
    "v_pk_mul_f32 v[64:65], v[64:65], v[228:229] op_sel_hi:[1,0]\n"
    "v_pk_mul_f32 v[66:67], v[66:67], v[228:229] op_sel_hi:[1,0]\n"
    "v_pk_mul_f32 v[72:73], v[72:73], v[228:229] op_sel_hi:[1,0]\n"
    "v_pk_mul_f32 v[74:75], v[74:75], v[228:229] op_sel_hi:[1,0]\n"
    "v_pk_mul_f32 v[80:81], v[80:81], v[228:229] op_sel_hi:[1,0]\n"
    "v_pk_mul_f32 v[82:83], v[82:83], v[228:229] op_sel_hi:[1,0]\n"
    "v_pk_mul_f32 v[88:89], v[88:89], v[228:229] op_sel_hi:[1,0]\n"
    "v_pk_mul_f32 v[90:91], v[90:91], v[228:229] op_sel_hi:[1,0]\n"
    "v_sub_f32_e32 v164, v164, v227\n"
    "v_sub_f32_e32 v165, v165, v227\n"
    "v_sub_f32_e32 v166, v166, v227\n"
    "v_sub_f32_e32 v167, v167, v227\n"
    "v_sub_f32_e32 v172, v172, v227\n"
    "v_sub_f32_e32 v173, v173, v227\n"
    "v_sub_f32_e32 v174, v174, v227\n"
    "v_sub_f32_e32 v175, v175, v227\n"
    "v_sub_f32_e32 v220, v220, v227\n"
    "v_sub_f32_e32 v221, v221, v227\n"
    "v_sub_f32_e32 v222, v222, v227\n"
    "v_sub_f32_e32 v223, v223, v227\n"
    "v_mul_f32_e64 v225, v225, v230\n"
    "v_pk_mul_f32 v[4:5], v[4:5], v[230:231] op_sel_hi:[1,0]\n"
    "v_pk_mul_f32 v[6:7], v[6:7], v[230:231] op_sel_hi:[1,0]\n"
    "v_pk_mul_f32 v[12:13], v[12:13], v[230:231] op_sel_hi:[1,0]\n"
    "v_pk_mul_f32 v[14:15], v[14:15], v[230:231] op_sel_hi:[1,0]\n"
    "v_pk_mul_f32 v[20:21], v[20:21], v[230:231] op_sel_hi:[1,0]\n"
    "v_pk_mul_f32 v[22:23], v[22:23], v[230:231] op_sel_hi:[1,0]\n"
    "v_pk_mul_f32 v[28:29], v[28:29], v[230:231] op_sel_hi:[1,0]\n"
    "v_pk_mul_f32 v[30:31], v[30:31], v[230:231] op_sel_hi:[1,0]\n"
    "v_pk_mul_f32 v[36:37], v[36:37], v[230:231] op_sel_hi:[1,0]\n"
    "v_pk_mul_f32 v[38:39], v[38:39], v[230:231] op_sel_hi:[1,0]\n"
    "v_pk_mul_f32 v[44:45], v[44:45], v[230:231] op_sel_hi:[1,0]\n"
    "v_pk_mul_f32 v[46:47], v[46:47], v[230:231] op_sel_hi:[1,0]\n"
    "v_pk_mul_f32 v[52:53], v[52:53], v[230:231] op_sel_hi:[1,0]\n"
    "v_pk_mul_f32 v[54:55], v[54:55], v[230:231] op_sel_hi:[1,0]\n"
    "v_pk_mul_f32 v[60:61], v[60:61], v[230:231] op_sel_hi:[1,0]\n"
    "v_pk_mul_f32 v[62:63], v[62:63], v[230:231] op_sel_hi:[1,0]\n"
    "v_pk_mul_f32 v[68:69], v[68:69], v[230:231] op_sel_hi:[1,0]\n"
    "v_pk_mul_f32 v[70:71], v[70:71], v[230:231] op_sel_hi:[1,0]\n"
    "v_pk_mul_f32 v[76:77], v[76:77], v[230:231] op_sel_hi:[1,0]\n"
    "v_pk_mul_f32 v[78:79], v[78:79], v[230:231] op_sel_hi:[1,0]\n"
    "v_pk_mul_f32 v[84:85], v[84:85], v[230:231] op_sel_hi:[1,0]\n"
    "v_pk_mul_f32 v[86:87], v[86:87], v[230:231] op_sel_hi:[1,0]\n"
    "v_pk_mul_f32 v[92:93], v[92:93], v[230:231] op_sel_hi:[1,0]\n"
    "v_pk_mul_f32 v[94:95], v[94:95], v[230:231] op_sel_hi:[1,0]\n"
    "s_nop 1\n"
    "s_branch .Lfz_RR2\n"
    ".Lfz_R3:\n"
    "s_nop 7\n"
    "v_mov_b32_e32 v228, v226\n"
    "v_mov_b32_e32 v229, v227\n"
    "s_nop 0\n"
    "v_permlane32_swap_b32_e32 v226, v228\n"
    "v_permlane32_swap_b32_e32 v227, v229\n"
    "v_max_f32_e32 v226, v226, v228\n"
    "v_max_f32_e32 v227, v227, v229\n"
    "v_mov_b32_e32 v228, v226\n"
    "v_mov_b32_e32 v229, v227\n"
    "s_nop 0\n"
    "v_permlane16_swap_b32_e32 v226, v228\n"
    "v_permlane16_swap_b32_e32 v227, v229\n"
    "v_max_f32_e32 v226, v226, v228\n"
    "v_max_f32_e32 v227, v227, v229\n"
    "v_max_f32_e32 v226, 0, v226\n"
    "v_max_f32_e64 v227, 0, v227\n"
    "v_exp_f32_e64 v228, -v226\n"
    "v_exp_f32_e64 v230, -v227\n"
    "v_sub_f32_e32 v144, v144, v226\n"
    "v_sub_f32_e32 v145, v145, v226\n"
    "v_sub_f32_e32 v146, v146, v226\n"
    "v_sub_f32_e32 v147, v147, v226\n"
    "v_sub_f32_e32 v152, v152, v226\n"
    "v_sub_f32_e32 v153, v153, v226\n"
    "v_sub_f32_e32 v154, v154, v226\n"
    "v_sub_f32_e32 v155, v155, v226\n"
    "v_sub_f32_e32 v216, v216, v226\n"
    "v_sub_f32_e32 v217, v217, v226\n"
    "v_sub_f32_e32 v218, v218, v226\n"
    "v_sub_f32_e32 v219, v219, v226\n"
    "v_mul_f32_e64 v224, v224, v228\n"
    "v_pk_mul_f32 v[0:1], v[0:1], v[228:229] op_sel_hi:[1,0]\n"
    "v_pk_mul_f32 v[2:3], v[2:3], v[228:229] op_sel_hi:[1,0]\n"
    "v_pk_mul_f32 v[8:9], v[8:9], v[228:229] op_sel_hi:[1,0]\n"
    "v_pk_mul_f32 v[10:11], v[10:11], v[228:229] op_sel_hi:[1,0]\n"
    "v_pk_mul_f32 v[16:17], v[16:17], v[228:229] op_sel_hi:[1,0]\n"
    "v_pk_mul_f32 v[18:19], v[18:19], v[228:229] op_sel_hi:[1,0]\n"
    "v_pk_mul_f32 v[24:25], v[24:25], v[228:229] op_sel_hi:[1,0]\n"
    "v_pk_mul_f32 v[26:27], v[26:27], v[228:229] op_sel_hi:[1,0]\n"
    "v_pk_mul_f32 v[32:33], v[32:33], v[228:229] op_sel_hi:[1,0]\n"
    "v_pk_mul_f32 v[34:35], v[34:35], v[228:229] op_sel_hi:[1,0]\n"
    "v_pk_mul_f32 v[40:41], v[40:41], v[228:229] op_sel_hi:[1,0]\n"
    "v_pk_mul_f32 v[42:43], v[42:43], v[228:229] op_sel_hi:[1,0]\n"
    "v_pk_mul_f32 v[48:49], v[48:49], v[228:229] op_sel_hi:[1,0]\n"
    "v_pk_mul_f32 v[50:51], v[50:51], v[228:229] op_sel_hi:[1,0]\n"
    "v_pk_mul_f32 v[56:57], v[56:57], v[228:229] op_sel_hi:[1,0]\n"
    "v_pk_mul_f32 v[58:59], v[58:59], v[228:229] op_sel_hi:[1,0]\n"
    "v_pk_mul_f32 v[64:65], v[64:65], v[228:229] op_sel_hi:[1,0]\n"
    "v_pk_mul_f32 v[66:67], v[66:67], v[228:229] op_sel_hi:[1,0]\n"
    "v_pk_mul_f32 v[72:73], v[72:73], v[228:229] op_sel_hi:[1,0]\n"
    "v_pk_mul_f32 v[74:75], v[74:75], v[228:229] op_sel_hi:[1,0]\n"
    "v_pk_mul_f32 v[80:81], v[80:81], v[228:229] op_sel_hi:[1,0]\n"
    "v_pk_mul_f32 v[82:83], v[82:83], v[228:229] op_sel_hi:[1,0]\n"
    "v_pk_mul_f32 v[88:89], v[88:89], v[228:229] op_sel_hi:[1,0]\n"
    "v_pk_mul_f32 v[90:91], v[90:91], v[228:229] op_sel_hi:[1,0]\n"
    "v_sub_f32_e32 v148, v148, v227\n"
    "v_sub_f32_e32 v149, v149, v227\n"
    "v_sub_f32_e32 v150, v150, v227\n"
    "v_sub_f32_e32 v151, v151, v227\n"
    "v_sub_f32_e32 v156, v156, v227\n"
    "v_sub_f32_e32 v157, v157, v227\n"
    "v_sub_f32_e32 v158, v158, v227\n"
    "v_sub_f32_e32 v159, v159, v227\n"
    "v_sub_f32_e32 v220, v220, v227\n"
    "v_sub_f32_e32 v221, v221, v227\n"
    "v_sub_f32_e32 v222, v222, v227\n"
    "v_sub_f32_e32 v223, v223, v227\n"
    "v_mul_f32_e64 v225, v225, v230\n"
    "v_pk_mul_f32 v[4:5], v[4:5], v[230:231] op_sel_hi:[1,0]\n"
    "v_pk_mul_f32 v[6:7], v[6:7], v[230:231] op_sel_hi:[1,0]\n"
    "v_pk_mul_f32 v[12:13], v[12:13], v[230:231] op_sel_hi:[1,0]\n"
    "v_pk_mul_f32 v[14:15], v[14:15], v[230:231] op_sel_hi:[1,0]\n"
    "v_pk_mul_f32 v[20:21], v[20:21], v[230:231] op_sel_hi:[1,0]\n"
    "v_pk_mul_f32 v[22:23], v[22:23], v[230:231] op_sel_hi:[1,0]\n"
    "v_pk_mul_f32 v[28:29], v[28:29], v[230:231] op_sel_hi:[1,0]\n"
    "v_pk_mul_f32 v[30:31], v[30:31], v[230:231] op_sel_hi:[1,0]\n"
    "v_pk_mul_f32 v[36:37], v[36:37], v[230:231] op_sel_hi:[1,0]\n"
    "v_pk_mul_f32 v[38:39], v[38:39], v[230:231] op_sel_hi:[1,0]\n"
    "v_pk_mul_f32 v[44:45], v[44:45], v[230:231] op_sel_hi:[1,0]\n"
    "v_pk_mul_f32 v[46:47], v[46:47], v[230:231] op_sel_hi:[1,0]\n"
    "v_pk_mul_f32 v[52:53], v[52:53], v[230:231] op_sel_hi:[1,0]\n"
    "v_pk_mul_f32 v[54:55], v[54:55], v[230:231] op_sel_hi:[1,0]\n"
    "v_pk_mul_f32 v[60:61], v[60:61], v[230:231] op_sel_hi:[1,0]\n"
    "v_pk_mul_f32 v[62:63], v[62:63], v[230:231] op_sel_hi:[1,0]\n"
    "v_pk_mul_f32 v[68:69], v[68:69], v[230:231] op_sel_hi:[1,0]\n"
    "v_pk_mul_f32 v[70:71], v[70:71], v[230:231] op_sel_hi:[1,0]\n"
    "v_pk_mul_f32 v[76:77], v[76:77], v[230:231] op_sel_hi:[1,0]\n"
    "v_pk_mul_f32 v[78:79], v[78:79], v[230:231] op_sel_hi:[1,0]\n"
    "v_pk_mul_f32 v[84:85], v[84:85], v[230:231] op_sel_hi:[1,0]\n"
    "v_pk_mul_f32 v[86:87], v[86:87], v[230:231] op_sel_hi:[1,0]\n"
    "v_pk_mul_f32 v[92:93], v[92:93], v[230:231] op_sel_hi:[1,0]\n"
    "v_pk_mul_f32 v[94:95], v[94:95], v[230:231] op_sel_hi:[1,0]\n"
    "s_nop 1\n"
    "s_branch .Lfz_RR3\n"
    ".Lfz_EXIT:\n"
    : "+{s[8:11]}"(srdW), "+{s[12:15]}"(srdX), "+{s[16:19]}"(srdK), "+{s[20:23]}"(srdV), "+{s[24:27]}"(srdO), "+{s[28:31]}"(srdP),
      "+{s32}"(grp), "+{s33}"(qg), "+{s34}"(mk), "+{s35}"(mx), "+{s59}"(ldsb), "+{s60}"(gw), "+{s61}"(gx), "+{s62}"(go), "+{s63}"(rot), "+{s73}"(pace), "+{s86}"(hpu), "+{s[88:91]}"(srdU),
      "+{v[184:187]}"(tq), "+{v[242:243]}"(to),
      "+{v[176:179]}"(wb), "+{v[180:183]}"(xa), "+{v[238:241]}"(in3), "+{v[244:247]}"(in0), "+{v[248:251]}"(in1), "+{v[252:255]}"(in2)
    :
    : "memory", "vcc", "scc",
      "s36", "s37", "s38", "s39", "s40", "s41", "s42", "s43", "s44", "s45", "s46", "s47", "s48", "s49", "s50", "s51", "s52", "s53", "s54", "s55", "s56", "s57", "s58",
      "s64", "s65", "s66", "s67", "s68", "s69", "s70", "s71", "s72", "s74", "s75", "s76", "s77", "s78", "s79", "s80", "s81", "s82", "s83", "s84", "s85", "s87", "s92", "s93", "s94", "s95", "s96", "s97", "s98", "s99",
      C4(), C4(1), C4(2), C4(3), C4(4), C4(5), C4(6), C4(7), C4(8), C4(9), C4(10), C4(11), C4(12), C4(13), C4(14), C4(15), C4(16),
      "v170", "v171", "v172", "v173", "v174", "v175", "v188", "v189", C4(19), C4(20), C4(21), C4(22),
      "v230", "v231", "v232", "v233", "v234", "v235", "v236", "v237");
}

extern "C" void kernel_launch(void* const* d_in, const int* in_sizes, int n_in, void* d_out, int out_size, void* d_ws, size_t ws_size,
                              hipStream_t stream) {
  char* part = (char*)d_ws + POFF;
  fused_kernel<<<256, 512, 0, stream>>>((const u16*)d_in[0], (const u16*)d_in[1], (const u16*)d_in[2], (const u16*)d_in[3], (const u16*)d_in[4],
                                        (const u16*)d_in[5], (const u16*)d_in[6], (const u16*)d_in[7], (const u16*)d_in[8], (const u16*)d_in[9], part, (u16*)d_out);
}
```
